```python
import torch

def _softmax(x, axis=-1):
    return torch.softmax(x, dim=axis)
def _gelu(x):
    return torch.nn.functional.gelu(x, approximate="tanh")
def _silu(x):
    return torch.nn.functional.silu(x)
def _sigmoid(x):
    return torch.sigmoid(x)
def _layernorm(x, axis=-1, eps=1e-5):
    m = x.mean(dim=axis, keepdim=True)
    v = x.var(dim=axis, keepdim=True, unbiased=False)
    return (x - m) / torch.sqrt(v + eps)
def _scatter_add(tgt, idx, src, axis=0):
    return tgt.clone().index_add_(axis, idx, src)
def _segment_sum(src, seg, n):
    out = torch.zeros((n,) + src.shape[1:], dtype=src.dtype)
    return out.index_add_(0, seg, src)
def _pad(x, pw):
    flat = tuple(v for pair in reversed(pw) for v in pair)
    return torch.nn.functional.pad(x, flat)

kernel_name = 'recurrent_scan__gqa_block_cached__mixer_channel_mlp__prompt'

def setup_inputs():

    _w = (lambda a: (lambda t: t.to(torch.bfloat16) if t.is_floating_point() else t)(torch.as_tensor(a)))
    def _A_setup():
        B, Sq, D = 1, 2048, 448
        Hq, Hkv, Sk, Dh = 16, 8, 2048, 256
        assert Hq % Hkv == 0, (Hq, Hkv)
        return {
            "x": _w((lambda *s: torch.randn(*s, dtype=torch.bfloat16) * 0.05)(B, Sq, D)),
            "k_cache": _w((lambda *s: torch.randn(*s, dtype=torch.bfloat16) * 1.6)(B, Hkv, Sk, Dh)),
            "v_cache": _w((lambda *s: torch.randn(*s, dtype=torch.bfloat16) * 0.8)(B, Hkv, Sk, Dh)),
            "wq": _w((lambda *s: torch.randn(*s, dtype=torch.bfloat16) * 1.6)(D, Hq * Dh) * (D ** -0.5)),
            "wo": _w((lambda *s: torch.randn(*s, dtype=torch.bfloat16) * 0.8)(Hq * Dh, D) * ((Hq * Dh) ** -0.5)),
        }
    _A_in = _A_setup()

    def _B_setup():
        B, S, D, F = 1, 2048, 448, 1792
        return {
            "x": _w((lambda *s: torch.randn(*s, dtype=torch.bfloat16) * 0.05)(B, S, D)),
            "w1": _w((lambda *s: torch.randn(*s, dtype=torch.bfloat16) * 1.6)(D, F) * (D ** -0.5)),
            "b1": _w((lambda *s: torch.randn(*s, dtype=torch.bfloat16) * 0.8)(F)),
            "w2": _w((lambda *s: torch.randn(*s, dtype=torch.bfloat16) * 0.2)(F, D) * (F ** -0.5)),
            "b2": _w((lambda *s: torch.randn(*s, dtype=torch.bfloat16) * 0.2)(D)),
        }
    _B_in = _B_setup()

    return {
        "A_x": _A_in["x"],
        "A_k_cache": _A_in["k_cache"],
        "A_v_cache": _A_in["v_cache"],
        "A_wq": _A_in["wq"],
        "A_wo": _A_in["wo"],
        "B_x": _B_in["x"],
        "B_w1": _B_in["w1"],
        "B_b1": _B_in["b1"],
        "B_w2": _B_in["w2"],
        "B_b2": _B_in["b2"],
    }

def reference(A_x, A_k_cache, A_v_cache, A_wq, A_wo, B_x, B_w1, B_b1, B_w2, B_b2):
    def _A_ref(x, k_cache, v_cache, wq, wo):
        B, Sq, D = x.shape
        Hq, Dh = 16, 256
        Hkv = k_cache.shape[1]
        rep = Hq // Hkv
        q = (lambda x, *d: x.permute(*d))((x @ wq).reshape(B, Sq, Hq, Dh), 0, 2, 1, 3)
        k = (lambda x, n, axis: torch.repeat_interleave(x, n, dim=axis))(k_cache, rep, axis=1)
        v = (lambda x, n, axis: torch.repeat_interleave(x, n, dim=axis))(v_cache, rep, axis=1)
        scores = torch.einsum("bhqd,bhkd->bhqk", q, k) * (Dh ** -0.5)
        p = _softmax(scores, axis=-1)
        o = torch.einsum("bhqk,bhkd->bhqd", p, v)
        return (lambda x, *d: x.permute(*d))(o, 0, 2, 1, 3).reshape(B, Sq, Hq * Dh) @ wo

    def _B_ref(x, w1, b1, w2, b2):
        y = _layernorm(x, axis=-1)
        h = _gelu(torch.einsum("bsd,df->bsf", y, w1) + b1)
        return torch.einsum("bsf,fd->bsd", h, w2) + b2

    x = A_x
    xn = _layernorm(x, axis=-1)
    s = _A_ref(xn, A_k_cache, A_v_cache, A_wq, A_wo)
    g = _silu(_B_ref(xn, B_w1, B_b1, B_w2, B_b2))
    return x + s * g
```

```cpp
#include <hip/hip_runtime.h>
#include <hip/hip_bf16.h>
extern "C" __global__ __launch_bounds__(256,1) void k_main(const void* x, const void* K, const void* Vv, const void* wq, const void* wo, const void* w1, const void* b1, const void* w2, const void* b2, void* out, void* ws) {
  __shared__ __attribute__((aligned(16))) char lds[163840];
  unsigned wave = __builtin_amdgcn_readfirstlane(threadIdx.x >> 6);
  asm volatile("; lds %0" :: "s"((unsigned)(size_t)lds));
  asm volatile(
    "s_mov_b64 s[8:9], %0\n"
    "s_mov_b32 s10, %1\n"
    "s_mov_b32 s11, %2\n"
    "s_load_dwordx16 s[48:63], s[8:9], 0x0\n"
    "s_load_dwordx4 s[64:67], s[8:9], 0x40\n"
    "s_load_dwordx2 s[68:69], s[8:9], 0x50\n"
    "s_and_b32 s44, s10, 1\n"
    "s_lshl_b32 s44, s44, 2\n"
    "s_lshr_b32 s45, s10, 3\n"
    "s_and_b32 s46, s45, 3\n"
    "s_add_u32 s13, s44, s46\n"
    "s_and_b32 s44, s10, 7\n"
    "s_lshr_b32 s44, s44, 1\n"
    "s_lshl_b32 s44, s44, 3\n"
    "s_lshr_b32 s45, s10, 5\n"
    "s_add_u32 s12, s44, s45\n"
    "s_lshr_b32 s14, s11, 1\n"
    "s_and_b32 s15, s11, 1\n"
    "s_mul_i32 s78, s11, 0x2100\n"
    "v_mbcnt_lo_u32_b32 v126, -1, 0\n"
    "v_mbcnt_hi_u32_b32 v126, -1, v126\n"
    "s_getpc_b64 s[30:31]\n"
    "L_pcs:\n"
    "s_mov_b32 s44, s12\n"
    "s_lshl_b32 s44, s44, 2\n"
    "s_add_u32 s44, s44, s11\n"
    "s_mul_i32 s44, s44, 0x200\n"
    "s_mov_b32 s45, L_end-L_pcs-4\n"
    "v_and_b32 v248, 3, v126\n"
    "v_lshlrev_b32 v248, 7, v248\n"
    "v_add_u32 v248, s44, v248\n"
    "v_min_u32 v248, s45, v248\n"
    "global_load_dword a231, v248, s[30:31]\n"
    "v_lshrrev_b32 v16, 3, v126\n"
    "v_mul_u32_u24 v16, 0x380, v16\n"
    "v_and_b32 v17, 7, v126\n"
    "v_lshl_add_u32 v16, v17, 4, v16\n"
    "v_add_u32 v17, 0x1c00, v16\n"
    "s_mul_i32 s44, s12, 0xe000\n"
    "s_mul_i32 s45, s11, 0x3800\n"
    "s_add_u32 s44, s44, s45\n"
    "s_waitcnt lgkmcnt(0)\n"
    "s_add_u32 s30, s48, s44\n"
    "s_addc_u32 s31, s49, 0\n"
    "global_load_dwordx4 v[128:131], v16, s[30:31]\n"
    "global_load_dwordx4 v[132:135], v16, s[30:31] offset:128\n"
    "global_load_dwordx4 v[136:139], v16, s[30:31] offset:256\n"
    "global_load_dwordx4 v[140:143], v16, s[30:31] offset:384\n"
    "global_load_dwordx4 v[144:147], v16, s[30:31] offset:512\n"
    "global_load_dwordx4 v[148:151], v16, s[30:31] offset:640\n"
    "global_load_dwordx4 v[152:155], v16, s[30:31] offset:768\n"
    "global_load_dwordx4 v[156:159], v17, s[30:31]\n"
    "global_load_dwordx4 v[160:163], v17, s[30:31] offset:128\n"
    "global_load_dwordx4 v[164:167], v17, s[30:31] offset:256\n"
    "global_load_dwordx4 v[168:171], v17, s[30:31] offset:384\n"
    "global_load_dwordx4 v[172:175], v17, s[30:31] offset:512\n"
    "global_load_dwordx4 v[176:179], v17, s[30:31] offset:640\n"
    "global_load_dwordx4 v[180:183], v17, s[30:31] offset:768\n"
    "s_add_u32 s100, s68, 0x3000000\n"
    "s_addc_u32 s101, s69, 0\n"
    "s_load_dword s100, s[100:101], 0x0\n"
    "s_lshl_b32 s44, s12, 2\n"
    "s_add_u32 s44, s44, s11\n"
    "s_mul_i32 s45, s44, 0x800\n"
    "s_lshl_b32 s46, s13, 20\n"
    "s_add_u32 s45, s45, s46\n"
    "s_add_u32 s34, s50, s45\n"
    "s_addc_u32 s35, s51, 0\n"
    "s_add_u32 s40, s52, s45\n"
    "s_addc_u32 s41, s53, 0\n"
    "s_min_u32 s45, s44, 0x61\n"
    "s_lshl_b32 s45, s45, 11\n"
    "s_mul_i32 s46, s13, 0x31000\n"
    "s_add_u32 s45, s45, s46\n"
    "s_add_u32 s28, s62, s45\n"
    "s_addc_u32 s29, s63, 0\n"
    "s_min_u32 s44, s44, 0x6f\n"
    "s_lshl_b32 s45, s44, 12\n"
    "s_mul_i32 s46, s13, 0x70000\n"
    "s_add_u32 s45, s45, s46\n"
    "s_add_u32 s42, s56, s45\n"
    "s_addc_u32 s43, s57, 0\n"
    "v_and_b32 v250, 15, v126\n"
    "v_lshlrev_b32 v250, 7, v250\n"
    "v_and_b32 v251, 31, v126\n"
    "v_lshlrev_b32 v251, 7, v251\n"
    "v_lshlrev_b32 v252, 7, v126\n"
    "v_bfe_u32 v253, v126, 3, 2\n"
    "v_lshlrev_b32 v253, 13, v253\n"
    "v_and_b32 v254, 7, v126\n"
    "v_lshl_add_u32 v253, v254, 7, v253\n"
    "v_bfe_u32 v254, v126, 2, 2\n"
    "v_mul_u32_u24 v254, 0xe00, v254\n"
    "v_and_b32 v249, 3, v126\n"
    "v_lshl_add_u32 v254, v249, 7, v254\n"
    "s_lshl_b32 s44, s12, 2\n"
    "s_add_u32 s44, s44, s11\n"
    "s_min_u32 s44, s44, 0x6f\n"
    "s_mul_i32 s45, s44, 0x3800\n"
    "s_mul_i32 s46, s13, 0x1c0\n"
    "s_add_u32 s45, s45, s46\n"
    "s_add_u32 s30, s58, s45\n"
    "s_addc_u32 s31, s59, 0\n"
    "global_load_dword a233, v254, s[30:31]\n"
    "v_lshrrev_b32 v18, 3, v126\n"
    "v_and_b32 v19, 1, v18\n"
    "v_lshlrev_b32 v19, 3, v19\n"
    "v_and_b32 v20, 2, v18\n"
    "v_lshl_add_u32 v19, v20, 1, v19\n"
    "v_and_b32 v20, 4, v18\n"
    "v_lshl_add_u32 v19, v20, 2, v19\n"
    "v_mul_u32_u24 v19, 0xe00, v19\n"
    "v_bfe_u32 v20, v126, 1, 2\n"
    "v_add_u32 v20, s15, v20\n"
    "v_and_b32 v20, 3, v20\n"
    "v_lshl_add_u32 v19, v20, 5, v19\n"
    "v_and_b32 v20, 1, v126\n"
    "v_lshl_add_u32 v0, v20, 4, v19\n"
    "v_add_u32 v1, 0x9e0, v0\n"
    "v_add_u32 v2, 0x13c0, v0\n"
    "v_add_u32 v3, 0x1da0, v0\n"
    "s_mul_i32 s44, s13, 0x1c0\n"
    "s_mul_i32 s45, s11, 0x70\n"
    "s_add_u32 s44, s44, s45\n"
    "s_lshl_b32 s45, s15, 4\n"
    "s_sub_u32 s44, s44, s45\n"
    "s_add_u32 s70, s58, s44\n"
    "s_addc_u32 s71, s59, 0\n"
    "s_add_u32 m0, s78, 0x8400\n"
    "s_nop 0\n"
    "global_load_lds_dwordx4 v0, s[70:71]\n"
    "global_load_lds_dwordx4 v1, s[70:71] offset:1056\n"
    "global_load_lds_dwordx4 v2, s[70:71] offset:2112\n"
    "global_load_lds_dwordx4 v3, s[70:71] offset:3168\n"
    "s_add_u32 s70, s70, 0x1c000\n"
    "s_addc_u32 s71, s71, 0\n"
    "v_lshrrev_b32 v23, 4, v126\n"
    "v_and_b32 v24, 15, v126\n"
    "v_lshrrev_b32 v25, 2, v24\n"
    "v_and_b32 v26, 3, v126\n"
    "v_and_b32 v18, 1, v23\n"
    "v_lshlrev_b32 v18, 2, v18\n"
    "v_and_b32 v19, 2, v23\n"
    "v_lshl_add_u32 v18, v19, 3, v18\n"
    "v_lshlrev_b32 v18, 13, v18\n"
    "v_lshl_add_u32 v60, v24, 4, v18\n"
    "v_add_u32 v61, 0x1be0, v60\n"
    "v_add_u32 v62, 0x37c0, v60\n"
    "v_add_u32 v63, 0x53a0, v60\n"
    "s_lshl_b32 s44, s13, 10\n"
    "s_lshl_b32 s45, s11, 8\n"
    "s_add_u32 s44, s44, s45\n"
    "s_add_u32 s72, s54, s44\n"
    "s_addc_u32 s73, s55, 0\n"
    "s_add_u32 s74, s72, 0x10000\n"
    "s_addc_u32 s75, s73, 0\n"
    "v_mul_u32_u24 v4, 0x420, v25\n"
    "v_and_b32 v18, 1, v23\n"
    "v_lshl_add_u32 v4, v18, 7, v4\n"
    "v_lshrrev_b32 v19, 1, v23\n"
    "v_lshl_add_u32 v4, v19, 9, v4\n"
    "v_lshl_add_u32 v4, v26, 3, v4\n"
    "v_add_u32 v4, s78, v4\n"
    "v_lshl_add_u32 v20, v18, 2, v25\n"
    "v_mul_u32_u24 v5, 0x420, v20\n"
    "v_lshl_add_u32 v5, v19, 9, v5\n"
    "v_lshl_add_u32 v5, v26, 3, v5\n"
    "v_add_u32 v5, s78, v5\n"
    "v_add_u32 v6, 0x10800, v5\n"
    "v_xor_b32 v20, v24, v23\n"
    "v_lshlrev_b32 v20, 4, v20\n"
    "v_lshl_add_u32 v20, v23, 8, v20\n"
    "s_mul_i32 s44, s15, 0x7700\n"
    "s_add_u32 s45, s44, 0x10800\n"
    "s_sub_u32 s46, 0x17f00, s44\n"
    "v_add_u32 v7, s45, v20\n"
    "v_add_u32 v8, s46, v20\n"
    "v_lshlrev_b32 v21, 4, v126\n"
    "s_mul_i32 s44, s14, 7\n"
    "s_mul_i32 s45, s15, 3\n"
    "s_add_u32 s44, s44, s45\n"
    "s_mul_i32 s45, s13, 0x1c0\n"
    "s_add_u32 s30, s60, s45\n"
    "s_addc_u32 s31, s61, 0\n"
    "s_add_u32 s45, s15, 0\n"
    "s_and_b32 s45, s45, 3\n"
    "s_add_u32 s45, s45, s44\n"
    "s_lshl_b32 s46, s45, 5\n"
    "v_lshlrev_b32 v22, 3, v23\n"
    "v_add_u32 v22, s46, v22\n"
    "global_load_dwordx2 v[34:35], v22, s[30:31]\n"
    "s_lshr_b32 s46, s45, 1\n"
    "s_lshl_b32 s46, s46, 12\n"
    "s_and_b32 s47, s45, 1\n"
    "s_lshl_b32 s47, s47, 3\n"
    "s_add_u32 s46, s46, s47\n"
    "s_lshl_b32 s47, s15, 11\n"
    "s_add_u32 s46, s46, s47\n"
    "s_add_u32 s46, s46, 0x21000\n"
    "v_add_u32 v120, s46, v21\n"
    "s_add_u32 s45, s15, 1\n"
    "s_and_b32 s45, s45, 3\n"
    "s_add_u32 s45, s45, s44\n"
    "s_lshl_b32 s46, s45, 5\n"
    "v_lshlrev_b32 v22, 3, v23\n"
    "v_add_u32 v22, s46, v22\n"
    "global_load_dwordx2 v[38:39], v22, s[30:31]\n"
    "s_lshr_b32 s46, s45, 1\n"
    "s_lshl_b32 s46, s46, 12\n"
    "s_and_b32 s47, s45, 1\n"
    "s_lshl_b32 s47, s47, 3\n"
    "s_add_u32 s46, s46, s47\n"
    "s_lshl_b32 s47, s15, 11\n"
    "s_add_u32 s46, s46, s47\n"
    "s_add_u32 s46, s46, 0x21000\n"
    "v_add_u32 v121, s46, v21\n"
    "s_add_u32 s45, s15, 2\n"
    "s_and_b32 s45, s45, 3\n"
    "s_add_u32 s45, s45, s44\n"
    "s_lshl_b32 s46, s45, 5\n"
    "v_lshlrev_b32 v22, 3, v23\n"
    "v_add_u32 v22, s46, v22\n"
    "global_load_dwordx2 v[42:43], v22, s[30:31]\n"
    "s_lshr_b32 s46, s45, 1\n"
    "s_lshl_b32 s46, s46, 12\n"
    "s_and_b32 s47, s45, 1\n"
    "s_lshl_b32 s47, s47, 3\n"
    "s_add_u32 s46, s46, s47\n"
    "s_lshl_b32 s47, s15, 11\n"
    "s_add_u32 s46, s46, s47\n"
    "s_add_u32 s46, s46, 0x21000\n"
    "v_add_u32 v122, s46, v21\n"
    "s_add_u32 s45, s15, 3\n"
    "s_and_b32 s45, s45, 3\n"
    "s_add_u32 s45, s45, s44\n"
    "s_lshl_b32 s46, s45, 5\n"
    "v_lshlrev_b32 v22, 3, v23\n"
    "v_add_u32 v22, s46, v22\n"
    "global_load_dwordx2 v[46:47], v22, s[30:31]\n"
    "s_lshr_b32 s46, s45, 1\n"
    "s_lshl_b32 s46, s46, 12\n"
    "s_and_b32 s47, s45, 1\n"
    "s_lshl_b32 s47, s47, 3\n"
    "s_add_u32 s46, s46, s47\n"
    "s_lshl_b32 s47, s15, 11\n"
    "s_add_u32 s46, s46, s47\n"
    "s_add_u32 s46, s46, 0x21000\n"
    "v_add_u32 v123, s46, v21\n"
    "s_lshl_b32 s44, s15, 8\n"
    "s_add_u32 s44, s44, 0x1f600\n"
    "v_lshlrev_b32 v127, 4, v24\n"
    "v_add_u32 v127, s44, v127\n"
    "v_lshrrev_b32 v18, 1, v23\n"
    "v_lshl_add_u32 v18, v18, 4, v24\n"
    "v_lshlrev_b32 v18, 4, v18\n"
    "v_and_b32 v19, 1, v23\n"
    "v_lshl_add_u32 v18, v19, 3, v18\n"
    "v_add_u32 v18, s78, v18\n"
    "v_add_u32 v124, 0x18c00, v18\n"
    "v_add_u32 v125, 0x10800, v18\n"
    "s_mov_b32 s79, 0x380\n"
    "s_mul_i32 s44, s13, 0x31000\n"
    "s_mul_i32 s45, s11, 0xe0\n"
    "s_add_u32 s44, s44, s45\n"
    "s_add_u32 s76, s62, s44\n"
    "s_addc_u32 s77, s63, 0\n"
    "s_sub_u32 s76, s76, 0xc00\n"
    "s_subb_u32 s77, s77, 0\n"
    "s_lshl_b32 s44, s13, 20\n"
    "s_lshl_b32 s45, s11, 13\n"
    "s_add_u32 s44, s44, s45\n"
    "s_add_u32 s16, s50, s44\n"
    "s_addc_u32 s17, s51, 0\n"
    "s_sub_u32 s16, s16, 0x60\n"
    "s_subb_u32 s17, s17, 0\n"
    "s_lshl_b32 s44, s13, 20\n"
    "s_lshl_b32 s45, s11, 13\n"
    "s_add_u32 s44, s44, s45\n"
    "s_add_u32 s18, s52, s44\n"
    "s_addc_u32 s19, s53, 0\n"
    "s_sub_u32 s18, s18, 0x60\n"
    "s_subb_u32 s19, s19, 0\n"
    "s_add_u32 s20, s78, 0\n"
    "s_add_u32 s21, s78, 0x1080\n"
    "s_add_u32 s22, s78, 0x8400\n"
    "s_add_u32 s23, s78, 0x9480\n"
    "s_add_u32 s24, s78, 0x10800\n"
    "s_add_u32 s25, s78, 0x11880\n"
    "s_add_u32 s26, s78, 0x18c00\n"
    "s_add_u32 s27, s78, 0x19c80\n"
    "s_mov_b32 s38, 0x41f00000\n"
    "s_mov_b32 s39, 0xc1f00000\n"
    "s_mov_b32 s37, 0\n"
    "s_lshl_b32 s44, s13, 5\n"
    "s_add_u32 s44, s44, s12\n"
    "s_lshl_b32 s44, s44, 2\n"
    "s_add_u32 s44, s44, s11\n"
    "s_mul_i32 s44, s44, 0x3800\n"
    "s_add_u32 s32, s68, s44\n"
    "s_addc_u32 s33, s69, 0\n"
    "s_mov_b32 s47, 0x3f803f80\n"
    "v_lshrrev_b32 v202, 3, v126\n"
    "v_and_b32 v203, 3, v126\n"
    "v_xor_b32 v204, v202, v203\n"
    "v_lshlrev_b32 v204, 4, v204\n"
    "v_lshl_add_u32 v204, v203, 8, v204\n"
    "v_bfe_u32 v203, v126, 2, 1\n"
    "v_mul_u32_u24 v203, 0x440, v203\n"
    "v_add_u32 v204, v204, v203\n"
    "s_mul_i32 s44, s11, 0x3b80\n"
    "s_add_u32 s44, s44, 0x10800\n"
    "v_add_u32 v204, s44, v204\n"
    "v_lshlrev_b32 v202, 4, v202\n"
    "s_lshl_b32 s44, s11, 2\n"
    "s_add_u32 s45, s44, 0x1f600\n"
    "v_add_u32 v205, s45, v202\n"
    "s_xor_b32 s44, s44, 8\n"
    "s_add_u32 s45, s44, 0x1f700\n"
    "v_add_u32 v206, s45, v202\n"
    "s_waitcnt vmcnt(22)\n"
    "v_dot2_f32_bf16 v184, v128, s47, 0\n"
    "v_dot2_f32_bf16 v186, v128, v128, 0\n"
    "v_dot2_f32_bf16 v184, v129, s47, v184\n"
    "v_dot2_f32_bf16 v186, v129, v129, v186\n"
    "v_dot2_f32_bf16 v184, v130, s47, v184\n"
    "v_dot2_f32_bf16 v186, v130, v130, v186\n"
    "v_dot2_f32_bf16 v184, v131, s47, v184\n"
    "s_add_u32 m0, s78, 0x9480\n"
    "v_dot2_f32_bf16 v186, v131, v131, v186\n"
    "global_load_lds_dwordx4 v0, s[70:71]\n"
    "s_waitcnt vmcnt(22)\n"
    "v_dot2_f32_bf16 v184, v132, s47, v184\n"
    "v_dot2_f32_bf16 v186, v132, v132, v186\n"
    "v_dot2_f32_bf16 v184, v133, s47, v184\n"
    "v_dot2_f32_bf16 v186, v133, v133, v186\n"
    "v_dot2_f32_bf16 v184, v134, s47, v184\n"
    "v_dot2_f32_bf16 v186, v134, v134, v186\n"
    "v_dot2_f32_bf16 v184, v135, s47, v184\n"
    "v_dot2_f32_bf16 v186, v135, v135, v186\n"
    "s_waitcnt vmcnt(21)\n"
    "v_dot2_f32_bf16 v184, v136, s47, v184\n"
    "v_dot2_f32_bf16 v186, v136, v136, v186\n"
    "v_dot2_f32_bf16 v184, v137, s47, v184\n"
    "v_dot2_f32_bf16 v186, v137, v137, v186\n"
    "v_dot2_f32_bf16 v184, v138, s47, v184\n"
    "v_dot2_f32_bf16 v186, v138, v138, v186\n"
    "v_dot2_f32_bf16 v184, v139, s47, v184\n"
    "v_dot2_f32_bf16 v186, v139, v139, v186\n"
    "global_load_lds_dwordx4 v1, s[70:71] offset:1056\n"
    "s_waitcnt vmcnt(21)\n"
    "v_dot2_f32_bf16 v184, v140, s47, v184\n"
    "v_dot2_f32_bf16 v186, v140, v140, v186\n"
    "v_dot2_f32_bf16 v184, v141, s47, v184\n"
    "v_dot2_f32_bf16 v186, v141, v141, v186\n"
    "v_dot2_f32_bf16 v184, v142, s47, v184\n"
    "v_dot2_f32_bf16 v186, v142, v142, v186\n"
    "v_dot2_f32_bf16 v184, v143, s47, v184\n"
    "v_dot2_f32_bf16 v186, v143, v143, v186\n"
    "s_waitcnt vmcnt(20)\n"
    "v_dot2_f32_bf16 v184, v144, s47, v184\n"
    "v_dot2_f32_bf16 v186, v144, v144, v186\n"
    "v_dot2_f32_bf16 v184, v145, s47, v184\n"
    "v_dot2_f32_bf16 v186, v145, v145, v186\n"
    "v_dot2_f32_bf16 v184, v146, s47, v184\n"
    "v_dot2_f32_bf16 v186, v146, v146, v186\n"
    "v_dot2_f32_bf16 v184, v147, s47, v184\n"
    "v_dot2_f32_bf16 v186, v147, v147, v186\n"
    "global_load_lds_dwordx4 v2, s[70:71] offset:2112\n"
    "s_waitcnt vmcnt(20)\n"
    "v_dot2_f32_bf16 v184, v148, s47, v184\n"
    "v_dot2_f32_bf16 v186, v148, v148, v186\n"
    "v_dot2_f32_bf16 v184, v149, s47, v184\n"
    "v_dot2_f32_bf16 v186, v149, v149, v186\n"
    "v_dot2_f32_bf16 v184, v150, s47, v184\n"
    "v_dot2_f32_bf16 v186, v150, v150, v186\n"
    "v_dot2_f32_bf16 v184, v151, s47, v184\n"
    "v_dot2_f32_bf16 v186, v151, v151, v186\n"
    "s_waitcnt vmcnt(19)\n"
    "v_dot2_f32_bf16 v184, v152, s47, v184\n"
    "v_dot2_f32_bf16 v186, v152, v152, v186\n"
    "v_dot2_f32_bf16 v184, v153, s47, v184\n"
    "v_dot2_f32_bf16 v186, v153, v153, v186\n"
    "v_dot2_f32_bf16 v184, v154, s47, v184\n"
    "v_dot2_f32_bf16 v186, v154, v154, v186\n"
    "v_dot2_f32_bf16 v184, v155, s47, v184\n"
    "v_dot2_f32_bf16 v186, v155, v155, v186\n"
    "global_load_lds_dwordx4 v3, s[70:71] offset:3168\n"
    "s_waitcnt vmcnt(19)\n"
    "v_dot2_f32_bf16 v185, v156, s47, 0\n"
    "s_add_u32 s70, s70, 0x1c000\n"
    "s_addc_u32 s71, s71, 0\n"
    "v_dot2_f32_bf16 v187, v156, v156, 0\n"
    "v_dot2_f32_bf16 v185, v157, s47, v185\n"
    "v_dot2_f32_bf16 v187, v157, v157, v187\n"
    "s_lshl_b32 s44, s12, 2\n"
    "s_add_u32 s44, s44, s11\n"
    "s_min_u32 s44, s44, 0x6f\n"
    "s_lshl_b32 s45, s44, 15\n"
    "s_lshl_b32 s46, s13, 10\n"
    "s_add_u32 s45, s45, s46\n"
    "s_add_u32 s30, s54, s45\n"
    "s_addc_u32 s31, s55, 0\n"
    "global_load_dword a234, v253, s[30:31]\n"
    "v_dot2_f32_bf16 v185, v158, s47, v185\n"
    "v_dot2_f32_bf16 v187, v158, v158, v187\n"
    "v_dot2_f32_bf16 v185, v159, s47, v185\n"
    "v_dot2_f32_bf16 v187, v159, v159, v187\n"
    "s_waitcnt vmcnt(19)\n"
    "v_dot2_f32_bf16 v185, v160, s47, v185\n"
    "v_dot2_f32_bf16 v187, v160, v160, v187\n"
    "v_dot2_f32_bf16 v185, v161, s47, v185\n"
    "v_dot2_f32_bf16 v187, v161, v161, v187\n"
    "v_dot2_f32_bf16 v185, v162, s47, v185\n"
    "v_dot2_f32_bf16 v187, v162, v162, v187\n"
    "v_dot2_f32_bf16 v185, v163, s47, v185\n"
    "s_add_u32 m0, s78, 0\n"
    "v_dot2_f32_bf16 v187, v163, v163, v187\n"
    "global_load_lds_dwordx4 v0, s[70:71]\n"
    "s_waitcnt vmcnt(19)\n"
    "v_dot2_f32_bf16 v185, v164, s47, v185\n"
    "v_dot2_f32_bf16 v187, v164, v164, v187\n"
    "v_dot2_f32_bf16 v185, v165, s47, v185\n"
    "v_dot2_f32_bf16 v187, v165, v165, v187\n"
    "v_dot2_f32_bf16 v185, v166, s47, v185\n"
    "v_dot2_f32_bf16 v187, v166, v166, v187\n"
    "v_dot2_f32_bf16 v185, v167, s47, v185\n"
    "v_dot2_f32_bf16 v187, v167, v167, v187\n"
    "s_waitcnt vmcnt(18)\n"
    "v_dot2_f32_bf16 v185, v168, s47, v185\n"
    "v_dot2_f32_bf16 v187, v168, v168, v187\n"
    "v_dot2_f32_bf16 v185, v169, s47, v185\n"
    "v_dot2_f32_bf16 v187, v169, v169, v187\n"
    "v_dot2_f32_bf16 v185, v170, s47, v185\n"
    "v_dot2_f32_bf16 v187, v170, v170, v187\n"
    "v_dot2_f32_bf16 v185, v171, s47, v185\n"
    "v_dot2_f32_bf16 v187, v171, v171, v187\n"
    "global_load_lds_dwordx4 v1, s[70:71] offset:1056\n"
    "s_waitcnt vmcnt(18)\n"
    "v_dot2_f32_bf16 v185, v172, s47, v185\n"
    "v_dot2_f32_bf16 v187, v172, v172, v187\n"
    "v_dot2_f32_bf16 v185, v173, s47, v185\n"
    "v_dot2_f32_bf16 v187, v173, v173, v187\n"
    "v_dot2_f32_bf16 v185, v174, s47, v185\n"
    "v_dot2_f32_bf16 v187, v174, v174, v187\n"
    "v_dot2_f32_bf16 v185, v175, s47, v185\n"
    "v_dot2_f32_bf16 v187, v175, v175, v187\n"
    "s_waitcnt vmcnt(17)\n"
    "v_dot2_f32_bf16 v185, v176, s47, v185\n"
    "v_dot2_f32_bf16 v187, v176, v176, v187\n"
    "v_dot2_f32_bf16 v185, v177, s47, v185\n"
    "v_dot2_f32_bf16 v187, v177, v177, v187\n"
    "v_dot2_f32_bf16 v185, v178, s47, v185\n"
    "v_dot2_f32_bf16 v187, v178, v178, v187\n"
    "v_dot2_f32_bf16 v185, v179, s47, v185\n"
    "v_dot2_f32_bf16 v187, v179, v179, v187\n"
    "global_load_lds_dwordx4 v2, s[70:71] offset:2112\n"
    "s_waitcnt vmcnt(17)\n"
    "v_dot2_f32_bf16 v185, v180, s47, v185\n"
    "v_dot2_f32_bf16 v187, v180, v180, v187\n"
    "v_dot2_f32_bf16 v185, v181, s47, v185\n"
    "v_dot2_f32_bf16 v187, v181, v181, v187\n"
    "v_dot2_f32_bf16 v185, v182, s47, v185\n"
    "v_dot2_f32_bf16 v187, v182, v182, v187\n"
    "v_dot2_f32_bf16 v185, v183, s47, v185\n"
    "v_dot2_f32_bf16 v187, v183, v183, v187\n"
    "v_add_f32_dpp v184, v184, v184 quad_perm:[1,0,3,2] row_mask:0xf bank_mask:0xf\n"
    "v_add_f32_dpp v186, v186, v186 quad_perm:[1,0,3,2] row_mask:0xf bank_mask:0xf\n"
    "v_add_f32_dpp v185, v185, v185 quad_perm:[1,0,3,2] row_mask:0xf bank_mask:0xf\n"
    "v_add_f32_dpp v187, v187, v187 quad_perm:[1,0,3,2] row_mask:0xf bank_mask:0xf\n"
    "v_add_f32_dpp v184, v184, v184 quad_perm:[2,3,0,1] row_mask:0xf bank_mask:0xf\n"
    "v_add_f32_dpp v186, v186, v186 quad_perm:[2,3,0,1] row_mask:0xf bank_mask:0xf\n"
    "v_add_f32_dpp v185, v185, v185 quad_perm:[2,3,0,1] row_mask:0xf bank_mask:0xf\n"
    "v_add_f32_dpp v187, v187, v187 quad_perm:[2,3,0,1] row_mask:0xf bank_mask:0xf\n"
    "global_load_lds_dwordx4 v3, s[70:71] offset:3168\n"
    "v_add_f32_dpp v184, v184, v184 row_half_mirror row_mask:0xf bank_mask:0xf\n"
    "s_add_u32 s70, s70, 0x1c000\n"
    "s_addc_u32 s71, s71, 0\n"
    "v_add_f32_dpp v186, v186, v186 row_half_mirror row_mask:0xf bank_mask:0xf\n"
    "v_add_f32_dpp v185, v185, v185 row_half_mirror row_mask:0xf bank_mask:0xf\n"
    "v_add_f32_dpp v187, v187, v187 row_half_mirror row_mask:0xf bank_mask:0xf\n"
    "v_mul_f32 v188, 0x3b124925, v184\n"
    "v_mul_f32 v202, v188, v188\n"
    "v_mul_f32 v203, 0x3b124925, v186\n"
    "v_sub_f32 v202, v203, v202\n"
    "v_max_f32 v202, 0, v202\n"
    "v_add_f32 v203, 0x3727c5ac, v202\n"
    "v_rsq_f32 v192, v203\n"
    "s_nop 0\n"
    "v_mul_f32 v203, v192, v192\n"
    "v_mul_f32 v203, v203, v202\n"
    "v_add_f32 v203, 0x3727c5ac, v203\n"
    "v_rsq_f32 v200, v203\n"
    "s_add_u32 m0, s78, 0x1080\n"
    "v_mul_f32 v200, 0x41317218, v200\n"
    "global_load_lds_dwordx4 v0, s[70:71]\n"
    "v_mul_f32 v192, 0x3db8aa3b, v192\n"
    "v_mul_f32 v196, v188, v192\n"
    "v_sub_f32 v196, 0, v196\n"
    "ds_write_b32 v205, v200\n"
    "ds_write_b32 v206, v200\n"
    "v_mul_f32 v190, 0x3b124925, v185\n"
    "v_mul_f32 v202, v190, v190\n"
    "v_mul_f32 v203, 0x3b124925, v187\n"
    "v_sub_f32 v202, v203, v202\n"
    "v_max_f32 v202, 0, v202\n"
    "v_add_f32 v203, 0x3727c5ac, v202\n"
    "v_rsq_f32 v194, v203\n"
    "s_nop 0\n"
    "v_mul_f32 v203, v194, v194\n"
    "v_mul_f32 v203, v203, v202\n"
    "v_add_f32 v203, 0x3727c5ac, v203\n"
    "v_rsq_f32 v201, v203\n"
    "global_load_lds_dwordx4 v1, s[70:71] offset:1056\n"
    "v_mul_f32 v201, 0x41317218, v201\n"
    "v_mul_f32 v194, 0x3db8aa3b, v194\n"
    "v_mul_f32 v198, v190, v194\n"
    "v_sub_f32 v198, 0, v198\n"
    "ds_write_b32 v205, v201 offset:128\n"
    "ds_write_b32 v206, v201 offset:128\n"
    "v_lshlrev_b32 v208, 16, v128\n"
    "v_and_b32 v209, 0xffff0000, v128\n"
    "v_lshlrev_b32 v210, 16, v129\n"
    "v_and_b32 v211, 0xffff0000, v129\n"
    "v_lshlrev_b32 v212, 16, v130\n"
    "v_and_b32 v213, 0xffff0000, v130\n"
    "v_lshlrev_b32 v214, 16, v131\n"
    "v_and_b32 v215, 0xffff0000, v131\n"
    "v_pk_fma_f32 v[208:209], v[208:209], v[192:193], v[196:197] op_sel_hi:[1,0,0]\n"
    "v_pk_fma_f32 v[210:211], v[210:211], v[192:193], v[196:197] op_sel_hi:[1,0,0]\n"
    "global_load_lds_dwordx4 v2, s[70:71] offset:2112\n"
    "v_pk_fma_f32 v[212:213], v[212:213], v[192:193], v[196:197] op_sel_hi:[1,0,0]\n"
    "v_pk_fma_f32 v[214:215], v[214:215], v[192:193], v[196:197] op_sel_hi:[1,0,0]\n"
    "v_cvt_pk_bf16_f32 v128, v208, v209\n"
    "v_cvt_pk_bf16_f32 v129, v210, v211\n"
    "v_cvt_pk_bf16_f32 v130, v212, v213\n"
    "v_cvt_pk_bf16_f32 v131, v214, v215\n"
    "ds_write_b128 v204, v[128:131]\n"
    "v_lshlrev_b32 v216, 16, v132\n"
    "v_and_b32 v217, 0xffff0000, v132\n"
    "v_lshlrev_b32 v218, 16, v133\n"
    "v_and_b32 v219, 0xffff0000, v133\n"
    "v_lshlrev_b32 v220, 16, v134\n"
    "v_and_b32 v221, 0xffff0000, v134\n"
    "v_lshlrev_b32 v222, 16, v135\n"
    "v_and_b32 v223, 0xffff0000, v135\n"
    "v_pk_fma_f32 v[216:217], v[216:217], v[192:193], v[196:197] op_sel_hi:[1,0,0]\n"
    "global_load_lds_dwordx4 v3, s[70:71] offset:3168\n"
    "v_pk_fma_f32 v[218:219], v[218:219], v[192:193], v[196:197] op_sel_hi:[1,0,0]\n"
    "s_add_u32 s70, s70, 0x1c000\n"
    "s_addc_u32 s71, s71, 0\n"
    "v_pk_fma_f32 v[220:221], v[220:221], v[192:193], v[196:197] op_sel_hi:[1,0,0]\n"
    "v_pk_fma_f32 v[222:223], v[222:223], v[192:193], v[196:197] op_sel_hi:[1,0,0]\n"
    "v_cvt_pk_bf16_f32 v132, v216, v217\n"
    "v_cvt_pk_bf16_f32 v133, v218, v219\n"
    "v_cvt_pk_bf16_f32 v134, v220, v221\n"
    "v_cvt_pk_bf16_f32 v135, v222, v223\n"
    "ds_write_b128 v204, v[132:135] offset:2176\n"
    "v_lshlrev_b32 v208, 16, v136\n"
    "v_and_b32 v209, 0xffff0000, v136\n"
    "v_lshlrev_b32 v210, 16, v137\n"
    "v_and_b32 v211, 0xffff0000, v137\n"
    "v_lshlrev_b32 v212, 16, v138\n"
    "v_and_b32 v213, 0xffff0000, v138\n"
    "v_lshlrev_b32 v214, 16, v139\n"
    "v_and_b32 v215, 0xffff0000, v139\n"
    "global_load_dword a235, v250, s[28:29]\n"
    "v_pk_fma_f32 v[208:209], v[208:209], v[192:193], v[196:197] op_sel_hi:[1,0,0]\n"
    "v_pk_fma_f32 v[210:211], v[210:211], v[192:193], v[196:197] op_sel_hi:[1,0,0]\n"
    "v_pk_fma_f32 v[212:213], v[212:213], v[192:193], v[196:197] op_sel_hi:[1,0,0]\n"
    "v_pk_fma_f32 v[214:215], v[214:215], v[192:193], v[196:197] op_sel_hi:[1,0,0]\n"
    "v_cvt_pk_bf16_f32 v136, v208, v209\n"
    "v_cvt_pk_bf16_f32 v137, v210, v211\n"
    "v_cvt_pk_bf16_f32 v138, v212, v213\n"
    "v_cvt_pk_bf16_f32 v139, v214, v215\n"
    "ds_write_b128 v204, v[136:139] offset:4352\n"
    "v_lshlrev_b32 v216, 16, v140\n"
    "v_and_b32 v217, 0xffff0000, v140\n"
    "v_lshlrev_b32 v218, 16, v141\n"
    "global_load_dword a236, v250, s[34:35]\n"
    "s_add_u32 s34, s34, 0x40000\n"
    "s_addc_u32 s35, s35, 0\n"
    "v_and_b32 v219, 0xffff0000, v141\n"
    "v_lshlrev_b32 v220, 16, v142\n"
    "v_and_b32 v221, 0xffff0000, v142\n"
    "v_lshlrev_b32 v222, 16, v143\n"
    "v_and_b32 v223, 0xffff0000, v143\n"
    "v_pk_fma_f32 v[216:217], v[216:217], v[192:193], v[196:197] op_sel_hi:[1,0,0]\n"
    "v_pk_fma_f32 v[218:219], v[218:219], v[192:193], v[196:197] op_sel_hi:[1,0,0]\n"
    "v_pk_fma_f32 v[220:221], v[220:221], v[192:193], v[196:197] op_sel_hi:[1,0,0]\n"
    "v_pk_fma_f32 v[222:223], v[222:223], v[192:193], v[196:197] op_sel_hi:[1,0,0]\n"
    "v_cvt_pk_bf16_f32 v140, v216, v217\n"
    "v_cvt_pk_bf16_f32 v141, v218, v219\n"
    "v_cvt_pk_bf16_f32 v142, v220, v221\n"
    "global_load_dword a237, v250, s[40:41]\n"
    "s_add_u32 s40, s40, 0x40000\n"
    "s_addc_u32 s41, s41, 0\n"
    "v_cvt_pk_bf16_f32 v143, v222, v223\n"
    "ds_write_b128 v204, v[140:143] offset:6528\n"
    "v_lshlrev_b32 v208, 16, v144\n"
    "v_and_b32 v209, 0xffff0000, v144\n"
    "v_lshlrev_b32 v210, 16, v145\n"
    "v_and_b32 v211, 0xffff0000, v145\n"
    "v_lshlrev_b32 v212, 16, v146\n"
    "v_and_b32 v213, 0xffff0000, v146\n"
    "v_lshlrev_b32 v214, 16, v147\n"
    "v_and_b32 v215, 0xffff0000, v147\n"
    "v_pk_fma_f32 v[208:209], v[208:209], v[192:193], v[196:197] op_sel_hi:[1,0,0]\n"
    "v_pk_fma_f32 v[210:211], v[210:211], v[192:193], v[196:197] op_sel_hi:[1,0,0]\n"
    "v_pk_fma_f32 v[212:213], v[212:213], v[192:193], v[196:197] op_sel_hi:[1,0,0]\n"
    "v_pk_fma_f32 v[214:215], v[214:215], v[192:193], v[196:197] op_sel_hi:[1,0,0]\n"
    "v_cvt_pk_bf16_f32 v144, v208, v209\n"
    "v_cvt_pk_bf16_f32 v145, v210, v211\n"
    "v_cvt_pk_bf16_f32 v146, v212, v213\n"
    "v_cvt_pk_bf16_f32 v147, v214, v215\n"
    "ds_write_b128 v204, v[144:147] offset:8704\n"
    "v_lshlrev_b32 v216, 16, v148\n"
    "v_and_b32 v217, 0xffff0000, v148\n"
    "v_lshlrev_b32 v218, 16, v149\n"
    "v_and_b32 v219, 0xffff0000, v149\n"
    "v_lshlrev_b32 v220, 16, v150\n"
    "v_and_b32 v221, 0xffff0000, v150\n"
    "v_lshlrev_b32 v222, 16, v151\n"
    "v_and_b32 v223, 0xffff0000, v151\n"
    "v_pk_fma_f32 v[216:217], v[216:217], v[192:193], v[196:197] op_sel_hi:[1,0,0]\n"
    "v_pk_fma_f32 v[218:219], v[218:219], v[192:193], v[196:197] op_sel_hi:[1,0,0]\n"
    "v_pk_fma_f32 v[220:221], v[220:221], v[192:193], v[196:197] op_sel_hi:[1,0,0]\n"
    "v_pk_fma_f32 v[222:223], v[222:223], v[192:193], v[196:197] op_sel_hi:[1,0,0]\n"
    "v_cvt_pk_bf16_f32 v148, v216, v217\n"
    "v_cvt_pk_bf16_f32 v149, v218, v219\n"
    "v_cvt_pk_bf16_f32 v150, v220, v221\n"
    "v_cvt_pk_bf16_f32 v151, v222, v223\n"
    "ds_write_b128 v204, v[148:151] offset:10880\n"
    "v_lshlrev_b32 v208, 16, v152\n"
    "v_and_b32 v209, 0xffff0000, v152\n"
    "v_lshlrev_b32 v210, 16, v153\n"
    "v_and_b32 v211, 0xffff0000, v153\n"
    "v_lshlrev_b32 v212, 16, v154\n"
    "v_and_b32 v213, 0xffff0000, v154\n"
    "v_lshlrev_b32 v214, 16, v155\n"
    "v_and_b32 v215, 0xffff0000, v155\n"
    "v_pk_fma_f32 v[208:209], v[208:209], v[192:193], v[196:197] op_sel_hi:[1,0,0]\n"
    "v_pk_fma_f32 v[210:211], v[210:211], v[192:193], v[196:197] op_sel_hi:[1,0,0]\n"
    "v_pk_fma_f32 v[212:213], v[212:213], v[192:193], v[196:197] op_sel_hi:[1,0,0]\n"
    "v_pk_fma_f32 v[214:215], v[214:215], v[192:193], v[196:197] op_sel_hi:[1,0,0]\n"
    "v_cvt_pk_bf16_f32 v152, v208, v209\n"
    "v_cvt_pk_bf16_f32 v153, v210, v211\n"
    "v_cvt_pk_bf16_f32 v154, v212, v213\n"
    "v_cvt_pk_bf16_f32 v155, v214, v215\n"
    "ds_write_b128 v204, v[152:155] offset:13056\n"
    "v_lshlrev_b32 v208, 16, v156\n"
    "v_and_b32 v209, 0xffff0000, v156\n"
    "v_lshlrev_b32 v210, 16, v157\n"
    "v_and_b32 v211, 0xffff0000, v157\n"
    "v_lshlrev_b32 v212, 16, v158\n"
    "v_and_b32 v213, 0xffff0000, v158\n"
    "v_lshlrev_b32 v214, 16, v159\n"
    "v_and_b32 v215, 0xffff0000, v159\n"
    "v_pk_fma_f32 v[208:209], v[208:209], v[194:195], v[198:199] op_sel_hi:[1,0,0]\n"
    "v_pk_fma_f32 v[210:211], v[210:211], v[194:195], v[198:199] op_sel_hi:[1,0,0]\n"
    "v_pk_fma_f32 v[212:213], v[212:213], v[194:195], v[198:199] op_sel_hi:[1,0,0]\n"
    "v_pk_fma_f32 v[214:215], v[214:215], v[194:195], v[198:199] op_sel_hi:[1,0,0]\n"
    "v_cvt_pk_bf16_f32 v156, v208, v209\n"
    "v_cvt_pk_bf16_f32 v157, v210, v211\n"
    "v_cvt_pk_bf16_f32 v158, v212, v213\n"
    "v_cvt_pk_bf16_f32 v159, v214, v215\n"
    "ds_write_b128 v204, v[156:159] offset:128\n"
    "v_lshlrev_b32 v216, 16, v160\n"
    "v_and_b32 v217, 0xffff0000, v160\n"
    "v_lshlrev_b32 v218, 16, v161\n"
    "v_and_b32 v219, 0xffff0000, v161\n"
    "v_lshlrev_b32 v220, 16, v162\n"
    "v_and_b32 v221, 0xffff0000, v162\n"
    "v_lshlrev_b32 v222, 16, v163\n"
    "v_and_b32 v223, 0xffff0000, v163\n"
    "v_pk_fma_f32 v[216:217], v[216:217], v[194:195], v[198:199] op_sel_hi:[1,0,0]\n"
    "v_pk_fma_f32 v[218:219], v[218:219], v[194:195], v[198:199] op_sel_hi:[1,0,0]\n"
    "v_pk_fma_f32 v[220:221], v[220:221], v[194:195], v[198:199] op_sel_hi:[1,0,0]\n"
    "v_pk_fma_f32 v[222:223], v[222:223], v[194:195], v[198:199] op_sel_hi:[1,0,0]\n"
    "v_cvt_pk_bf16_f32 v160, v216, v217\n"
    "v_cvt_pk_bf16_f32 v161, v218, v219\n"
    "v_cvt_pk_bf16_f32 v162, v220, v221\n"
    "v_cvt_pk_bf16_f32 v163, v222, v223\n"
    "ds_write_b128 v204, v[160:163] offset:2304\n"
    "v_lshlrev_b32 v208, 16, v164\n"
    "v_and_b32 v209, 0xffff0000, v164\n"
    "v_lshlrev_b32 v210, 16, v165\n"
    "v_and_b32 v211, 0xffff0000, v165\n"
    "v_lshlrev_b32 v212, 16, v166\n"
    "v_and_b32 v213, 0xffff0000, v166\n"
    "v_lshlrev_b32 v214, 16, v167\n"
    "v_and_b32 v215, 0xffff0000, v167\n"
    "v_pk_fma_f32 v[208:209], v[208:209], v[194:195], v[198:199] op_sel_hi:[1,0,0]\n"
    "v_pk_fma_f32 v[210:211], v[210:211], v[194:195], v[198:199] op_sel_hi:[1,0,0]\n"
    "v_pk_fma_f32 v[212:213], v[212:213], v[194:195], v[198:199] op_sel_hi:[1,0,0]\n"
    "v_pk_fma_f32 v[214:215], v[214:215], v[194:195], v[198:199] op_sel_hi:[1,0,0]\n"
    "v_cvt_pk_bf16_f32 v164, v208, v209\n"
    "v_cvt_pk_bf16_f32 v165, v210, v211\n"
    "v_cvt_pk_bf16_f32 v166, v212, v213\n"
    "v_cvt_pk_bf16_f32 v167, v214, v215\n"
    "ds_write_b128 v204, v[164:167] offset:4480\n"
    "v_lshlrev_b32 v216, 16, v168\n"
    "v_and_b32 v217, 0xffff0000, v168\n"
    "v_lshlrev_b32 v218, 16, v169\n"
    "v_and_b32 v219, 0xffff0000, v169\n"
    "v_lshlrev_b32 v220, 16, v170\n"
    "v_and_b32 v221, 0xffff0000, v170\n"
    "v_lshlrev_b32 v222, 16, v171\n"
    "v_and_b32 v223, 0xffff0000, v171\n"
    "v_pk_fma_f32 v[216:217], v[216:217], v[194:195], v[198:199] op_sel_hi:[1,0,0]\n"
    "v_pk_fma_f32 v[218:219], v[218:219], v[194:195], v[198:199] op_sel_hi:[1,0,0]\n"
    "v_pk_fma_f32 v[220:221], v[220:221], v[194:195], v[198:199] op_sel_hi:[1,0,0]\n"
    "v_pk_fma_f32 v[222:223], v[222:223], v[194:195], v[198:199] op_sel_hi:[1,0,0]\n"
    "v_cvt_pk_bf16_f32 v168, v216, v217\n"
    "v_cvt_pk_bf16_f32 v169, v218, v219\n"
    "v_cvt_pk_bf16_f32 v170, v220, v221\n"
    "v_cvt_pk_bf16_f32 v171, v222, v223\n"
    "ds_write_b128 v204, v[168:171] offset:6656\n"
    "v_lshlrev_b32 v208, 16, v172\n"
    "v_and_b32 v209, 0xffff0000, v172\n"
    "v_lshlrev_b32 v210, 16, v173\n"
    "v_and_b32 v211, 0xffff0000, v173\n"
    "v_lshlrev_b32 v212, 16, v174\n"
    "v_and_b32 v213, 0xffff0000, v174\n"
    "v_lshlrev_b32 v214, 16, v175\n"
    "v_and_b32 v215, 0xffff0000, v175\n"
    "v_pk_fma_f32 v[208:209], v[208:209], v[194:195], v[198:199] op_sel_hi:[1,0,0]\n"
    "v_pk_fma_f32 v[210:211], v[210:211], v[194:195], v[198:199] op_sel_hi:[1,0,0]\n"
    "v_pk_fma_f32 v[212:213], v[212:213], v[194:195], v[198:199] op_sel_hi:[1,0,0]\n"
    "v_pk_fma_f32 v[214:215], v[214:215], v[194:195], v[198:199] op_sel_hi:[1,0,0]\n"
    "v_cvt_pk_bf16_f32 v172, v208, v209\n"
    "v_cvt_pk_bf16_f32 v173, v210, v211\n"
    "v_cvt_pk_bf16_f32 v174, v212, v213\n"
    "v_cvt_pk_bf16_f32 v175, v214, v215\n"
    "ds_write_b128 v204, v[172:175] offset:8832\n"
    "v_lshlrev_b32 v216, 16, v176\n"
    "v_and_b32 v217, 0xffff0000, v176\n"
    "v_lshlrev_b32 v218, 16, v177\n"
    "v_and_b32 v219, 0xffff0000, v177\n"
    "v_lshlrev_b32 v220, 16, v178\n"
    "v_and_b32 v221, 0xffff0000, v178\n"
    "v_lshlrev_b32 v222, 16, v179\n"
    "v_and_b32 v223, 0xffff0000, v179\n"
    "v_pk_fma_f32 v[216:217], v[216:217], v[194:195], v[198:199] op_sel_hi:[1,0,0]\n"
    "v_pk_fma_f32 v[218:219], v[218:219], v[194:195], v[198:199] op_sel_hi:[1,0,0]\n"
    "v_pk_fma_f32 v[220:221], v[220:221], v[194:195], v[198:199] op_sel_hi:[1,0,0]\n"
    "v_pk_fma_f32 v[222:223], v[222:223], v[194:195], v[198:199] op_sel_hi:[1,0,0]\n"
    "v_cvt_pk_bf16_f32 v176, v216, v217\n"
    "v_cvt_pk_bf16_f32 v177, v218, v219\n"
    "v_cvt_pk_bf16_f32 v178, v220, v221\n"
    "v_cvt_pk_bf16_f32 v179, v222, v223\n"
    "ds_write_b128 v204, v[176:179] offset:11008\n"
    "v_lshlrev_b32 v208, 16, v180\n"
    "v_and_b32 v209, 0xffff0000, v180\n"
    "v_lshlrev_b32 v210, 16, v181\n"
    "v_and_b32 v211, 0xffff0000, v181\n"
    "v_lshlrev_b32 v212, 16, v182\n"
    "v_and_b32 v213, 0xffff0000, v182\n"
    "v_lshlrev_b32 v214, 16, v183\n"
    "v_and_b32 v215, 0xffff0000, v183\n"
    "v_pk_fma_f32 v[208:209], v[208:209], v[194:195], v[198:199] op_sel_hi:[1,0,0]\n"
    "v_pk_fma_f32 v[210:211], v[210:211], v[194:195], v[198:199] op_sel_hi:[1,0,0]\n"
    "v_pk_fma_f32 v[212:213], v[212:213], v[194:195], v[198:199] op_sel_hi:[1,0,0]\n"
    "v_pk_fma_f32 v[214:215], v[214:215], v[194:195], v[198:199] op_sel_hi:[1,0,0]\n"
    "v_cvt_pk_bf16_f32 v180, v208, v209\n"
    "v_cvt_pk_bf16_f32 v181, v210, v211\n"
    "v_cvt_pk_bf16_f32 v182, v212, v213\n"
    "v_cvt_pk_bf16_f32 v183, v214, v215\n"
    "ds_write_b128 v204, v[180:183] offset:13184\n"
    "v_mov_b32 v58, s78\n"
    "v_add_u32 v58, 0x20e0, v58\n"
    "v_mov_b32 v52, 0\n"
    "v_mov_b32 v53, 0\n"
    "v_mov_b32 v54, 0\n"
    "v_mov_b32 v55, 0\n"
    "ds_write_b128 v58, v[52:55]\n"
    "s_waitcnt vmcnt(19)\n"
    "v_lshlrev_b32 v32, 16, v34\n"
    "v_and_b32 v33, 0xffff0000, v34\n"
    "v_lshlrev_b32 v34, 16, v35\n"
    "v_and_b32 v35, 0xffff0000, v35\n"
    "s_waitcnt vmcnt(18)\n"
    "v_lshlrev_b32 v36, 16, v38\n"
    "v_and_b32 v37, 0xffff0000, v38\n"
    "v_lshlrev_b32 v38, 16, v39\n"
    "v_and_b32 v39, 0xffff0000, v39\n"
    "s_waitcnt vmcnt(17)\n"
    "v_lshlrev_b32 v40, 16, v42\n"
    "v_and_b32 v41, 0xffff0000, v42\n"
    "v_lshlrev_b32 v42, 16, v43\n"
    "v_and_b32 v43, 0xffff0000, v43\n"
    "s_waitcnt vmcnt(16)\n"
    "v_lshlrev_b32 v44, 16, v46\n"
    "v_and_b32 v45, 0xffff0000, v46\n"
    "v_lshlrev_b32 v46, 16, v47\n"
    "v_and_b32 v47, 0xffff0000, v47\n"
    "s_waitcnt lgkmcnt(0)\n"
    "s_add_u32 s100, s100, 1\n"
    "s_xor_b32 s101, s100, 0x9e3779b9\n"
    "s_waitcnt lgkmcnt(0)\n"
    "s_barrier\n"
    "ds_read_b128 a[0:3], v7\n"
    "ds_read_b128 a[56:59], v7 offset:15232\n"
    "ds_read_b128 a[112:115], v8\n"
    "ds_read_b128 a[168:171], v8 offset:15232\n"
    "ds_read_b128 a[4:7], v7 offset:1088\n"
    "ds_read_b128 a[60:63], v7 offset:16320\n"
    "ds_read_b128 a[116:119], v8 offset:1088\n"
    "ds_read_b128 a[172:175], v8 offset:16320\n"
    "ds_read_b128 v[12:15], v127\n"
    "ds_read_b64_tr_b16 v[16:17], v4 offset:33792\n"
    "ds_read_b64_tr_b16 v[18:19], v4 offset:34048\n"
    "ds_read_b64_tr_b16 v[20:21], v4 offset:33824\n"
    "ds_read_b64_tr_b16 v[22:23], v4 offset:34080\n"
    "ds_read_b64_tr_b16 v[24:25], v4 offset:33856\n"
    "ds_read_b64_tr_b16 v[26:27], v4 offset:34112\n"
    "ds_read_b64_tr_b16 v[28:29], v4 offset:33888\n"
    "ds_read_b64_tr_b16 v[30:31], v4 offset:34144\n"
    "s_waitcnt lgkmcnt(6)\n"
    "v_mfma_f32_16x16x32_bf16 v[64:67], v[16:19], a[0:3], 0\n"
    "ds_read_b128 a[8:11], v7 offset:2176\n"
    "v_mfma_f32_16x16x32_bf16 v[68:71], v[16:19], a[56:59], 0\n"
    "ds_read_b128 a[64:67], v7 offset:17408\n"
    "v_mfma_f32_16x16x32_bf16 v[72:75], v[16:19], a[112:115], 0\n"
    "ds_read_b128 a[120:123], v8 offset:2176\n"
    "v_mfma_f32_16x16x32_bf16 v[76:79], v[16:19], a[168:171], 0\n"
    "s_waitcnt lgkmcnt(7)\n"
    "s_waitcnt vmcnt(12)\n"
    "ds_read_b64_tr_b16 v[16:17], v4 offset:38016\n"
    "ds_read_b64_tr_b16 v[18:19], v4 offset:38272\n"
    "v_mfma_f32_16x16x32_bf16 v[80:83], v[20:23], a[0:3], 0\n"
    "ds_read_b128 a[176:179], v8 offset:17408\n"
    "v_mfma_f32_16x16x32_bf16 v[84:87], v[20:23], a[56:59], 0\n"
    "ds_read_b128 a[12:15], v7 offset:3264\n"
    "v_mfma_f32_16x16x32_bf16 v[88:91], v[20:23], a[112:115], 0\n"
    "ds_read_b128 a[68:71], v7 offset:18496\n"
    "v_mfma_f32_16x16x32_bf16 v[92:95], v[20:23], a[168:171], 0\n"
    "s_waitcnt lgkmcnt(10)\n"
    "ds_read_b64_tr_b16 v[20:21], v4 offset:38048\n"
    "ds_read_b64_tr_b16 v[22:23], v4 offset:38304\n"
    "v_mfma_f32_16x16x32_bf16 v[96:99], v[24:27], a[0:3], 0\n"
    "ds_read_b128 a[124:127], v8 offset:3264\n"
    "v_mfma_f32_16x16x32_bf16 v[100:103], v[24:27], a[56:59], 0\n"
    "ds_read_b128 a[180:183], v8 offset:18496\n"
    "v_mfma_f32_16x16x32_bf16 v[104:107], v[24:27], a[112:115], 0\n"
    "v_mfma_f32_16x16x32_bf16 v[108:111], v[24:27], a[168:171], 0\n"
    "s_waitcnt lgkmcnt(12)\n"
    "ds_read_b64_tr_b16 v[24:25], v4 offset:38080\n"
    "ds_read_b64_tr_b16 v[26:27], v4 offset:38336\n"
    "v_mfma_f32_16x16x32_bf16 v[112:115], v[28:31], a[0:3], 0\n"
    "v_mfma_f32_16x16x32_bf16 v[116:119], v[28:31], a[56:59], 0\n"
    "s_add_u32 m0, s78, 0x8400\n"
    "s_waitcnt lgkmcnt(9)\n"
    "ds_read_b64_tr_b16 v[28:29], v4 offset:38112\n"
    "ds_read_b64_tr_b16 v[30:31], v4 offset:38368\n"
    "v_mfma_f32_16x16x32_bf16 v[64:67], v[16:19], a[4:7], v[64:67]\n"
    "global_load_lds_dwordx4 v0, s[70:71]\n"
    "ds_read_b128 a[16:19], v7 offset:4352\n"
    "v_mfma_f32_16x16x32_bf16 v[68:71], v[16:19], a[60:63], v[68:71]\n"
    "global_load_lds_dwordx4 v1, s[70:71] offset:1056\n"
    "ds_read_b128 a[72:75], v7 offset:19584\n"
    "v_mfma_f32_16x16x32_bf16 v[72:75], v[16:19], a[116:119], v[72:75]\n"
    "global_load_lds_dwordx4 v2, s[70:71] offset:2112\n"
    "ds_read_b128 a[128:131], v8 offset:4352\n"
    "v_mfma_f32_16x16x32_bf16 v[76:79], v[16:19], a[172:175], v[76:79]\n"
    "s_waitcnt lgkmcnt(9)\n"
    "s_waitcnt vmcnt(10)\n"
    "ds_read_b64_tr_b16 v[16:17], v4\n"
    "ds_read_b64_tr_b16 v[18:19], v4 offset:256\n"
    "v_mfma_f32_16x16x32_bf16 v[80:83], v[20:23], a[4:7], v[80:83]\n"
    "global_load_lds_dwordx4 v3, s[70:71] offset:3168\n"
    "ds_read_b128 a[184:187], v8 offset:19584\n"
    "v_mfma_f32_16x16x32_bf16 v[84:87], v[20:23], a[60:63], v[84:87]\n"
    "s_add_u32 s70, s70, 0x1c000\n"
    "s_addc_u32 s71, s71, 0\n"
    "ds_read_b128 a[20:23], v7 offset:5440\n"
    "v_mfma_f32_16x16x32_bf16 v[88:91], v[20:23], a[116:119], v[88:91]\n"
    "ds_read_b128 a[76:79], v7 offset:20672\n"
    "v_mfma_f32_16x16x32_bf16 v[92:95], v[20:23], a[172:175], v[92:95]\n"
    "s_waitcnt lgkmcnt(10)\n"
    "ds_read_b64_tr_b16 v[20:21], v4 offset:32\n"
    "ds_read_b64_tr_b16 v[22:23], v4 offset:288\n"
    "v_mfma_f32_16x16x32_bf16 v[96:99], v[24:27], a[4:7], v[96:99]\n"
    "ds_read_b128 a[132:135], v8 offset:5440\n"
    "v_mfma_f32_16x16x32_bf16 v[100:103], v[24:27], a[60:63], v[100:103]\n"
    "ds_read_b128 a[188:191], v8 offset:20672\n"
    "v_mfma_f32_16x16x32_bf16 v[104:107], v[24:27], a[116:119], v[104:107]\n"
    "v_mfma_f32_16x16x32_bf16 v[108:111], v[24:27], a[172:175], v[108:111]\n"
    "s_waitcnt lgkmcnt(12)\n"
    "ds_read_b64_tr_b16 v[24:25], v4 offset:64\n"
    "ds_read_b64_tr_b16 v[26:27], v4 offset:320\n"
    "v_mfma_f32_16x16x32_bf16 v[112:115], v[28:31], a[4:7], v[112:115]\n"
    "v_mfma_f32_16x16x32_bf16 v[116:119], v[28:31], a[60:63], v[116:119]\n"
    "s_add_u32 m0, s78, 0x9480\n"
    "s_waitcnt lgkmcnt(9)\n"
    "ds_read_b64_tr_b16 v[28:29], v4 offset:96\n"
    "ds_read_b64_tr_b16 v[30:31], v4 offset:352\n"
    "v_mfma_f32_16x16x32_bf16 v[64:67], v[16:19], a[8:11], v[64:67]\n"
    "global_load_lds_dwordx4 v0, s[70:71]\n"
    "ds_read_b128 a[24:27], v7 offset:6528\n"
    "v_mfma_f32_16x16x32_bf16 v[68:71], v[16:19], a[64:67], v[68:71]\n"
    "global_load_lds_dwordx4 v1, s[70:71] offset:1056\n"
    "ds_read_b128 a[80:83], v7 offset:21760\n"
    "v_mfma_f32_16x16x32_bf16 v[72:75], v[16:19], a[120:123], v[72:75]\n"
    "global_load_lds_dwordx4 v2, s[70:71] offset:2112\n"
    "ds_read_b128 a[136:139], v8 offset:6528\n"
    "v_mfma_f32_16x16x32_bf16 v[76:79], v[16:19], a[176:179], v[76:79]\n"
    "s_waitcnt lgkmcnt(9)\n"
    "s_waitcnt vmcnt(10)\n"
    "ds_read_b64_tr_b16 v[16:17], v4 offset:4224\n"
    "ds_read_b64_tr_b16 v[18:19], v4 offset:4480\n"
    "v_mfma_f32_16x16x32_bf16 v[80:83], v[20:23], a[8:11], v[80:83]\n"
    "global_load_lds_dwordx4 v3, s[70:71] offset:3168\n"
    "ds_read_b128 a[192:195], v8 offset:21760\n"
    "v_mfma_f32_16x16x32_bf16 v[84:87], v[20:23], a[64:67], v[84:87]\n"
    "s_add_u32 s70, s70, 0x1c000\n"
    "s_addc_u32 s71, s71, 0\n"
    "ds_read_b128 a[28:31], v7 offset:7616\n"
    "v_mfma_f32_16x16x32_bf16 v[88:91], v[20:23], a[120:123], v[88:91]\n"
    "ds_read_b128 a[84:87], v7 offset:22848\n"
    "v_mfma_f32_16x16x32_bf16 v[92:95], v[20:23], a[176:179], v[92:95]\n"
    "s_waitcnt lgkmcnt(10)\n"
    "ds_read_b64_tr_b16 v[20:21], v4 offset:4256\n"
    "ds_read_b64_tr_b16 v[22:23], v4 offset:4512\n"
    "v_mfma_f32_16x16x32_bf16 v[96:99], v[24:27], a[8:11], v[96:99]\n"
    "ds_read_b128 a[140:143], v8 offset:7616\n"
    "v_mfma_f32_16x16x32_bf16 v[100:103], v[24:27], a[64:67], v[100:103]\n"
    "ds_read_b128 a[196:199], v8 offset:22848\n"
    "v_mfma_f32_16x16x32_bf16 v[104:107], v[24:27], a[120:123], v[104:107]\n"
    "v_mfma_f32_16x16x32_bf16 v[108:111], v[24:27], a[176:179], v[108:111]\n"
    "s_waitcnt lgkmcnt(12)\n"
    "ds_read_b64_tr_b16 v[24:25], v4 offset:4288\n"
    "ds_read_b64_tr_b16 v[26:27], v4 offset:4544\n"
    "v_mfma_f32_16x16x32_bf16 v[112:115], v[28:31], a[8:11], v[112:115]\n"
    "v_mfma_f32_16x16x32_bf16 v[116:119], v[28:31], a[64:67], v[116:119]\n"
    "s_add_u32 m0, s78, 0\n"
    "s_waitcnt lgkmcnt(9)\n"
    "ds_read_b64_tr_b16 v[28:29], v4 offset:4320\n"
    "ds_read_b64_tr_b16 v[30:31], v4 offset:4576\n"
    "v_mfma_f32_16x16x32_bf16 v[64:67], v[16:19], a[12:15], v[64:67]\n"
    "global_load_lds_dwordx4 v0, s[70:71]\n"
    "ds_read_b128 a[32:35], v7 offset:8704\n"
    "v_mfma_f32_16x16x32_bf16 v[68:71], v[16:19], a[68:71], v[68:71]\n"
    "global_load_lds_dwordx4 v1, s[70:71] offset:1056\n"
    "ds_read_b128 a[88:91], v7 offset:23936\n"
    "v_mfma_f32_16x16x32_bf16 v[72:75], v[16:19], a[124:127], v[72:75]\n"
    "global_load_lds_dwordx4 v2, s[70:71] offset:2112\n"
    "ds_read_b128 a[144:147], v8 offset:8704\n"
    "v_mfma_f32_16x16x32_bf16 v[76:79], v[16:19], a[180:183], v[76:79]\n"
    "s_waitcnt lgkmcnt(9)\n"
    "s_waitcnt vmcnt(7)\n"
    "ds_read_b64_tr_b16 v[16:17], v4 offset:33792\n"
    "ds_read_b64_tr_b16 v[18:19], v4 offset:34048\n"
    "v_mfma_f32_16x16x32_bf16 v[80:83], v[20:23], a[12:15], v[80:83]\n"
    "global_load_lds_dwordx4 v3, s[70:71] offset:3168\n"
    "ds_read_b128 a[200:203], v8 offset:23936\n"
    "v_mfma_f32_16x16x32_bf16 v[84:87], v[20:23], a[68:71], v[84:87]\n"
    "s_add_u32 s70, s70, 0x1c000\n"
    "s_addc_u32 s71, s71, 0\n"
    "ds_read_b128 a[36:39], v7 offset:9792\n"
    "v_mfma_f32_16x16x32_bf16 v[88:91], v[20:23], a[124:127], v[88:91]\n"
    "ds_read_b128 a[92:95], v7 offset:25024\n"
    "v_mfma_f32_16x16x32_bf16 v[92:95], v[20:23], a[180:183], v[92:95]\n"
    "s_waitcnt lgkmcnt(10)\n"
    "ds_read_b64_tr_b16 v[20:21], v4 offset:33824\n"
    "ds_read_b64_tr_b16 v[22:23], v4 offset:34080\n"
    "v_mfma_f32_16x16x32_bf16 v[96:99], v[24:27], a[12:15], v[96:99]\n"
    "ds_read_b128 a[148:151], v8 offset:9792\n"
    "v_mfma_f32_16x16x32_bf16 v[100:103], v[24:27], a[68:71], v[100:103]\n"
    "ds_read_b128 a[204:207], v8 offset:25024\n"
    "v_mfma_f32_16x16x32_bf16 v[104:107], v[24:27], a[124:127], v[104:107]\n"
    "v_mfma_f32_16x16x32_bf16 v[108:111], v[24:27], a[180:183], v[108:111]\n"
    "s_waitcnt lgkmcnt(12)\n"
    "ds_read_b64_tr_b16 v[24:25], v4 offset:33856\n"
    "ds_read_b64_tr_b16 v[26:27], v4 offset:34112\n"
    "v_mfma_f32_16x16x32_bf16 v[112:115], v[28:31], a[12:15], v[112:115]\n"
    "v_mfma_f32_16x16x32_bf16 v[116:119], v[28:31], a[68:71], v[116:119]\n"
    "s_add_u32 m0, s78, 0x1080\n"
    "s_waitcnt lgkmcnt(9)\n"
    "ds_read_b64_tr_b16 v[28:29], v4 offset:33888\n"
    "ds_read_b64_tr_b16 v[30:31], v4 offset:34144\n"
    "v_mfma_f32_16x16x32_bf16 v[64:67], v[16:19], a[16:19], v[64:67]\n"
    "global_load_lds_dwordx4 v0, s[70:71]\n"
    "ds_read_b128 a[40:43], v7 offset:10880\n"
    "v_mfma_f32_16x16x32_bf16 v[68:71], v[16:19], a[72:75], v[68:71]\n"
    "global_load_lds_dwordx4 v1, s[70:71] offset:1056\n"
    "ds_read_b128 a[96:99], v7 offset:26112\n"
    "v_mfma_f32_16x16x32_bf16 v[72:75], v[16:19], a[128:131], v[72:75]\n"
    "global_load_lds_dwordx4 v2, s[70:71] offset:2112\n"
    "ds_read_b128 a[152:155], v8 offset:10880\n"
    "v_mfma_f32_16x16x32_bf16 v[76:79], v[16:19], a[184:187], v[76:79]\n"
    "s_waitcnt lgkmcnt(9)\n"
    "s_waitcnt vmcnt(7)\n"
    "ds_read_b64_tr_b16 v[16:17], v4 offset:38016\n"
    "ds_read_b64_tr_b16 v[18:19], v4 offset:38272\n"
    "v_mfma_f32_16x16x32_bf16 v[80:83], v[20:23], a[16:19], v[80:83]\n"
    "global_load_lds_dwordx4 v3, s[70:71] offset:3168\n"
    "ds_read_b128 a[208:211], v8 offset:26112\n"
    "v_mfma_f32_16x16x32_bf16 v[84:87], v[20:23], a[72:75], v[84:87]\n"
    "s_add_u32 s70, s70, 0x1c000\n"
    "s_addc_u32 s71, s71, 0\n"
    "ds_read_b128 a[44:47], v7 offset:11968\n"
    "v_mfma_f32_16x16x32_bf16 v[88:91], v[20:23], a[128:131], v[88:91]\n"
    "ds_read_b128 a[100:103], v7 offset:27200\n"
    "v_mfma_f32_16x16x32_bf16 v[92:95], v[20:23], a[184:187], v[92:95]\n"
    "s_waitcnt lgkmcnt(10)\n"
    "ds_read_b64_tr_b16 v[20:21], v4 offset:38048\n"
    "ds_read_b64_tr_b16 v[22:23], v4 offset:38304\n"
    "v_mfma_f32_16x16x32_bf16 v[96:99], v[24:27], a[16:19], v[96:99]\n"
    "ds_read_b128 a[156:159], v8 offset:11968\n"
    "v_mfma_f32_16x16x32_bf16 v[100:103], v[24:27], a[72:75], v[100:103]\n"
    "ds_read_b128 a[212:215], v8 offset:27200\n"
    "v_mfma_f32_16x16x32_bf16 v[104:107], v[24:27], a[128:131], v[104:107]\n"
    "v_mfma_f32_16x16x32_bf16 v[108:111], v[24:27], a[184:187], v[108:111]\n"
    "s_waitcnt lgkmcnt(12)\n"
    "ds_read_b64_tr_b16 v[24:25], v4 offset:38080\n"
    "ds_read_b64_tr_b16 v[26:27], v4 offset:38336\n"
    "v_mfma_f32_16x16x32_bf16 v[112:115], v[28:31], a[16:19], v[112:115]\n"
    "v_mfma_f32_16x16x32_bf16 v[116:119], v[28:31], a[72:75], v[116:119]\n"
    "s_add_u32 m0, s78, 0x8400\n"
    "s_waitcnt lgkmcnt(9)\n"
    "ds_read_b64_tr_b16 v[28:29], v4 offset:38112\n"
    "ds_read_b64_tr_b16 v[30:31], v4 offset:38368\n"
    "v_mfma_f32_16x16x32_bf16 v[64:67], v[16:19], a[20:23], v[64:67]\n"
    "global_load_lds_dwordx4 v0, s[70:71]\n"
    "ds_read_b128 a[48:51], v7 offset:13056\n"
    "v_mfma_f32_16x16x32_bf16 v[68:71], v[16:19], a[76:79], v[68:71]\n"
    "global_load_lds_dwordx4 v1, s[70:71] offset:1056\n"
    "ds_read_b128 a[104:107], v7 offset:28288\n"
    "v_mfma_f32_16x16x32_bf16 v[72:75], v[16:19], a[132:135], v[72:75]\n"
    "global_load_lds_dwordx4 v2, s[70:71] offset:2112\n"
    "ds_read_b128 a[160:163], v8 offset:13056\n"
    "v_mfma_f32_16x16x32_bf16 v[76:79], v[16:19], a[188:191], v[76:79]\n"
    "s_waitcnt lgkmcnt(9)\n"
    "s_waitcnt vmcnt(7)\n"
    "ds_read_b64_tr_b16 v[16:17], v4\n"
    "ds_read_b64_tr_b16 v[18:19], v4 offset:256\n"
    "v_mfma_f32_16x16x32_bf16 v[80:83], v[20:23], a[20:23], v[80:83]\n"
    "global_load_lds_dwordx4 v3, s[70:71] offset:3168\n"
    "ds_read_b128 a[216:219], v8 offset:28288\n"
    "v_mfma_f32_16x16x32_bf16 v[84:87], v[20:23], a[76:79], v[84:87]\n"
    "s_add_u32 s70, s70, 0x1c000\n"
    "s_addc_u32 s71, s71, 0\n"
    "ds_read_b128 a[52:55], v7 offset:14144\n"
    "v_mfma_f32_16x16x32_bf16 v[88:91], v[20:23], a[132:135], v[88:91]\n"
    "ds_read_b128 a[108:111], v7 offset:29376\n"
    "v_mfma_f32_16x16x32_bf16 v[92:95], v[20:23], a[188:191], v[92:95]\n"
    "s_waitcnt lgkmcnt(10)\n"
    "ds_read_b64_tr_b16 v[20:21], v4 offset:32\n"
    "ds_read_b64_tr_b16 v[22:23], v4 offset:288\n"
    "v_mfma_f32_16x16x32_bf16 v[96:99], v[24:27], a[20:23], v[96:99]\n"
    "ds_read_b128 a[164:167], v8 offset:14144\n"
    "v_mfma_f32_16x16x32_bf16 v[100:103], v[24:27], a[76:79], v[100:103]\n"
    "ds_read_b128 a[220:223], v8 offset:29376\n"
    "v_mfma_f32_16x16x32_bf16 v[104:107], v[24:27], a[132:135], v[104:107]\n"
    "v_mfma_f32_16x16x32_bf16 v[108:111], v[24:27], a[188:191], v[108:111]\n"
    "s_waitcnt lgkmcnt(12)\n"
    "ds_read_b64_tr_b16 v[24:25], v4 offset:64\n"
    "ds_read_b64_tr_b16 v[26:27], v4 offset:320\n"
    "v_mfma_f32_16x16x32_bf16 v[112:115], v[28:31], a[20:23], v[112:115]\n"
    "v_mfma_f32_16x16x32_bf16 v[116:119], v[28:31], a[76:79], v[116:119]\n"
    "s_add_u32 m0, s78, 0x9480\n"
    "s_waitcnt lgkmcnt(9)\n"
    "ds_read_b64_tr_b16 v[28:29], v4 offset:96\n"
    "ds_read_b64_tr_b16 v[30:31], v4 offset:352\n"
    "v_mfma_f32_16x16x32_bf16 v[64:67], v[16:19], a[24:27], v[64:67]\n"
    "global_load_lds_dwordx4 v0, s[70:71]\n"
    "v_mfma_f32_16x16x32_bf16 v[68:71], v[16:19], a[80:83], v[68:71]\n"
    "global_load_lds_dwordx4 v1, s[70:71] offset:1056\n"
    "v_mfma_f32_16x16x32_bf16 v[72:75], v[16:19], a[136:139], v[72:75]\n"
    "global_load_lds_dwordx4 v2, s[70:71] offset:2112\n"
    "v_mfma_f32_16x16x32_bf16 v[76:79], v[16:19], a[192:195], v[76:79]\n"
    "s_waitcnt lgkmcnt(6)\n"
    "s_waitcnt vmcnt(7)\n"
    "ds_read_b64_tr_b16 v[16:17], v4 offset:4224\n"
    "ds_read_b64_tr_b16 v[18:19], v4 offset:4480\n"
    "v_mfma_f32_16x16x32_bf16 v[80:83], v[20:23], a[24:27], v[80:83]\n"
    "global_load_lds_dwordx4 v3, s[70:71] offset:3168\n"
    "v_add_u32 v240, 0, v126\n"
    "v_mfma_f32_16x16x32_bf16 v[84:87], v[20:23], a[80:83], v[84:87]\n"
    "s_add_u32 s70, s70, 0x1c000\n"
    "s_addc_u32 s71, s71, 0\n"
    "v_mul_u32_u24 v241, 0x124a, v240\n"
    "v_mfma_f32_16x16x32_bf16 v[88:91], v[20:23], a[136:139], v[88:91]\n"
    "v_lshrrev_b32 v241, 16, v241\n"
    "v_mul_u32_u24 v242, 14, v241\n"
    "v_mfma_f32_16x16x32_bf16 v[92:95], v[20:23], a[192:195], v[92:95]\n"
    "s_waitcnt lgkmcnt(4)\n"
    "ds_read_b64_tr_b16 v[20:21], v4 offset:4256\n"
    "ds_read_b64_tr_b16 v[22:23], v4 offset:4512\n"
    "v_mfma_f32_16x16x32_bf16 v[96:99], v[24:27], a[24:27], v[96:99]\n"
    "v_sub_u32 v242, v240, v242\n"
    "v_lshlrev_b32 v242, 4, v242\n"
    "v_mfma_f32_16x16x32_bf16 v[100:103], v[24:27], a[80:83], v[100:103]\n"
    "v_mad_u32_u24 v242, v241, s79, v242\n"
    "v_add_u32 v242, 0xc00, v242\n"
    "v_mfma_f32_16x16x32_bf16 v[104:107], v[24:27], a[136:139], v[104:107]\n"
    "v_accvgpr_write_b32 a224, v242\n"
    "v_add_u32 v240, 64, v126\n"
    "v_mfma_f32_16x16x32_bf16 v[108:111], v[24:27], a[192:195], v[108:111]\n"
    "s_waitcnt lgkmcnt(4)\n"
    "ds_read_b64_tr_b16 v[24:25], v4 offset:4288\n"
    "ds_read_b64_tr_b16 v[26:27], v4 offset:4544\n"
    "v_mfma_f32_16x16x32_bf16 v[112:115], v[28:31], a[24:27], v[112:115]\n"
    "v_mul_u32_u24 v241, 0x124a, v240\n"
    "v_lshrrev_b32 v241, 16, v241\n"
    "v_mfma_f32_16x16x32_bf16 v[116:119], v[28:31], a[80:83], v[116:119]\n"
    "s_add_u32 m0, s78, 0\n"
    "s_waitcnt lgkmcnt(4)\n"
    "ds_read_b64_tr_b16 v[28:29], v4 offset:4320\n"
    "ds_read_b64_tr_b16 v[30:31], v4 offset:4576\n"
    "v_mfma_f32_16x16x32_bf16 v[64:67], v[16:19], a[28:31], v[64:67]\n"
    "global_load_lds_dwordx4 v0, s[70:71]\n"
    "v_mul_u32_u24 v242, 14, v241\n"
    "v_mfma_f32_16x16x32_bf16 v[68:71], v[16:19], a[84:87], v[68:71]\n"
    "s_barrier\n"
    "global_load_lds_dwordx4 v1, s[70:71] offset:1056\n"
    "v_mfma_f32_16x16x32_bf16 v[72:75], v[16:19], a[140:143], v[72:75]\n"
    "global_load_lds_dwordx4 v2, s[70:71] offset:2112\n"
    "v_sub_u32 v242, v240, v242\n"
    "v_mfma_f32_16x16x32_bf16 v[76:79], v[16:19], a[196:199], v[76:79]\n"
    "s_waitcnt lgkmcnt(4)\n"
    "s_waitcnt vmcnt(7)\n"
    "ds_read_b64_tr_b16 v[16:17], v4 offset:33792\n"
    "ds_read_b64_tr_b16 v[18:19], v4 offset:34048\n"
    "v_mfma_f32_16x16x32_bf16 v[80:83], v[20:23], a[28:31], v[80:83]\n"
    "global_load_lds_dwordx4 v3, s[70:71] offset:3168\n"
    "v_lshlrev_b32 v242, 4, v242\n"
    "v_mfma_f32_16x16x32_bf16 v[84:87], v[20:23], a[84:87], v[84:87]\n"
    "s_add_u32 s70, s70, 0x1c000\n"
    "s_addc_u32 s71, s71, 0\n"
    "s_add_u32 m0, s78, 0x10800\n"
    "v_mfma_f32_16x16x32_bf16 v[88:91], v[20:23], a[140:143], v[88:91]\n"
    "global_load_lds_dwordx4 v60, s[72:73]\n"
    "v_mad_u32_u24 v242, v241, s79, v242\n"
    "v_mfma_f32_16x16x32_bf16 v[92:95], v[20:23], a[196:199], v[92:95]\n"
    "s_waitcnt lgkmcnt(4)\n"
    "ds_read_b64_tr_b16 v[20:21], v4 offset:33824\n"
    "ds_read_b64_tr_b16 v[22:23], v4 offset:34080\n"
    "v_mfma_f32_16x16x32_bf16 v[96:99], v[24:27], a[28:31], v[96:99]\n"
    "global_load_lds_dwordx4 v61, s[72:73] offset:1056\n"
    "v_add_u32 v242, 0x800, v242\n"
    "v_mfma_f32_16x16x32_bf16 v[100:103], v[24:27], a[84:87], v[100:103]\n"
    "global_load_lds_dwordx4 v62, s[72:73] offset:2112\n"
    "v_accvgpr_write_b32 a225, v242\n"
    "v_mfma_f32_16x16x32_bf16 v[104:107], v[24:27], a[140:143], v[104:107]\n"
    "global_load_lds_dwordx4 v63, s[72:73] offset:3168\n"
    "v_add_u32 v240, 0x80, v126\n"
    "v_mfma_f32_16x16x32_bf16 v[108:111], v[24:27], a[196:199], v[108:111]\n"
    "s_waitcnt lgkmcnt(4)\n"
    "ds_read_b64_tr_b16 v[24:25], v4 offset:33856\n"
    "ds_read_b64_tr_b16 v[26:27], v4 offset:34112\n"
    "v_mfma_f32_16x16x32_bf16 v[112:115], v[28:31], a[28:31], v[112:115]\n"
    "v_mul_u32_u24 v241, 0x124a, v240\n"
    "v_lshrrev_b32 v241, 16, v241\n"
    "v_mfma_f32_16x16x32_bf16 v[116:119], v[28:31], a[84:87], v[116:119]\n"
    "s_add_u32 m0, s78, 0x1080\n"
    "s_waitcnt lgkmcnt(4)\n"
    "ds_read_b64_tr_b16 v[28:29], v4 offset:33888\n"
    "ds_read_b64_tr_b16 v[30:31], v4 offset:34144\n"
    "v_mfma_f32_16x16x32_bf16 v[64:67], v[16:19], a[32:35], v[64:67]\n"
    "global_load_lds_dwordx4 v0, s[70:71]\n"
    "v_mul_u32_u24 v242, 14, v241\n"
    "v_mfma_f32_16x16x32_bf16 v[68:71], v[16:19], a[88:91], v[68:71]\n"
    "global_load_lds_dwordx4 v1, s[70:71] offset:1056\n"
    "v_sub_u32 v242, v240, v242\n"
    "v_mfma_f32_16x16x32_bf16 v[72:75], v[16:19], a[144:147], v[72:75]\n"
    "global_load_lds_dwordx4 v2, s[70:71] offset:2112\n"
    "v_lshlrev_b32 v242, 4, v242\n"
    "v_mfma_f32_16x16x32_bf16 v[76:79], v[16:19], a[200:203], v[76:79]\n"
    "s_waitcnt lgkmcnt(4)\n"
    "s_waitcnt vmcnt(11)\n"
    "ds_read_b64_tr_b16 v[16:17], v4 offset:38016\n"
    "ds_read_b64_tr_b16 v[18:19], v4 offset:38272\n"
    "v_mfma_f32_16x16x32_bf16 v[80:83], v[20:23], a[32:35], v[80:83]\n"
    "global_load_lds_dwordx4 v3, s[70:71] offset:3168\n"
    "v_mad_u32_u24 v242, v241, s79, v242\n"
    "v_mfma_f32_16x16x32_bf16 v[84:87], v[20:23], a[88:91], v[84:87]\n"
    "s_add_u32 s70, s70, 0x1c000\n"
    "s_addc_u32 s71, s71, 0\n"
    "s_add_u32 m0, s78, 0x11880\n"
    "v_mfma_f32_16x16x32_bf16 v[88:91], v[20:23], a[144:147], v[88:91]\n"
    "global_load_lds_dwordx4 v60, s[74:75]\n"
    "v_add_u32 v242, 0x400, v242\n"
    "v_mfma_f32_16x16x32_bf16 v[92:95], v[20:23], a[200:203], v[92:95]\n"
    "s_waitcnt lgkmcnt(4)\n"
    "ds_read_b64_tr_b16 v[20:21], v4 offset:38048\n"
    "ds_read_b64_tr_b16 v[22:23], v4 offset:38304\n"
    "v_mfma_f32_16x16x32_bf16 v[96:99], v[24:27], a[32:35], v[96:99]\n"
    "s_add_u32 s72, s72, 0x40000\n"
    "s_addc_u32 s73, s73, 0\n"
    "global_load_lds_dwordx4 v61, s[74:75] offset:1056\n"
    "v_mfma_f32_16x16x32_bf16 v[100:103], v[24:27], a[88:91], v[100:103]\n"
    "global_load_lds_dwordx4 v62, s[74:75] offset:2112\n"
    "v_accvgpr_write_b32 a226, v242\n"
    "v_mfma_f32_16x16x32_bf16 v[104:107], v[24:27], a[144:147], v[104:107]\n"
    "global_load_lds_dwordx4 v63, s[74:75] offset:3168\n"
    "v_add_u32 v240, 0xc0, v126\n"
    "v_mfma_f32_16x16x32_bf16 v[108:111], v[24:27], a[200:203], v[108:111]\n"
    "s_waitcnt lgkmcnt(4)\n"
    "ds_read_b64_tr_b16 v[24:25], v4 offset:38080\n"
    "ds_read_b64_tr_b16 v[26:27], v4 offset:38336\n"
    "v_mfma_f32_16x16x32_bf16 v[112:115], v[28:31], a[32:35], v[112:115]\n"
    "s_add_u32 s74, s74, 0x40000\n"
    "s_addc_u32 s75, s75, 0\n"
    "v_mul_u32_u24 v241, 0x124a, v240\n"
    "v_mfma_f32_16x16x32_bf16 v[116:119], v[28:31], a[88:91], v[116:119]\n"
    "s_add_u32 m0, s78, 0x8400\n"
    "s_waitcnt lgkmcnt(4)\n"
    "ds_read_b64_tr_b16 v[28:29], v4 offset:38112\n"
    "ds_read_b64_tr_b16 v[30:31], v4 offset:38368\n"
    "v_mfma_f32_16x16x32_bf16 v[64:67], v[16:19], a[36:39], v[64:67]\n"
    "global_load_lds_dwordx4 v0, s[70:71]\n"
    "v_lshrrev_b32 v241, 16, v241\n"
    "v_mfma_f32_16x16x32_bf16 v[68:71], v[16:19], a[92:95], v[68:71]\n"
    "global_load_lds_dwordx4 v1, s[70:71] offset:1056\n"
    "v_mul_u32_u24 v242, 14, v241\n"
    "v_mfma_f32_16x16x32_bf16 v[72:75], v[16:19], a[148:151], v[72:75]\n"
    "global_load_lds_dwordx4 v2, s[70:71] offset:2112\n"
    "v_sub_u32 v242, v240, v242\n"
    "v_mfma_f32_16x16x32_bf16 v[76:79], v[16:19], a[204:207], v[76:79]\n"
    "s_waitcnt lgkmcnt(4)\n"
    "s_waitcnt vmcnt(15)\n"
    "ds_read_b64_tr_b16 v[16:17], v4\n"
    "ds_read_b64_tr_b16 v[18:19], v4 offset:256\n"
    "v_mfma_f32_16x16x32_bf16 v[80:83], v[20:23], a[36:39], v[80:83]\n"
    "global_load_lds_dwordx4 v3, s[70:71] offset:3168\n"
    "v_lshlrev_b32 v242, 4, v242\n"
    "v_mfma_f32_16x16x32_bf16 v[84:87], v[20:23], a[92:95], v[84:87]\n"
    "s_add_u32 s70, s70, 0x1c000\n"
    "s_addc_u32 s71, s71, 0\n"
    "s_add_u32 m0, s78, 0x18c00\n"
    "v_mfma_f32_16x16x32_bf16 v[88:91], v[20:23], a[148:151], v[88:91]\n"
    "global_load_lds_dwordx4 v60, s[72:73]\n"
    "v_mad_u32_u24 v242, v241, s79, v242\n"
    "v_mfma_f32_16x16x32_bf16 v[92:95], v[20:23], a[204:207], v[92:95]\n"
    "s_waitcnt lgkmcnt(4)\n"
    "ds_read_b64_tr_b16 v[20:21], v4 offset:32\n"
    "ds_read_b64_tr_b16 v[22:23], v4 offset:288\n"
    "v_mfma_f32_16x16x32_bf16 v[96:99], v[24:27], a[36:39], v[96:99]\n"
    "global_load_lds_dwordx4 v61, s[72:73] offset:1056\n"
    "v_add_u32 v242, 0, v242\n"
    "v_mfma_f32_16x16x32_bf16 v[100:103], v[24:27], a[92:95], v[100:103]\n"
    "global_load_lds_dwordx4 v62, s[72:73] offset:2112\n"
    "v_accvgpr_write_b32 a227, v242\n"
    "v_mfma_f32_16x16x32_bf16 v[104:107], v[24:27], a[148:151], v[104:107]\n"
    "global_load_lds_dwordx4 v63, s[72:73] offset:3168\n"
    "v_add_u32 v240, 0x100, v126\n"
    "v_mfma_f32_16x16x32_bf16 v[108:111], v[24:27], a[204:207], v[108:111]\n"
    "s_waitcnt lgkmcnt(4)\n"
    "ds_read_b64_tr_b16 v[24:25], v4 offset:64\n"
    "ds_read_b64_tr_b16 v[26:27], v4 offset:320\n"
    "v_mfma_f32_16x16x32_bf16 v[112:115], v[28:31], a[36:39], v[112:115]\n"
    "v_mul_u32_u24 v241, 0x124a, v240\n"
    "v_lshrrev_b32 v241, 16, v241\n"
    "v_mfma_f32_16x16x32_bf16 v[116:119], v[28:31], a[92:95], v[116:119]\n"
    "s_add_u32 m0, s78, 0x9480\n"
    "s_waitcnt lgkmcnt(4)\n"
    "ds_read_b64_tr_b16 v[28:29], v4 offset:96\n"
    "ds_read_b64_tr_b16 v[30:31], v4 offset:352\n"
    "v_mfma_f32_16x16x32_bf16 v[64:67], v[16:19], a[40:43], v[64:67]\n"
    "global_load_lds_dwordx4 v0, s[70:71]\n"
    "v_mul_u32_u24 v242, 14, v241\n"
    "v_mfma_f32_16x16x32_bf16 v[68:71], v[16:19], a[96:99], v[68:71]\n"
    "global_load_lds_dwordx4 v1, s[70:71] offset:1056\n"
    "v_sub_u32 v242, v240, v242\n"
    "v_mfma_f32_16x16x32_bf16 v[72:75], v[16:19], a[152:155], v[72:75]\n"
    "global_load_lds_dwordx4 v2, s[70:71] offset:2112\n"
    "v_lshlrev_b32 v242, 4, v242\n"
    "v_mfma_f32_16x16x32_bf16 v[76:79], v[16:19], a[208:211], v[76:79]\n"
    "s_waitcnt lgkmcnt(4)\n"
    "s_waitcnt vmcnt(15)\n"
    "ds_read_b64_tr_b16 v[16:17], v4 offset:4224\n"
    "ds_read_b64_tr_b16 v[18:19], v4 offset:4480\n"
    "v_mfma_f32_16x16x32_bf16 v[80:83], v[20:23], a[40:43], v[80:83]\n"
    "global_load_lds_dwordx4 v3, s[70:71] offset:3168\n"
    "v_mad_u32_u24 v242, v241, s79, v242\n"
    "v_mfma_f32_16x16x32_bf16 v[84:87], v[20:23], a[96:99], v[84:87]\n"
    "s_add_u32 s70, s70, 0x1c000\n"
    "s_addc_u32 s71, s71, 0\n"
    "s_add_u32 m0, s78, 0x19c80\n"
    "v_mfma_f32_16x16x32_bf16 v[88:91], v[20:23], a[152:155], v[88:91]\n"
    "global_load_lds_dwordx4 v60, s[74:75]\n"
    "v_add_u32 v242, 0xc00, v242\n"
    "v_mfma_f32_16x16x32_bf16 v[92:95], v[20:23], a[208:211], v[92:95]\n"
    "s_waitcnt lgkmcnt(4)\n"
    "ds_read_b64_tr_b16 v[20:21], v4 offset:4256\n"
    "ds_read_b64_tr_b16 v[22:23], v4 offset:4512\n"
    "v_mfma_f32_16x16x32_bf16 v[96:99], v[24:27], a[40:43], v[96:99]\n"
    "s_add_u32 s72, s72, 0x40000\n"
    "s_addc_u32 s73, s73, 0\n"
    "global_load_lds_dwordx4 v61, s[74:75] offset:1056\n"
    "v_mfma_f32_16x16x32_bf16 v[100:103], v[24:27], a[96:99], v[100:103]\n"
    "global_load_lds_dwordx4 v62, s[74:75] offset:2112\n"
    "v_accvgpr_write_b32 a228, v242\n"
    "v_mfma_f32_16x16x32_bf16 v[104:107], v[24:27], a[152:155], v[104:107]\n"
    "global_load_lds_dwordx4 v63, s[74:75] offset:3168\n"
    "v_add_u32 v240, 0x140, v126\n"
    "v_mfma_f32_16x16x32_bf16 v[108:111], v[24:27], a[208:211], v[108:111]\n"
    "s_waitcnt lgkmcnt(4)\n"
    "ds_read_b64_tr_b16 v[24:25], v4 offset:4288\n"
    "ds_read_b64_tr_b16 v[26:27], v4 offset:4544\n"
    "v_mfma_f32_16x16x32_bf16 v[112:115], v[28:31], a[40:43], v[112:115]\n"
    "s_add_u32 s74, s74, 0x40000\n"
    "s_addc_u32 s75, s75, 0\n"
    "v_mul_u32_u24 v241, 0x124a, v240\n"
    "v_mfma_f32_16x16x32_bf16 v[116:119], v[28:31], a[96:99], v[116:119]\n"
    "s_waitcnt lgkmcnt(4)\n"
    "ds_read_b64_tr_b16 v[28:29], v4 offset:4320\n"
    "ds_read_b64_tr_b16 v[30:31], v4 offset:4576\n"
    "v_mfma_f32_16x16x32_bf16 v[64:67], v[16:19], a[44:47], v[64:67]\n"
    "v_lshrrev_b32 v241, 16, v241\n"
    "v_mul_u32_u24 v242, 14, v241\n"
    "v_mfma_f32_16x16x32_bf16 v[68:71], v[16:19], a[100:103], v[68:71]\n"
    "v_sub_u32 v242, v240, v242\n"
    "v_lshlrev_b32 v242, 4, v242\n"
    "v_mfma_f32_16x16x32_bf16 v[72:75], v[16:19], a[156:159], v[72:75]\n"
    "v_mad_u32_u24 v242, v241, s79, v242\n"
    "v_add_u32 v242, 0x800, v242\n"
    "v_mfma_f32_16x16x32_bf16 v[76:79], v[16:19], a[212:215], v[76:79]\n"
    "s_waitcnt lgkmcnt(4)\n"
    "s_waitcnt vmcnt(12)\n"
    "ds_read_b64_tr_b16 v[16:17], v4 offset:33792\n"
    "ds_read_b64_tr_b16 v[18:19], v4 offset:34048\n"
    "v_mfma_f32_16x16x32_bf16 v[80:83], v[20:23], a[44:47], v[80:83]\n"
    "v_accvgpr_write_b32 a229, v242\n"
    "v_add_u32 v240, 0x180, v126\n"
    "v_mfma_f32_16x16x32_bf16 v[84:87], v[20:23], a[100:103], v[84:87]\n"
    "v_mul_u32_u24 v241, 0x124a, v240\n"
    "v_lshrrev_b32 v241, 16, v241\n"
    "v_mfma_f32_16x16x32_bf16 v[88:91], v[20:23], a[156:159], v[88:91]\n"
    "v_mul_u32_u24 v242, 14, v241\n"
    "v_sub_u32 v242, v240, v242\n"
    "v_mfma_f32_16x16x32_bf16 v[92:95], v[20:23], a[212:215], v[92:95]\n"
    "s_waitcnt lgkmcnt(4)\n"
    "ds_read_b64_tr_b16 v[20:21], v4 offset:33824\n"
    "ds_read_b64_tr_b16 v[22:23], v4 offset:34080\n"
    "v_mfma_f32_16x16x32_bf16 v[96:99], v[24:27], a[44:47], v[96:99]\n"
    "v_lshlrev_b32 v242, 4, v242\n"
    "v_mad_u32_u24 v242, v241, s79, v242\n"
    "v_mfma_f32_16x16x32_bf16 v[100:103], v[24:27], a[100:103], v[100:103]\n"
    "v_add_u32 v242, 0x400, v242\n"
    "v_accvgpr_write_b32 a230, v242\n"
    "v_mfma_f32_16x16x32_bf16 v[104:107], v[24:27], a[156:159], v[104:107]\n"
    "v_lshrrev_b32 v240, 2, v126\n"
    "v_mul_u32_u24 v9, 0xe0, v240\n"
    "v_mfma_f32_16x16x32_bf16 v[108:111], v[24:27], a[212:215], v[108:111]\n"
    "s_waitcnt lgkmcnt(4)\n"
    "ds_read_b64_tr_b16 v[24:25], v4 offset:33856\n"
    "ds_read_b64_tr_b16 v[26:27], v4 offset:34112\n"
    "v_mfma_f32_16x16x32_bf16 v[112:115], v[28:31], a[44:47], v[112:115]\n"
    "s_add_u32 m0, s78, 0\n"
    "v_and_b32 v241, 3, v126\n"
    "v_lshl_add_u32 v9, v241, 3, v9\n"
    "v_mfma_f32_16x16x32_bf16 v[116:119], v[28:31], a[100:103], v[116:119]\n"
    "s_waitcnt lgkmcnt(4)\n"
    "ds_read_b64_tr_b16 v[28:29], v4 offset:33888\n"
    "ds_read_b64_tr_b16 v[30:31], v4 offset:34144\n"
    "v_mfma_f32_16x16x32_bf16 v[64:67], v[16:19], a[48:51], v[64:67]\n"
    "global_load_lds_dwordx4 v60, s[72:73]\n"
    "v_add_u32 v9, s78, v9\n"
    "v_mfma_f32_16x16x32_bf16 v[68:71], v[16:19], a[104:107], v[68:71]\n"
    "global_load_lds_dwordx4 v61, s[72:73] offset:1056\n"
    "v_add_u32 v10, 0x10800, v9\n"
    "v_mfma_f32_16x16x32_bf16 v[72:75], v[16:19], a[160:163], v[72:75]\n"
    "global_load_lds_dwordx4 v62, s[72:73] offset:2112\n"
    "v_lshlrev_b32 v11, 4, v126\n"
    "v_mfma_f32_16x16x32_bf16 v[76:79], v[16:19], a[216:219], v[76:79]\n"
    "s_waitcnt lgkmcnt(4)\n"
    "s_waitcnt vmcnt(7)\n"
    "ds_read_b64_tr_b16 v[16:17], v4 offset:38016\n"
    "ds_read_b64_tr_b16 v[18:19], v4 offset:38272\n"
    "v_mfma_f32_16x16x32_bf16 v[80:83], v[20:23], a[48:51], v[80:83]\n"
    "global_load_lds_dwordx4 v63, s[72:73] offset:3168\n"
    "v_add_u32 v11, 0x21000, v11\n"
    "v_mfma_f32_16x16x32_bf16 v[84:87], v[20:23], a[104:107], v[84:87]\n"
    "s_add_u32 m0, s78, 0x1080\n"
    "v_mfma_f32_16x16x32_bf16 v[88:91], v[20:23], a[160:163], v[88:91]\n"
    "global_load_lds_dwordx4 v60, s[74:75]\n"
    "v_mfma_f32_16x16x32_bf16 v[92:95], v[20:23], a[216:219], v[92:95]\n"
    "s_waitcnt lgkmcnt(4)\n"
    "ds_read_b64_tr_b16 v[20:21], v4 offset:38048\n"
    "ds_read_b64_tr_b16 v[22:23], v4 offset:38304\n"
    "v_mfma_f32_16x16x32_bf16 v[96:99], v[24:27], a[48:51], v[96:99]\n"
    "global_load_lds_dwordx4 v61, s[74:75] offset:1056\n"
    "v_mfma_f32_16x16x32_bf16 v[100:103], v[24:27], a[104:107], v[100:103]\n"
    "global_load_lds_dwordx4 v62, s[74:75] offset:2112\n"
    "v_mfma_f32_16x16x32_bf16 v[104:107], v[24:27], a[160:163], v[104:107]\n"
    "global_load_lds_dwordx4 v63, s[74:75] offset:3168\n"
    "v_mfma_f32_16x16x32_bf16 v[108:111], v[24:27], a[216:219], v[108:111]\n"
    "s_waitcnt lgkmcnt(4)\n"
    "ds_read_b64_tr_b16 v[24:25], v4 offset:38080\n"
    "ds_read_b64_tr_b16 v[26:27], v4 offset:38336\n"
    "v_mfma_f32_16x16x32_bf16 v[112:115], v[28:31], a[48:51], v[112:115]\n"
    "s_add_u32 s72, s72, 0x40000\n"
    "s_addc_u32 s73, s73, 0\n"
    "v_mfma_f32_16x16x32_bf16 v[116:119], v[28:31], a[104:107], v[116:119]\n"
    "s_waitcnt lgkmcnt(4)\n"
    "ds_read_b64_tr_b16 v[28:29], v4 offset:38112\n"
    "ds_read_b64_tr_b16 v[30:31], v4 offset:38368\n"
    "v_mfma_f32_16x16x32_bf16 v[64:67], v[16:19], a[52:55], v[64:67]\n"
    "s_add_u32 s74, s74, 0x40000\n"
    "s_addc_u32 s75, s75, 0\n"
    "v_mfma_f32_16x16x32_bf16 v[68:71], v[16:19], a[108:111], v[68:71]\n"
    "v_mfma_f32_16x16x32_bf16 v[72:75], v[16:19], a[164:167], v[72:75]\n"
    "v_mfma_f32_16x16x32_bf16 v[76:79], v[16:19], a[220:223], v[76:79]\n"
    "s_waitcnt lgkmcnt(4)\n"
    "v_mfma_f32_16x16x32_bf16 v[80:83], v[20:23], a[52:55], v[80:83]\n"
    "v_mfma_f32_16x16x32_bf16 v[84:87], v[20:23], a[108:111], v[84:87]\n"
    "v_mfma_f32_16x16x32_bf16 v[88:91], v[20:23], a[164:167], v[88:91]\n"
    "v_mfma_f32_16x16x32_bf16 v[92:95], v[20:23], a[220:223], v[92:95]\n"
    "s_waitcnt lgkmcnt(2)\n"
    "v_mfma_f32_16x16x32_bf16 v[96:99], v[24:27], a[52:55], v[96:99]\n"
    "v_mfma_f32_16x16x32_bf16 v[100:103], v[24:27], a[108:111], v[100:103]\n"
    "v_mfma_f32_16x16x32_bf16 v[104:107], v[24:27], a[164:167], v[104:107]\n"
    "v_mfma_f32_16x16x32_bf16 v[108:111], v[24:27], a[220:223], v[108:111]\n"
    "s_waitcnt lgkmcnt(0)\n"
    "v_mfma_f32_16x16x32_bf16 v[112:115], v[28:31], a[52:55], v[112:115]\n"
    "v_mfma_f32_16x16x32_bf16 v[116:119], v[28:31], a[108:111], v[116:119]\n"
    "v_mov_b32 v127, 0xbdd2d3e8\n"
    "ds_read_b64_tr_b16 v[16:17], v6\n"
    "ds_read_b64_tr_b16 v[18:19], v6 offset:256\n"
    "ds_read_b64_tr_b16 v[20:21], v6 offset:32\n"
    "ds_read_b64_tr_b16 v[22:23], v6 offset:288\n"
    "ds_read_b64_tr_b16 v[24:25], v6 offset:64\n"
    "ds_read_b64_tr_b16 v[26:27], v6 offset:320\n"
    "ds_read_b64_tr_b16 v[28:29], v6 offset:96\n"
    "ds_read_b64_tr_b16 v[30:31], v6 offset:352\n"
    "s_waitcnt lgkmcnt(6)\n"
    "v_mfma_f32_16x16x32_bf16 v[128:131], v[16:19], a[0:3], 0\n"
    "s_add_u32 m0, s78, 0x8400\n"
    "v_mfma_f32_16x16x32_bf16 v[132:135], v[16:19], a[56:59], 0\n"
    "global_load_lds_dwordx4 v60, s[72:73]\n"
    "v_mfma_f32_16x16x32_bf16 v[136:139], v[16:19], a[112:115], 0\n"
    "v_fma_f32 v64, v64, v12, v32\n"
    "v_fma_f32 v65, v65, v12, v33\n"
    "v_fma_f32 v66, v66, v12, v34\n"
    "v_mfma_f32_16x16x32_bf16 v[140:143], v[16:19], a[168:171], 0\n"
    "s_waitcnt lgkmcnt(4)\n"
    "ds_read_b64_tr_b16 v[16:17], v6 offset:128\n"
    "ds_read_b64_tr_b16 v[18:19], v6 offset:384\n"
    "v_mfma_f32_16x16x32_bf16 v[144:147], v[20:23], a[0:3], 0\n"
    "v_fma_f32 v67, v67, v12, v35\n"
    "v_mul_f32 v48, v64, v64\n"
    "v_mfma_f32_16x16x32_bf16 v[148:151], v[20:23], a[56:59], 0\n"
    "global_load_lds_dwordx4 v61, s[72:73] offset:1056\n"
    "v_mul_f32 v49, v65, v65\n"
    "v_mfma_f32_16x16x32_bf16 v[152:155], v[20:23], a[112:115], 0\n"
    "v_mul_f32 v50, v66, v66\n"
    "v_mul_f32 v51, v67, v67\n"
    "v_mfma_f32_16x16x32_bf16 v[156:159], v[20:23], a[168:171], 0\n"
    "s_waitcnt lgkmcnt(4)\n"
    "ds_read_b64_tr_b16 v[20:21], v6 offset:160\n"
    "ds_read_b64_tr_b16 v[22:23], v6 offset:416\n"
    "v_fmaak_f32 v48, v48, v127, 0xc0135761\n"
    "v_mfma_f32_16x16x32_bf16 v[160:163], v[24:27], a[0:3], 0\n"
    "v_fmaak_f32 v49, v49, v127, 0xc0135761\n"
    "v_fmaak_f32 v50, v50, v127, 0xc0135761\n"
    "v_mfma_f32_16x16x32_bf16 v[164:167], v[24:27], a[56:59], 0\n"
    "global_load_lds_dwordx4 v62, s[72:73] offset:2112\n"
    "v_fmaak_f32 v51, v51, v127, 0xc0135761\n"
    "v_mfma_f32_16x16x32_bf16 v[168:171], v[24:27], a[112:115], 0\n"
    "v_mul_f32 v48, v64, v48\n"
    "v_mul_f32 v49, v65, v49\n"
    "v_mfma_f32_16x16x32_bf16 v[172:175], v[24:27], a[168:171], 0\n"
    "s_waitcnt lgkmcnt(4)\n"
    "ds_read_b64_tr_b16 v[24:25], v6 offset:192\n"
    "ds_read_b64_tr_b16 v[26:27], v6 offset:448\n"
    "v_mfma_f32_16x16x32_bf16 v[176:179], v[28:31], a[0:3], 0\n"
    "v_mul_f32 v50, v66, v50\n"
    "v_mul_f32 v51, v67, v51\n"
    "v_fma_f32 v68, v68, v13, v32\n"
    "v_mfma_f32_16x16x32_bf16 v[180:183], v[28:31], a[56:59], 0\n"
    "global_load_lds_dwordx4 v63, s[72:73] offset:3168\n"
    "v_fma_f32 v69, v69, v13, v33\n"
    "v_mfma_f32_16x16x32_bf16 v[184:187], v[28:31], a[112:115], 0\n"
    "v_exp_f32 v48, v48\n"
    "v_mfma_f32_16x16x32_bf16 v[188:191], v[28:31], a[168:171], 0\n"
    "s_waitcnt lgkmcnt(4)\n"
    "s_add_u32 s72, s72, 0x40000\n"
    "s_addc_u32 s73, s73, 0\n"
    "ds_read_b64_tr_b16 v[28:29], v6 offset:224\n"
    "ds_read_b64_tr_b16 v[30:31], v6 offset:480\n"
    "v_mfma_f32_16x16x32_bf16 v[192:195], v[16:19], a[0:3], 0\n"
    "s_add_u32 m0, s78, 0x9480\n"
    "v_exp_f32 v49, v49\n"
    "v_mfma_f32_16x16x32_bf16 v[196:199], v[16:19], a[56:59], 0\n"
    "global_load_lds_dwordx4 v60, s[74:75]\n"
    "v_exp_f32 v50, v50\n"
    "v_mfma_f32_16x16x32_bf16 v[200:203], v[16:19], a[112:115], 0\n"
    "v_exp_f32 v51, v51\n"
    "v_mfma_f32_16x16x32_bf16 v[204:207], v[16:19], a[168:171], 0\n"
    "s_waitcnt lgkmcnt(4)\n"
    "s_waitcnt vmcnt(13)\n"
    "ds_read_b64_tr_b16 v[16:17], v6 offset:33792\n"
    "ds_read_b64_tr_b16 v[18:19], v6 offset:34048\n"
    "v_mfma_f32_16x16x32_bf16 v[208:211], v[20:23], a[0:3], 0\n"
    "v_add_f32 v48, 1.0, v48\n"
    "v_add_f32 v49, 1.0, v49\n"
    "v_mfma_f32_16x16x32_bf16 v[212:215], v[20:23], a[56:59], 0\n"
    "global_load_lds_dwordx4 v61, s[74:75] offset:1056\n"
    "v_add_f32 v50, 1.0, v50\n"
    "v_mfma_f32_16x16x32_bf16 v[216:219], v[20:23], a[112:115], 0\n"
    "v_add_f32 v51, 1.0, v51\n"
    "v_rcp_f32 v48, v48\n"
    "v_mfma_f32_16x16x32_bf16 v[220:223], v[20:23], a[168:171], 0\n"
    "s_waitcnt lgkmcnt(4)\n"
    "ds_read_b64_tr_b16 v[20:21], v6 offset:33824\n"
    "ds_read_b64_tr_b16 v[22:23], v6 offset:34080\n"
    "v_mfma_f32_16x16x32_bf16 v[224:227], v[24:27], a[0:3], 0\n"
    "v_rcp_f32 v49, v49\n"
    "v_mfma_f32_16x16x32_bf16 v[228:231], v[24:27], a[56:59], 0\n"
    "global_load_lds_dwordx4 v62, s[74:75] offset:2112\n"
    "v_fma_f32 v70, v70, v13, v34\n"
    "v_mfma_f32_16x16x32_bf16 v[232:235], v[24:27], a[112:115], 0\n"
    "v_rcp_f32 v50, v50\n"
    "v_mfma_f32_16x16x32_bf16 v[236:239], v[24:27], a[168:171], 0\n"
    "s_waitcnt lgkmcnt(4)\n"
    "ds_read_b64_tr_b16 v[24:25], v6 offset:33856\n"
    "ds_read_b64_tr_b16 v[26:27], v6 offset:34112\n"
    "v_fma_f32 v71, v71, v13, v35\n"
    "v_mfma_f32_16x16x32_bf16 v[240:243], v[28:31], a[0:3], 0\n"
    "v_rcp_f32 v51, v51\n"
    "v_mfma_f32_16x16x32_bf16 v[244:247], v[28:31], a[56:59], 0\n"
    "global_load_lds_dwordx4 v63, s[74:75] offset:3168\n"
    "v_mul_f32 v64, v64, v48\n"
    "v_mfma_f32_16x16x32_bf16 v[248:251], v[28:31], a[112:115], 0\n"
    "v_mul_f32 v65, v65, v49\n"
    "v_mul_f32 v66, v66, v50\n"
    "v_mfma_f32_16x16x32_bf16 v[252:255], v[28:31], a[168:171], 0\n"
    "s_add_u32 s74, s74, 0x40000\n"
    "s_addc_u32 s75, s75, 0\n"
    "s_waitcnt lgkmcnt(4)\n"
    "ds_read_b64_tr_b16 v[28:29], v6 offset:33888\n"
    "ds_read_b64_tr_b16 v[30:31], v6 offset:34144\n"
    "v_mfma_f32_16x16x32_bf16 v[128:131], v[16:19], a[4:7], v[128:131]\n"
    "s_add_u32 m0, s78, 0x10800\n"
    "v_mul_f32 v67, v67, v51\n"
    "v_cvt_pk_bf16_f32 v64, v64, v65\n"
    "v_cvt_pk_bf16_f32 v65, v66, v67\n"
    "v_mfma_f32_16x16x32_bf16 v[132:135], v[16:19], a[60:63], v[132:135]\n"
    "global_load_lds_dwordx4 v60, s[72:73]\n"
    "v_mul_f32 v52, v68, v68\n"
    "v_mfma_f32_16x16x32_bf16 v[136:139], v[16:19], a[116:119], v[136:139]\n"
    "ds_write_b64 v120, v[64:65]\n"
    "v_mfma_f32_16x16x32_bf16 v[140:143], v[16:19], a[172:175], v[140:143]\n"
    "s_waitcnt lgkmcnt(5)\n"
    "ds_read_b64_tr_b16 v[16:17], v6 offset:33920\n"
    "ds_read_b64_tr_b16 v[18:19], v6 offset:34176\n"
    "v_mfma_f32_16x16x32_bf16 v[144:147], v[20:23], a[4:7], v[144:147]\n"
    "v_fma_f32 v76, v76, v15, v32\n"
    "v_fma_f32 v77, v77, v15, v33\n"
    "v_mfma_f32_16x16x32_bf16 v[148:151], v[20:23], a[60:63], v[148:151]\n"
    "global_load_lds_dwordx4 v61, s[72:73] offset:1056\n"
    "v_fma_f32 v78, v78, v15, v34\n"
    "v_fma_f32 v79, v79, v15, v35\n"
    "v_mfma_f32_16x16x32_bf16 v[152:155], v[20:23], a[116:119], v[152:155]\n"
    "v_mul_f32 v48, v76, v76\n"
    "v_mul_f32 v49, v77, v77\n"
    "v_mfma_f32_16x16x32_bf16 v[156:159], v[20:23], a[172:175], v[156:159]\n"
    "s_waitcnt lgkmcnt(5)\n"
    "ds_read_b64_tr_b16 v[20:21], v6 offset:33952\n"
    "ds_read_b64_tr_b16 v[22:23], v6 offset:34208\n"
    "v_mfma_f32_16x16x32_bf16 v[160:163], v[24:27], a[4:7], v[160:163]\n"
    "v_mul_f32 v50, v78, v78\n"
    "v_mul_f32 v51, v79, v79\n"
    "v_mfma_f32_16x16x32_bf16 v[164:167], v[24:27], a[60:63], v[164:167]\n"
    "global_load_lds_dwordx4 v62, s[72:73] offset:2112\n"
    "v_fmaak_f32 v48, v48, v127, 0xc0135761\n"
    "v_mfma_f32_16x16x32_bf16 v[168:171], v[24:27], a[116:119], v[168:171]\n"
    "v_fmaak_f32 v49, v49, v127, 0xc0135761\n"
    "v_fmaak_f32 v50, v50, v127, 0xc0135761\n"
    "v_fmaak_f32 v51, v51, v127, 0xc0135761\n"
    "v_mfma_f32_16x16x32_bf16 v[172:175], v[24:27], a[172:175], v[172:175]\n"
    "s_waitcnt lgkmcnt(5)\n"
    "ds_read_b64_tr_b16 v[24:25], v6 offset:33984\n"
    "ds_read_b64_tr_b16 v[26:27], v6 offset:34240\n"
    "v_mfma_f32_16x16x32_bf16 v[176:179], v[28:31], a[4:7], v[176:179]\n"
    "v_mul_f32 v48, v76, v48\n"
    "v_mul_f32 v49, v77, v49\n"
    "v_mfma_f32_16x16x32_bf16 v[180:183], v[28:31], a[60:63], v[180:183]\n"
    "global_load_lds_dwordx4 v63, s[72:73] offset:3168\n"
    "v_mul_f32 v50, v78, v50\n"
    "v_mfma_f32_16x16x32_bf16 v[184:187], v[28:31], a[116:119], v[184:187]\n"
    "v_mul_f32 v51, v79, v51\n"
    "v_mul_f32 v53, v69, v69\n"
    "v_mfma_f32_16x16x32_bf16 v[188:191], v[28:31], a[172:175], v[188:191]\n"
    "s_add_u32 s72, s72, 0x40000\n"
    "s_addc_u32 s73, s73, 0\n"
    "s_waitcnt lgkmcnt(4)\n"
    "ds_read_b64_tr_b16 v[28:29], v6 offset:34016\n"
    "ds_read_b64_tr_b16 v[30:31], v6 offset:34272\n"
    "v_mfma_f32_16x16x32_bf16 v[192:195], v[16:19], a[4:7], v[192:195]\n"
    "s_add_u32 m0, s78, 0x11880\n"
    "v_exp_f32 v48, v48\n"
    "v_mfma_f32_16x16x32_bf16 v[196:199], v[16:19], a[60:63], v[196:199]\n"
    "global_load_lds_dwordx4 v60, s[74:75]\n"
    "v_mul_f32 v54, v70, v70\n"
    "v_mfma_f32_16x16x32_bf16 v[200:203], v[16:19], a[116:119], v[200:203]\n"
    "v_exp_f32 v49, v49\n"
    "v_mfma_f32_16x16x32_bf16 v[204:207], v[16:19], a[172:175], v[204:207]\n"
    "s_waitcnt lgkmcnt(4)\n"
    "s_waitcnt vmcnt(13)\n"
    "ds_read_b64_tr_b16 v[16:17], v5\n"
    "ds_read_b64_tr_b16 v[18:19], v5 offset:256\n"
    "v_mfma_f32_16x16x32_bf16 v[208:211], v[20:23], a[4:7], v[208:211]\n"
    "v_exp_f32 v50, v50\n"
    "v_mul_f32 v55, v71, v71\n"
    "v_mfma_f32_16x16x32_bf16 v[212:215], v[20:23], a[60:63], v[212:215]\n"
    "global_load_lds_dwordx4 v61, s[74:75] offset:1056\n"
    "v_fmaak_f32 v52, v52, v127, 0xc0135761\n"
    "v_mfma_f32_16x16x32_bf16 v[216:219], v[20:23], a[116:119], v[216:219]\n"
    "v_exp_f32 v51, v51\n"
    "v_mfma_f32_16x16x32_bf16 v[220:223], v[20:23], a[172:175], v[220:223]\n"
    "s_waitcnt lgkmcnt(4)\n"
    "ds_read_b64_tr_b16 v[20:21], v5 offset:32\n"
    "ds_read_b64_tr_b16 v[22:23], v5 offset:288\n"
    "v_mfma_f32_16x16x32_bf16 v[224:227], v[24:27], a[4:7], v[224:227]\n"
    "v_add_f32 v48, 1.0, v48\n"
    "v_add_f32 v49, 1.0, v49\n"
    "v_mfma_f32_16x16x32_bf16 v[228:231], v[24:27], a[60:63], v[228:231]\n"
    "global_load_lds_dwordx4 v62, s[74:75] offset:2112\n"
    "v_add_f32 v50, 1.0, v50\n"
    "v_add_f32 v51, 1.0, v51\n"
    "v_mfma_f32_16x16x32_bf16 v[232:235], v[24:27], a[116:119], v[232:235]\n"
    "v_rcp_f32 v48, v48\n"
    "v_mfma_f32_16x16x32_bf16 v[236:239], v[24:27], a[172:175], v[236:239]\n"
    "s_waitcnt lgkmcnt(4)\n"
    "ds_read_b64_tr_b16 v[24:25], v5 offset:64\n"
    "ds_read_b64_tr_b16 v[26:27], v5 offset:320\n"
    "v_mfma_f32_16x16x32_bf16 v[240:243], v[28:31], a[4:7], v[240:243]\n"
    "v_rcp_f32 v49, v49\n"
    "v_mfma_f32_16x16x32_bf16 v[244:247], v[28:31], a[60:63], v[244:247]\n"
    "global_load_lds_dwordx4 v63, s[74:75] offset:3168\n"
    "v_fmaak_f32 v53, v53, v127, 0xc0135761\n"
    "v_mfma_f32_16x16x32_bf16 v[248:251], v[28:31], a[116:119], v[248:251]\n"
    "v_rcp_f32 v50, v50\n"
    "v_fmaak_f32 v54, v54, v127, 0xc0135761\n"
    "v_mfma_f32_16x16x32_bf16 v[252:255], v[28:31], a[172:175], v[252:255]\n"
    "s_add_u32 s74, s74, 0x40000\n"
    "s_addc_u32 s75, s75, 0\n"
    "s_waitcnt lgkmcnt(4)\n"
    "ds_read_b64_tr_b16 v[28:29], v5 offset:96\n"
    "ds_read_b64_tr_b16 v[30:31], v5 offset:352\n"
    "v_mfma_f32_16x16x32_bf16 v[128:131], v[16:19], a[8:11], v[128:131]\n"
    "s_add_u32 m0, s78, 0x18c00\n"
    "v_rcp_f32 v51, v51\n"
    "v_mfma_f32_16x16x32_bf16 v[132:135], v[16:19], a[64:67], v[132:135]\n"
    "global_load_lds_dwordx4 v60, s[72:73]\n"
    "v_mul_f32 v76, v76, v48\n"
    "v_mfma_f32_16x16x32_bf16 v[136:139], v[16:19], a[120:123], v[136:139]\n"
    "v_mul_f32 v77, v77, v49\n"
    "v_mul_f32 v78, v78, v50\n"
    "v_mfma_f32_16x16x32_bf16 v[140:143], v[16:19], a[176:179], v[140:143]\n"
    "s_waitcnt lgkmcnt(4)\n"
    "ds_read_b64_tr_b16 v[16:17], v5 offset:128\n"
    "ds_read_b64_tr_b16 v[18:19], v5 offset:384\n"
    "v_mul_f32 v79, v79, v51\n"
    "v_mfma_f32_16x16x32_bf16 v[144:147], v[20:23], a[8:11], v[144:147]\n"
    "v_cvt_pk_bf16_f32 v76, v76, v77\n"
    "v_cvt_pk_bf16_f32 v77, v78, v79\n"
    "v_mfma_f32_16x16x32_bf16 v[148:151], v[20:23], a[64:67], v[148:151]\n"
    "global_load_lds_dwordx4 v61, s[72:73] offset:1056\n"
    "v_fmaak_f32 v55, v55, v127, 0xc0135761\n"
    "v_mfma_f32_16x16x32_bf16 v[152:155], v[20:23], a[120:123], v[152:155]\n"
    "v_mul_f32 v52, v68, v52\n"
    "v_mul_f32 v53, v69, v53\n"
    "v_mfma_f32_16x16x32_bf16 v[156:159], v[20:23], a[176:179], v[156:159]\n"
    "s_waitcnt lgkmcnt(4)\n"
    "ds_read_b64_tr_b16 v[20:21], v5 offset:160\n"
    "ds_read_b64_tr_b16 v[22:23], v5 offset:416\n"
    "v_mfma_f32_16x16x32_bf16 v[160:163], v[24:27], a[8:11], v[160:163]\n"
    "v_mul_f32 v54, v70, v54\n"
    "v_mul_f32 v55, v71, v55\n"
    "v_fma_f32 v72, v72, v14, v32\n"
    "v_mfma_f32_16x16x32_bf16 v[164:167], v[24:27], a[64:67], v[164:167]\n"
    "global_load_lds_dwordx4 v62, s[72:73] offset:2112\n"
    "v_fma_f32 v73, v73, v14, v33\n"
    "v_mfma_f32_16x16x32_bf16 v[168:171], v[24:27], a[120:123], v[168:171]\n"
    "v_exp_f32 v52, v52\n"
    "v_mfma_f32_16x16x32_bf16 v[172:175], v[24:27], a[176:179], v[172:175]\n"
    "s_waitcnt lgkmcnt(4)\n"
    "ds_read_b64_tr_b16 v[24:25], v5 offset:192\n"
    "ds_read_b64_tr_b16 v[26:27], v5 offset:448\n"
    "v_mfma_f32_16x16x32_bf16 v[176:179], v[28:31], a[8:11], v[176:179]\n"
    "v_exp_f32 v53, v53\n"
    "v_mfma_f32_16x16x32_bf16 v[180:183], v[28:31], a[64:67], v[180:183]\n"
    "global_load_lds_dwordx4 v63, s[72:73] offset:3168\n"
    "v_fma_f32 v74, v74, v14, v34\n"
    "v_mfma_f32_16x16x32_bf16 v[184:187], v[28:31], a[120:123], v[184:187]\n"
    "v_exp_f32 v54, v54\n"
    "v_fma_f32 v75, v75, v14, v35\n"
    "v_mfma_f32_16x16x32_bf16 v[188:191], v[28:31], a[176:179], v[188:191]\n"
    "s_add_u32 s72, s72, 0x40000\n"
    "s_addc_u32 s73, s73, 0\n"
    "s_waitcnt lgkmcnt(4)\n"
    "ds_read_b64_tr_b16 v[28:29], v5 offset:224\n"
    "ds_read_b64_tr_b16 v[30:31], v5 offset:480\n"
    "v_mfma_f32_16x16x32_bf16 v[192:195], v[16:19], a[8:11], v[192:195]\n"
    "s_add_u32 m0, s78, 0x19c80\n"
    "v_exp_f32 v55, v55\n"
    "v_mfma_f32_16x16x32_bf16 v[196:199], v[16:19], a[64:67], v[196:199]\n"
    "global_load_lds_dwordx4 v60, s[74:75]\n"
    "v_add_f32 v52, 1.0, v52\n"
    "v_mfma_f32_16x16x32_bf16 v[200:203], v[16:19], a[120:123], v[200:203]\n"
    "v_add_f32 v53, 1.0, v53\n"
    "v_add_f32 v54, 1.0, v54\n"
    "v_mfma_f32_16x16x32_bf16 v[204:207], v[16:19], a[176:179], v[204:207]\n"
    "s_waitcnt lgkmcnt(4)\n"
    "s_waitcnt vmcnt(13)\n"
    "ds_read_b64_tr_b16 v[16:17], v5 offset:33792\n"
    "ds_read_b64_tr_b16 v[18:19], v5 offset:34048\n"
    "v_add_f32 v55, 1.0, v55\n"
    "v_mfma_f32_16x16x32_bf16 v[208:211], v[20:23], a[8:11], v[208:211]\n"
    "v_rcp_f32 v52, v52\n"
    "v_mfma_f32_16x16x32_bf16 v[212:215], v[20:23], a[64:67], v[212:215]\n"
    "global_load_lds_dwordx4 v61, s[74:75] offset:1056\n"
    "v_mul_f32 v56, v72, v72\n"
    "v_mfma_f32_16x16x32_bf16 v[216:219], v[20:23], a[120:123], v[216:219]\n"
    "v_rcp_f32 v53, v53\n"
    "v_mfma_f32_16x16x32_bf16 v[220:223], v[20:23], a[176:179], v[220:223]\n"
    "s_waitcnt lgkmcnt(4)\n"
    "ds_read_b64_tr_b16 v[20:21], v5 offset:33824\n"
    "ds_read_b64_tr_b16 v[22:23], v5 offset:34080\n"
    "v_mfma_f32_16x16x32_bf16 v[224:227], v[24:27], a[8:11], v[224:227]\n"
    "v_rcp_f32 v54, v54\n"
    "v_mul_f32 v57, v73, v73\n"
    "v_mfma_f32_16x16x32_bf16 v[228:231], v[24:27], a[64:67], v[228:231]\n"
    "global_load_lds_dwordx4 v62, s[74:75] offset:2112\n"
    "v_mul_f32 v58, v74, v74\n"
    "v_mfma_f32_16x16x32_bf16 v[232:235], v[24:27], a[120:123], v[232:235]\n"
    "v_rcp_f32 v55, v55\n"
    "v_mfma_f32_16x16x32_bf16 v[236:239], v[24:27], a[176:179], v[236:239]\n"
    "s_waitcnt lgkmcnt(4)\n"
    "ds_read_b64_tr_b16 v[24:25], v5 offset:33856\n"
    "ds_read_b64_tr_b16 v[26:27], v5 offset:34112\n"
    "v_mfma_f32_16x16x32_bf16 v[240:243], v[28:31], a[8:11], v[240:243]\n"
    "v_mul_f32 v68, v68, v52\n"
    "v_mul_f32 v69, v69, v53\n"
    "v_mfma_f32_16x16x32_bf16 v[244:247], v[28:31], a[64:67], v[244:247]\n"
    "global_load_lds_dwordx4 v63, s[74:75] offset:3168\n"
    "v_mul_f32 v70, v70, v54\n"
    "v_mul_f32 v71, v71, v55\n"
    "v_mfma_f32_16x16x32_bf16 v[248:251], v[28:31], a[120:123], v[248:251]\n"
    "v_cvt_pk_bf16_f32 v68, v68, v69\n"
    "v_cvt_pk_bf16_f32 v69, v70, v71\n"
    "v_mfma_f32_16x16x32_bf16 v[252:255], v[28:31], a[176:179], v[252:255]\n"
    "s_add_u32 s74, s74, 0x40000\n"
    "s_addc_u32 s75, s75, 0\n"
    "s_waitcnt lgkmcnt(4)\n"
    "ds_read_b64_tr_b16 v[28:29], v5 offset:33888\n"
    "ds_read_b64_tr_b16 v[30:31], v5 offset:34144\n"
    "v_mfma_f32_16x16x32_bf16 v[128:131], v[16:19], a[12:15], v[128:131]\n"
    "s_add_u32 m0, s78, 0\n"
    "ds_write_b64 v120, v[68:69] offset:1024\n"
    "v_mfma_f32_16x16x32_bf16 v[132:135], v[16:19], a[68:71], v[132:135]\n"
    "global_load_lds_dwordx4 v60, s[72:73]\n"
    "v_fma_f32 v80, v80, v12, v36\n"
    "v_mfma_f32_16x16x32_bf16 v[136:139], v[16:19], a[124:127], v[136:139]\n"
    "v_fma_f32 v81, v81, v12, v37\n"
    "v_fma_f32 v82, v82, v12, v38\n"
    "v_fma_f32 v83, v83, v12, v39\n"
    "v_mfma_f32_16x16x32_bf16 v[140:143], v[16:19], a[180:183], v[140:143]\n"
    "s_waitcnt lgkmcnt(5)\n"
    "ds_read_b64_tr_b16 v[16:17], v5 offset:33920\n"
    "ds_read_b64_tr_b16 v[18:19], v5 offset:34176\n"
    "v_mfma_f32_16x16x32_bf16 v[144:147], v[20:23], a[12:15], v[144:147]\n"
    "v_mul_f32 v52, v80, v80\n"
    "v_mul_f32 v53, v81, v81\n"
    "v_mfma_f32_16x16x32_bf16 v[148:151], v[20:23], a[68:71], v[148:151]\n"
    "global_load_lds_dwordx4 v61, s[72:73] offset:1056\n"
    "v_mul_f32 v54, v82, v82\n"
    "v_mfma_f32_16x16x32_bf16 v[152:155], v[20:23], a[124:127], v[152:155]\n"
    "v_mul_f32 v55, v83, v83\n"
    "v_fmaak_f32 v52, v52, v127, 0xc0135761\n"
    "v_mfma_f32_16x16x32_bf16 v[156:159], v[20:23], a[180:183], v[156:159]\n"
    "s_waitcnt lgkmcnt(5)\n"
    "ds_read_b64_tr_b16 v[20:21], v5 offset:33952\n"
    "ds_read_b64_tr_b16 v[22:23], v5 offset:34208\n"
    "v_fmaak_f32 v53, v53, v127, 0xc0135761\n"
    "v_mfma_f32_16x16x32_bf16 v[160:163], v[24:27], a[12:15], v[160:163]\n"
    "v_fmaak_f32 v54, v54, v127, 0xc0135761\n"
    "v_fmaak_f32 v55, v55, v127, 0xc0135761\n"
    "v_mfma_f32_16x16x32_bf16 v[164:167], v[24:27], a[68:71], v[164:167]\n"
    "global_load_lds_dwordx4 v62, s[72:73] offset:2112\n"
    "v_mul_f32 v52, v80, v52\n"
    "v_mfma_f32_16x16x32_bf16 v[168:171], v[24:27], a[124:127], v[168:171]\n"
    "v_mul_f32 v53, v81, v53\n"
    "v_mul_f32 v54, v82, v54\n"
    "v_mfma_f32_16x16x32_bf16 v[172:175], v[24:27], a[180:183], v[172:175]\n"
    "s_waitcnt lgkmcnt(5)\n"
    "ds_read_b64_tr_b16 v[24:25], v5 offset:33984\n"
    "ds_read_b64_tr_b16 v[26:27], v5 offset:34240\n"
    "v_mfma_f32_16x16x32_bf16 v[176:179], v[28:31], a[12:15], v[176:179]\n"
    "v_mul_f32 v55, v83, v55\n"
    "v_exp_f32 v52, v52\n"
    "v_mfma_f32_16x16x32_bf16 v[180:183], v[28:31], a[68:71], v[180:183]\n"
    "global_load_lds_dwordx4 v63, s[72:73] offset:3168\n"
    "v_mul_f32 v59, v75, v75\n"
    "v_mfma_f32_16x16x32_bf16 v[184:187], v[28:31], a[124:127], v[184:187]\n"
    "v_exp_f32 v53, v53\n"
    "v_mfma_f32_16x16x32_bf16 v[188:191], v[28:31], a[180:183], v[188:191]\n"
    "s_add_u32 s72, s72, 0x40000\n"
    "s_addc_u32 s73, s73, 0\n"
    "s_waitcnt lgkmcnt(4)\n"
    "ds_read_b64_tr_b16 v[28:29], v5 offset:34016\n"
    "ds_read_b64_tr_b16 v[30:31], v5 offset:34272\n"
    "v_mfma_f32_16x16x32_bf16 v[192:195], v[16:19], a[12:15], v[192:195]\n"
    "s_add_u32 m0, s78, 0x1080\n"
    "v_exp_f32 v54, v54\n"
    "v_mfma_f32_16x16x32_bf16 v[196:199], v[16:19], a[68:71], v[196:199]\n"
    "global_load_lds_dwordx4 v60, s[74:75]\n"
    "v_exp_f32 v55, v55\n"
    "v_mfma_f32_16x16x32_bf16 v[200:203], v[16:19], a[124:127], v[200:203]\n"
    "v_add_f32 v52, 1.0, v52\n"
    "v_add_f32 v53, 1.0, v53\n"
    "v_mfma_f32_16x16x32_bf16 v[204:207], v[16:19], a[180:183], v[204:207]\n"
    "s_waitcnt lgkmcnt(4)\n"
    "s_waitcnt vmcnt(13)\n"
    "ds_read_b64_tr_b16 v[16:17], v6\n"
    "ds_read_b64_tr_b16 v[18:19], v6 offset:256\n"
    "v_mfma_f32_16x16x32_bf16 v[208:211], v[20:23], a[12:15], v[208:211]\n"
    "v_add_f32 v54, 1.0, v54\n"
    "v_add_f32 v55, 1.0, v55\n"
    "v_mfma_f32_16x16x32_bf16 v[212:215], v[20:23], a[68:71], v[212:215]\n"
    "global_load_lds_dwordx4 v61, s[74:75] offset:1056\n"
    "v_fmaak_f32 v56, v56, v127, 0xc0135761\n"
    "v_mfma_f32_16x16x32_bf16 v[216:219], v[20:23], a[124:127], v[216:219]\n"
    "v_rcp_f32 v52, v52\n"
    "v_fmaak_f32 v57, v57, v127, 0xc0135761\n"
    "v_mfma_f32_16x16x32_bf16 v[220:223], v[20:23], a[180:183], v[220:223]\n"
    "s_waitcnt lgkmcnt(4)\n"
    "ds_read_b64_tr_b16 v[20:21], v6 offset:32\n"
    "ds_read_b64_tr_b16 v[22:23], v6 offset:288\n"
    "v_mfma_f32_16x16x32_bf16 v[224:227], v[24:27], a[12:15], v[224:227]\n"
    "v_rcp_f32 v53, v53\n"
    "v_mfma_f32_16x16x32_bf16 v[228:231], v[24:27], a[68:71], v[228:231]\n"
    "global_load_lds_dwordx4 v62, s[74:75] offset:2112\n"
    "v_fmaak_f32 v58, v58, v127, 0xc0135761\n"
    "v_mfma_f32_16x16x32_bf16 v[232:235], v[24:27], a[124:127], v[232:235]\n"
    "v_rcp_f32 v54, v54\n"
    "v_mfma_f32_16x16x32_bf16 v[236:239], v[24:27], a[180:183], v[236:239]\n"
    "s_waitcnt lgkmcnt(4)\n"
    "ds_read_b64_tr_b16 v[24:25], v6 offset:64\n"
    "ds_read_b64_tr_b16 v[26:27], v6 offset:320\n"
    "v_fmaak_f32 v59, v59, v127, 0xc0135761\n"
    "v_mfma_f32_16x16x32_bf16 v[240:243], v[28:31], a[12:15], v[240:243]\n"
    "v_rcp_f32 v55, v55\n"
    "v_mfma_f32_16x16x32_bf16 v[244:247], v[28:31], a[68:71], v[244:247]\n"
    "global_load_lds_dwordx4 v63, s[74:75] offset:3168\n"
    "v_mul_f32 v80, v80, v52\n"
    "v_mfma_f32_16x16x32_bf16 v[248:251], v[28:31], a[124:127], v[248:251]\n"
    "v_mul_f32 v81, v81, v53\n"
    "v_mul_f32 v82, v82, v54\n"
    "v_mfma_f32_16x16x32_bf16 v[252:255], v[28:31], a[180:183], v[252:255]\n"
    "s_add_u32 s74, s74, 0x40000\n"
    "s_addc_u32 s75, s75, 0\n"
    "s_waitcnt lgkmcnt(4)\n"
    "ds_read_b64_tr_b16 v[28:29], v6 offset:96\n"
    "ds_read_b64_tr_b16 v[30:31], v6 offset:352\n"
    "v_mfma_f32_16x16x32_bf16 v[128:131], v[16:19], a[16:19], v[128:131]\n"
    "s_add_u32 m0, s78, 0x8400\n"
    "v_mul_f32 v83, v83, v55\n"
    "v_cvt_pk_bf16_f32 v80, v80, v81\n"
    "v_cvt_pk_bf16_f32 v81, v82, v83\n"
    "v_mfma_f32_16x16x32_bf16 v[132:135], v[16:19], a[72:75], v[132:135]\n"
    "global_load_lds_dwordx4 v60, s[72:73]\n"
    "v_mul_f32 v56, v72, v56\n"
    "v_mfma_f32_16x16x32_bf16 v[136:139], v[16:19], a[128:131], v[136:139]\n"
    "ds_write_b64 v121, v[80:81]\n"
    "v_mfma_f32_16x16x32_bf16 v[140:143], v[16:19], a[184:187], v[140:143]\n"
    "s_waitcnt lgkmcnt(5)\n"
    "ds_read_b64_tr_b16 v[16:17], v6 offset:128\n"
    "ds_read_b64_tr_b16 v[18:19], v6 offset:384\n"
    "v_mfma_f32_16x16x32_bf16 v[144:147], v[20:23], a[16:19], v[144:147]\n"
    "v_fma_f32 v92, v92, v15, v36\n"
    "v_fma_f32 v93, v93, v15, v37\n"
    "v_mfma_f32_16x16x32_bf16 v[148:151], v[20:23], a[72:75], v[148:151]\n"
    "global_load_lds_dwordx4 v61, s[72:73] offset:1056\n"
    "v_fma_f32 v94, v94, v15, v38\n"
    "v_fma_f32 v95, v95, v15, v39\n"
    "v_mfma_f32_16x16x32_bf16 v[152:155], v[20:23], a[128:131], v[152:155]\n"
    "v_mul_f32 v52, v92, v92\n"
    "v_mul_f32 v53, v93, v93\n"
    "v_mfma_f32_16x16x32_bf16 v[156:159], v[20:23], a[184:187], v[156:159]\n"
    "s_waitcnt lgkmcnt(5)\n"
    "ds_read_b64_tr_b16 v[20:21], v6 offset:160\n"
    "ds_read_b64_tr_b16 v[22:23], v6 offset:416\n"
    "v_mfma_f32_16x16x32_bf16 v[160:163], v[24:27], a[16:19], v[160:163]\n"
    "v_mul_f32 v54, v94, v94\n"
    "v_mul_f32 v55, v95, v95\n"
    "v_mfma_f32_16x16x32_bf16 v[164:167], v[24:27], a[72:75], v[164:167]\n"
    "global_load_lds_dwordx4 v62, s[72:73] offset:2112\n"
    "v_fmaak_f32 v52, v52, v127, 0xc0135761\n"
    "v_mfma_f32_16x16x32_bf16 v[168:171], v[24:27], a[128:131], v[168:171]\n"
    "v_fmaak_f32 v53, v53, v127, 0xc0135761\n"
    "v_fmaak_f32 v54, v54, v127, 0xc0135761\n"
    "v_fmaak_f32 v55, v55, v127, 0xc0135761\n"
    "v_mfma_f32_16x16x32_bf16 v[172:175], v[24:27], a[184:187], v[172:175]\n"
    "s_waitcnt lgkmcnt(5)\n"
    "ds_read_b64_tr_b16 v[24:25], v6 offset:192\n"
    "ds_read_b64_tr_b16 v[26:27], v6 offset:448\n"
    "v_mfma_f32_16x16x32_bf16 v[176:179], v[28:31], a[16:19], v[176:179]\n"
    "v_mul_f32 v52, v92, v52\n"
    "v_mul_f32 v53, v93, v53\n"
    "v_mfma_f32_16x16x32_bf16 v[180:183], v[28:31], a[72:75], v[180:183]\n"
    "global_load_lds_dwordx4 v63, s[72:73] offset:3168\n"
    "v_mul_f32 v54, v94, v54\n"
    "v_mfma_f32_16x16x32_bf16 v[184:187], v[28:31], a[128:131], v[184:187]\n"
    "v_mul_f32 v55, v95, v55\n"
    "v_mul_f32 v57, v73, v57\n"
    "v_mfma_f32_16x16x32_bf16 v[188:191], v[28:31], a[184:187], v[188:191]\n"
    "s_add_u32 s72, s72, 0x40000\n"
    "s_addc_u32 s73, s73, 0\n"
    "s_waitcnt lgkmcnt(4)\n"
    "ds_read_b64_tr_b16 v[28:29], v6 offset:224\n"
    "ds_read_b64_tr_b16 v[30:31], v6 offset:480\n"
    "v_mfma_f32_16x16x32_bf16 v[192:195], v[16:19], a[16:19], v[192:195]\n"
    "s_add_u32 m0, s78, 0x9480\n"
    "v_exp_f32 v52, v52\n"
    "v_mfma_f32_16x16x32_bf16 v[196:199], v[16:19], a[72:75], v[196:199]\n"
    "global_load_lds_dwordx4 v60, s[74:75]\n"
    "v_mul_f32 v58, v74, v58\n"
    "v_mfma_f32_16x16x32_bf16 v[200:203], v[16:19], a[128:131], v[200:203]\n"
    "v_exp_f32 v53, v53\n"
    "v_mfma_f32_16x16x32_bf16 v[204:207], v[16:19], a[184:187], v[204:207]\n"
    "s_waitcnt lgkmcnt(4)\n"
    "s_waitcnt vmcnt(13)\n"
    "ds_read_b64_tr_b16 v[16:17], v6 offset:33792\n"
    "ds_read_b64_tr_b16 v[18:19], v6 offset:34048\n"
    "v_mfma_f32_16x16x32_bf16 v[208:211], v[20:23], a[16:19], v[208:211]\n"
    "v_exp_f32 v54, v54\n"
    "v_mul_f32 v59, v75, v59\n"
    "v_mfma_f32_16x16x32_bf16 v[212:215], v[20:23], a[72:75], v[212:215]\n"
    "global_load_lds_dwordx4 v61, s[74:75] offset:1056\n"
    "v_mfma_f32_16x16x32_bf16 v[216:219], v[20:23], a[128:131], v[216:219]\n"
    "v_exp_f32 v55, v55\n"
    "v_mfma_f32_16x16x32_bf16 v[220:223], v[20:23], a[184:187], v[220:223]\n"
    "s_waitcnt lgkmcnt(4)\n"
    "ds_read_b64_tr_b16 v[20:21], v6 offset:33824\n"
    "ds_read_b64_tr_b16 v[22:23], v6 offset:34080\n"
    "v_mfma_f32_16x16x32_bf16 v[224:227], v[24:27], a[16:19], v[224:227]\n"
    "v_exp_f32 v56, v56\n"
    "v_mfma_f32_16x16x32_bf16 v[228:231], v[24:27], a[72:75], v[228:231]\n"
    "global_load_lds_dwordx4 v62, s[74:75] offset:2112\n"
    "v_add_f32 v52, 1.0, v52\n"
    "v_mfma_f32_16x16x32_bf16 v[232:235], v[24:27], a[128:131], v[232:235]\n"
    "v_exp_f32 v57, v57\n"
    "v_add_f32 v53, 1.0, v53\n"
    "v_mfma_f32_16x16x32_bf16 v[236:239], v[24:27], a[184:187], v[236:239]\n"
    "s_waitcnt lgkmcnt(4)\n"
    "ds_read_b64_tr_b16 v[24:25], v6 offset:33856\n"
    "ds_read_b64_tr_b16 v[26:27], v6 offset:34112\n"
    "v_mfma_f32_16x16x32_bf16 v[240:243], v[28:31], a[16:19], v[240:243]\n"
    "v_exp_f32 v58, v58\n"
    "v_mfma_f32_16x16x32_bf16 v[244:247], v[28:31], a[72:75], v[244:247]\n"
    "global_load_lds_dwordx4 v63, s[74:75] offset:3168\n"
    "v_add_f32 v54, 1.0, v54\n"
    "v_mfma_f32_16x16x32_bf16 v[248:251], v[28:31], a[128:131], v[248:251]\n"
    "v_exp_f32 v59, v59\n"
    "v_mfma_f32_16x16x32_bf16 v[252:255], v[28:31], a[184:187], v[252:255]\n"
    "s_add_u32 s74, s74, 0x40000\n"
    "s_addc_u32 s75, s75, 0\n"
    "s_waitcnt lgkmcnt(4)\n"
    "ds_read_b64_tr_b16 v[28:29], v6 offset:33888\n"
    "ds_read_b64_tr_b16 v[30:31], v6 offset:34144\n"
    "v_mfma_f32_16x16x32_bf16 v[128:131], v[16:19], a[20:23], v[128:131]\n"
    "s_add_u32 m0, s78, 0x10800\n"
    "v_add_f32 v55, 1.0, v55\n"
    "v_add_f32 v56, 1.0, v56\n"
    "v_mfma_f32_16x16x32_bf16 v[132:135], v[16:19], a[76:79], v[132:135]\n"
    "global_load_lds_dwordx4 v60, s[72:73]\n"
    "v_add_f32 v57, 1.0, v57\n"
    "v_mfma_f32_16x16x32_bf16 v[136:139], v[16:19], a[132:135], v[136:139]\n"
    "v_rcp_f32 v52, v52\n"
    "v_mfma_f32_16x16x32_bf16 v[140:143], v[16:19], a[188:191], v[140:143]\n"
    "s_waitcnt lgkmcnt(4)\n"
    "ds_read_b64_tr_b16 v[16:17], v6 offset:33920\n"
    "ds_read_b64_tr_b16 v[18:19], v6 offset:34176\n"
    "v_mfma_f32_16x16x32_bf16 v[144:147], v[20:23], a[20:23], v[144:147]\n"
    "v_rcp_f32 v53, v53\n"
    "v_add_f32 v58, 1.0, v58\n"
    "v_mfma_f32_16x16x32_bf16 v[148:151], v[20:23], a[76:79], v[148:151]\n"
    "global_load_lds_dwordx4 v61, s[72:73] offset:1056\n"
    "v_add_f32 v59, 1.0, v59\n"
    "v_mfma_f32_16x16x32_bf16 v[152:155], v[20:23], a[132:135], v[152:155]\n"
    "v_rcp_f32 v54, v54\n"
    "v_mfma_f32_16x16x32_bf16 v[156:159], v[20:23], a[188:191], v[156:159]\n"
    "s_waitcnt lgkmcnt(4)\n"
    "ds_read_b64_tr_b16 v[20:21], v6 offset:33952\n"
    "ds_read_b64_tr_b16 v[22:23], v6 offset:34208\n"
    "v_mfma_f32_16x16x32_bf16 v[160:163], v[24:27], a[20:23], v[160:163]\n"
    "v_rcp_f32 v55, v55\n"
    "v_mfma_f32_16x16x32_bf16 v[164:167], v[24:27], a[76:79], v[164:167]\n"
    "global_load_lds_dwordx4 v62, s[72:73] offset:2112\n"
    "v_rcp_f32 v56, v56\n"
    "v_mfma_f32_16x16x32_bf16 v[168:171], v[24:27], a[132:135], v[168:171]\n"
    "v_rcp_f32 v57, v57\n"
    "v_mfma_f32_16x16x32_bf16 v[172:175], v[24:27], a[188:191], v[172:175]\n"
    "s_waitcnt lgkmcnt(4)\n"
    "ds_read_b64_tr_b16 v[24:25], v6 offset:33984\n"
    "ds_read_b64_tr_b16 v[26:27], v6 offset:34240\n"
    "v_mfma_f32_16x16x32_bf16 v[176:179], v[28:31], a[20:23], v[176:179]\n"
    "v_rcp_f32 v58, v58\n"
    "v_mfma_f32_16x16x32_bf16 v[180:183], v[28:31], a[76:79], v[180:183]\n"
    "global_load_lds_dwordx4 v63, s[72:73] offset:3168\n"
    "v_mul_f32 v92, v92, v52\n"
    "v_mfma_f32_16x16x32_bf16 v[184:187], v[28:31], a[132:135], v[184:187]\n"
    "v_rcp_f32 v59, v59\n"
    "v_mul_f32 v93, v93, v53\n"
    "v_mfma_f32_16x16x32_bf16 v[188:191], v[28:31], a[188:191], v[188:191]\n"
    "s_add_u32 s72, s72, 0x40000\n"
    "s_addc_u32 s73, s73, 0\n"
    "s_waitcnt lgkmcnt(4)\n"
    "ds_read_b64_tr_b16 v[28:29], v6 offset:34016\n"
    "ds_read_b64_tr_b16 v[30:31], v6 offset:34272\n"
    "v_mfma_f32_16x16x32_bf16 v[192:195], v[16:19], a[20:23], v[192:195]\n"
    "s_add_u32 m0, s78, 0x11880\n"
    "v_mul_f32 v94, v94, v54\n"
    "v_mul_f32 v95, v95, v55\n"
    "v_mfma_f32_16x16x32_bf16 v[196:199], v[16:19], a[76:79], v[196:199]\n"
    "global_load_lds_dwordx4 v60, s[74:75]\n"
    "v_cvt_pk_bf16_f32 v92, v92, v93\n"
    "v_mfma_f32_16x16x32_bf16 v[200:203], v[16:19], a[132:135], v[200:203]\n"
    "v_cvt_pk_bf16_f32 v93, v94, v95\n"
    "v_mul_f32 v72, v72, v56\n"
    "v_mfma_f32_16x16x32_bf16 v[204:207], v[16:19], a[188:191], v[204:207]\n"
    "s_waitcnt lgkmcnt(4)\n"
    "s_waitcnt vmcnt(13)\n"
    "ds_read_b64_tr_b16 v[16:17], v5\n"
    "ds_read_b64_tr_b16 v[18:19], v5 offset:256\n"
    "v_mul_f32 v73, v73, v57\n"
    "v_mfma_f32_16x16x32_bf16 v[208:211], v[20:23], a[20:23], v[208:211]\n"
    "v_mul_f32 v74, v74, v58\n"
    "v_mul_f32 v75, v75, v59\n"
    "v_mfma_f32_16x16x32_bf16 v[212:215], v[20:23], a[76:79], v[212:215]\n"
    "global_load_lds_dwordx4 v61, s[74:75] offset:1056\n"
    "v_cvt_pk_bf16_f32 v72, v72, v73\n"
    "v_mfma_f32_16x16x32_bf16 v[216:219], v[20:23], a[132:135], v[216:219]\n"
    "v_cvt_pk_bf16_f32 v73, v74, v75\n"
    "v_xor_b32 v120, 0x800, v120\n"
    "v_mfma_f32_16x16x32_bf16 v[220:223], v[20:23], a[188:191], v[220:223]\n"
    "s_waitcnt lgkmcnt(4)\n"
    "ds_read_b64_tr_b16 v[20:21], v5 offset:32\n"
    "ds_read_b64_tr_b16 v[22:23], v5 offset:288\n"
    "v_mfma_f32_16x16x32_bf16 v[224:227], v[24:27], a[20:23], v[224:227]\n"
    "ds_write_b64 v120, v[76:77] offset:1024\n"
    "v_mfma_f32_16x16x32_bf16 v[228:231], v[24:27], a[76:79], v[228:231]\n"
    "global_load_lds_dwordx4 v62, s[74:75] offset:2112\n"
    "v_fma_f32 v88, v88, v14, v36\n"
    "v_mfma_f32_16x16x32_bf16 v[232:235], v[24:27], a[132:135], v[232:235]\n"
    "ds_write_b64 v120, v[72:73]\n"
    "v_mfma_f32_16x16x32_bf16 v[236:239], v[24:27], a[188:191], v[236:239]\n"
    "s_waitcnt lgkmcnt(6)\n"
    "ds_read_b64_tr_b16 v[24:25], v5 offset:64\n"
    "ds_read_b64_tr_b16 v[26:27], v5 offset:320\n"
    "v_mfma_f32_16x16x32_bf16 v[240:243], v[28:31], a[20:23], v[240:243]\n"
    "v_fma_f32 v89, v89, v14, v37\n"
    "v_fma_f32 v90, v90, v14, v38\n"
    "v_mfma_f32_16x16x32_bf16 v[244:247], v[28:31], a[76:79], v[244:247]\n"
    "global_load_lds_dwordx4 v63, s[74:75] offset:3168\n"
    "v_fma_f32 v91, v91, v14, v39\n"
    "v_mul_f32 v48, v88, v88\n"
    "v_mfma_f32_16x16x32_bf16 v[248:251], v[28:31], a[132:135], v[248:251]\n"
    "v_mul_f32 v49, v89, v89\n"
    "v_mul_f32 v50, v90, v90\n"
    "v_mfma_f32_16x16x32_bf16 v[252:255], v[28:31], a[188:191], v[252:255]\n"
    "s_add_u32 s74, s74, 0x40000\n"
    "s_addc_u32 s75, s75, 0\n"
    "s_waitcnt lgkmcnt(6)\n"
    "ds_read_b64_tr_b16 v[28:29], v5 offset:96\n"
    "ds_read_b64_tr_b16 v[30:31], v5 offset:352\n"
    "v_mfma_f32_16x16x32_bf16 v[128:131], v[16:19], a[24:27], v[128:131]\n"
    "s_add_u32 m0, s78, 0x18c00\n"
    "v_mul_f32 v51, v91, v91\n"
    "v_fmaak_f32 v48, v48, v127, 0xc0135761\n"
    "v_mfma_f32_16x16x32_bf16 v[132:135], v[16:19], a[80:83], v[132:135]\n"
    "global_load_lds_dwordx4 v60, s[72:73]\n"
    "v_fmaak_f32 v49, v49, v127, 0xc0135761\n"
    "v_mfma_f32_16x16x32_bf16 v[136:139], v[16:19], a[136:139], v[136:139]\n"
    "v_fmaak_f32 v50, v50, v127, 0xc0135761\n"
    "v_fmaak_f32 v51, v51, v127, 0xc0135761\n"
    "v_mul_f32 v48, v88, v48\n"
    "v_mfma_f32_16x16x32_bf16 v[140:143], v[16:19], a[192:195], v[140:143]\n"
    "s_waitcnt lgkmcnt(6)\n"
    "ds_read_b64_tr_b16 v[16:17], v5 offset:128\n"
    "ds_read_b64_tr_b16 v[18:19], v5 offset:384\n"
    "v_mfma_f32_16x16x32_bf16 v[144:147], v[20:23], a[24:27], v[144:147]\n"
    "v_mul_f32 v49, v89, v49\n"
    "v_mul_f32 v50, v90, v50\n"
    "v_mfma_f32_16x16x32_bf16 v[148:151], v[20:23], a[80:83], v[148:151]\n"
    "global_load_lds_dwordx4 v61, s[72:73] offset:1056\n"
    "v_mul_f32 v51, v91, v51\n"
    "v_mfma_f32_16x16x32_bf16 v[152:155], v[20:23], a[136:139], v[152:155]\n"
    "v_exp_f32 v48, v48\n"
    "v_mfma_f32_16x16x32_bf16 v[156:159], v[20:23], a[192:195], v[156:159]\n"
    "s_waitcnt lgkmcnt(4)\n"
    "ds_read_b64_tr_b16 v[20:21], v5 offset:160\n"
    "ds_read_b64_tr_b16 v[22:23], v5 offset:416\n"
    "v_fma_f32 v84, v84, v13, v36\n"
    "v_mfma_f32_16x16x32_bf16 v[160:163], v[24:27], a[24:27], v[160:163]\n"
    "v_exp_f32 v49, v49\n"
    "v_mfma_f32_16x16x32_bf16 v[164:167], v[24:27], a[80:83], v[164:167]\n"
    "global_load_lds_dwordx4 v62, s[72:73] offset:2112\n"
    "v_fma_f32 v85, v85, v13, v37\n"
    "v_mfma_f32_16x16x32_bf16 v[168:171], v[24:27], a[136:139], v[168:171]\n"
    "v_exp_f32 v50, v50\n"
    "v_mfma_f32_16x16x32_bf16 v[172:175], v[24:27], a[192:195], v[172:175]\n"
    "s_waitcnt lgkmcnt(4)\n"
    "ds_read_b64_tr_b16 v[24:25], v5 offset:192\n"
    "ds_read_b64_tr_b16 v[26:27], v5 offset:448\n"
    "v_mfma_f32_16x16x32_bf16 v[176:179], v[28:31], a[24:27], v[176:179]\n"
    "v_exp_f32 v51, v51\n"
    "v_add_f32 v48, 1.0, v48\n"
    "v_mfma_f32_16x16x32_bf16 v[180:183], v[28:31], a[80:83], v[180:183]\n"
    "global_load_lds_dwordx4 v63, s[72:73] offset:3168\n"
    "v_add_f32 v49, 1.0, v49\n"
    "v_mfma_f32_16x16x32_bf16 v[184:187], v[28:31], a[136:139], v[184:187]\n"
    "v_add_f32 v50, 1.0, v50\n"
    "v_add_f32 v51, 1.0, v51\n"
    "v_mfma_f32_16x16x32_bf16 v[188:191], v[28:31], a[192:195], v[188:191]\n"
    "s_add_u32 s72, s72, 0x40000\n"
    "s_addc_u32 s73, s73, 0\n"
    "s_waitcnt lgkmcnt(4)\n"
    "ds_read_b64_tr_b16 v[28:29], v5 offset:224\n"
    "ds_read_b64_tr_b16 v[30:31], v5 offset:480\n"
    "v_mfma_f32_16x16x32_bf16 v[192:195], v[16:19], a[24:27], v[192:195]\n"
    "s_add_u32 m0, s78, 0x19c80\n"
    "v_rcp_f32 v48, v48\n"
    "v_mfma_f32_16x16x32_bf16 v[196:199], v[16:19], a[80:83], v[196:199]\n"
    "global_load_lds_dwordx4 v60, s[74:75]\n"
    "v_rcp_f32 v49, v49\n"
    "v_mfma_f32_16x16x32_bf16 v[200:203], v[16:19], a[136:139], v[200:203]\n"
    "v_rcp_f32 v50, v50\n"
    "v_mfma_f32_16x16x32_bf16 v[204:207], v[16:19], a[192:195], v[204:207]\n"
    "s_waitcnt lgkmcnt(4)\n"
    "s_waitcnt vmcnt(13)\n"
    "ds_read_b64_tr_b16 v[16:17], v5 offset:33792\n"
    "ds_read_b64_tr_b16 v[18:19], v5 offset:34048\n"
    "v_mfma_f32_16x16x32_bf16 v[208:211], v[20:23], a[24:27], v[208:211]\n"
    "v_rcp_f32 v51, v51\n"
    "v_mfma_f32_16x16x32_bf16 v[212:215], v[20:23], a[80:83], v[212:215]\n"
    "global_load_lds_dwordx4 v61, s[74:75] offset:1056\n"
    "v_mul_f32 v88, v88, v48\n"
    "v_mfma_f32_16x16x32_bf16 v[216:219], v[20:23], a[136:139], v[216:219]\n"
    "v_mul_f32 v89, v89, v49\n"
    "v_mul_f32 v90, v90, v50\n"
    "v_mul_f32 v91, v91, v51\n"
    "v_mfma_f32_16x16x32_bf16 v[220:223], v[20:23], a[192:195], v[220:223]\n"
    "s_waitcnt lgkmcnt(4)\n"
    "ds_read_b64_tr_b16 v[20:21], v5 offset:33824\n"
    "ds_read_b64_tr_b16 v[22:23], v5 offset:34080\n"
    "v_mfma_f32_16x16x32_bf16 v[224:227], v[24:27], a[24:27], v[224:227]\n"
    "v_cvt_pk_bf16_f32 v88, v88, v89\n"
    "v_cvt_pk_bf16_f32 v89, v90, v91\n"
    "v_mfma_f32_16x16x32_bf16 v[228:231], v[24:27], a[80:83], v[228:231]\n"
    "global_load_lds_dwordx4 v62, s[74:75] offset:2112\n"
    "v_fma_f32 v86, v86, v13, v38\n"
    "v_mfma_f32_16x16x32_bf16 v[232:235], v[24:27], a[136:139], v[232:235]\n"
    "v_fma_f32 v87, v87, v13, v39\n"
    "v_mul_f32 v56, v84, v84\n"
    "v_mfma_f32_16x16x32_bf16 v[236:239], v[24:27], a[192:195], v[236:239]\n"
    "s_waitcnt lgkmcnt(4)\n"
    "ds_read_b64_tr_b16 v[24:25], v5 offset:33856\n"
    "ds_read_b64_tr_b16 v[26:27], v5 offset:34112\n"
    "v_mul_f32 v57, v85, v85\n"
    "v_mfma_f32_16x16x32_bf16 v[240:243], v[28:31], a[24:27], v[240:243]\n"
    "v_mul_f32 v58, v86, v86\n"
    "v_mul_f32 v59, v87, v87\n"
    "v_mfma_f32_16x16x32_bf16 v[244:247], v[28:31], a[80:83], v[244:247]\n"
    "global_load_lds_dwordx4 v63, s[74:75] offset:3168\n"
    "v_fmaak_f32 v56, v56, v127, 0xc0135761\n"
    "v_mfma_f32_16x16x32_bf16 v[248:251], v[28:31], a[136:139], v[248:251]\n"
    "v_fmaak_f32 v57, v57, v127, 0xc0135761\n"
    "v_fmaak_f32 v58, v58, v127, 0xc0135761\n"
    "v_mfma_f32_16x16x32_bf16 v[252:255], v[28:31], a[192:195], v[252:255]\n"
    "s_add_u32 s74, s74, 0x40000\n"
    "s_addc_u32 s75, s75, 0\n"
    "s_waitcnt lgkmcnt(4)\n"
    "ds_read_b64_tr_b16 v[28:29], v5 offset:33888\n"
    "ds_read_b64_tr_b16 v[30:31], v5 offset:34144\n"
    "v_mfma_f32_16x16x32_bf16 v[128:131], v[16:19], a[28:31], v[128:131]\n"
    "s_add_u32 m0, s78, 0\n"
    "v_fmaak_f32 v59, v59, v127, 0xc0135761\n"
    "v_mul_f32 v56, v84, v56\n"
    "v_mfma_f32_16x16x32_bf16 v[132:135], v[16:19], a[84:87], v[132:135]\n"
    "global_load_lds_dwordx4 v60, s[72:73]\n"
    "v_mul_f32 v57, v85, v57\n"
    "v_mul_f32 v58, v86, v58\n"
    "v_mfma_f32_16x16x32_bf16 v[136:139], v[16:19], a[140:143], v[136:139]\n"
    "v_mul_f32 v59, v87, v59\n"
    "v_mfma_f32_16x16x32_bf16 v[140:143], v[16:19], a[196:199], v[140:143]\n"
    "s_waitcnt lgkmcnt(4)\n"
    "ds_read_b64_tr_b16 v[16:17], v5 offset:33920\n"
    "ds_read_b64_tr_b16 v[18:19], v5 offset:34176\n"
    "v_mfma_f32_16x16x32_bf16 v[144:147], v[20:23], a[28:31], v[144:147]\n"
    "v_exp_f32 v56, v56\n"
    "v_mfma_f32_16x16x32_bf16 v[148:151], v[20:23], a[84:87], v[148:151]\n"
    "global_load_lds_dwordx4 v61, s[72:73] offset:1056\n"
    "v_mfma_f32_16x16x32_bf16 v[152:155], v[20:23], a[140:143], v[152:155]\n"
    "v_exp_f32 v57, v57\n"
    "v_mfma_f32_16x16x32_bf16 v[156:159], v[20:23], a[196:199], v[156:159]\n"
    "s_waitcnt lgkmcnt(4)\n"
    "ds_read_b64_tr_b16 v[20:21], v5 offset:33952\n"
    "ds_read_b64_tr_b16 v[22:23], v5 offset:34208\n"
    "v_mfma_f32_16x16x32_bf16 v[160:163], v[24:27], a[28:31], v[160:163]\n"
    "v_exp_f32 v58, v58\n"
    "v_mfma_f32_16x16x32_bf16 v[164:167], v[24:27], a[84:87], v[164:167]\n"
    "global_load_lds_dwordx4 v62, s[72:73] offset:2112\n"
    "v_mfma_f32_16x16x32_bf16 v[168:171], v[24:27], a[140:143], v[168:171]\n"
    "v_exp_f32 v59, v59\n"
    "v_mfma_f32_16x16x32_bf16 v[172:175], v[24:27], a[196:199], v[172:175]\n"
    "s_waitcnt lgkmcnt(4)\n"
    "ds_read_b64_tr_b16 v[24:25], v5 offset:33984\n"
    "ds_read_b64_tr_b16 v[26:27], v5 offset:34240\n"
    "v_add_f32 v56, 1.0, v56\n"
    "v_mfma_f32_16x16x32_bf16 v[176:179], v[28:31], a[28:31], v[176:179]\n"
    "v_add_f32 v57, 1.0, v57\n"
    "v_add_f32 v58, 1.0, v58\n"
    "v_mfma_f32_16x16x32_bf16 v[180:183], v[28:31], a[84:87], v[180:183]\n"
    "global_load_lds_dwordx4 v63, s[72:73] offset:3168\n"
    "v_add_f32 v59, 1.0, v59\n"
    "v_mfma_f32_16x16x32_bf16 v[184:187], v[28:31], a[140:143], v[184:187]\n"
    "v_rcp_f32 v56, v56\n"
    "v_mfma_f32_16x16x32_bf16 v[188:191], v[28:31], a[196:199], v[188:191]\n"
    "s_add_u32 s72, s72, 0x40000\n"
    "s_addc_u32 s73, s73, 0\n"
    "s_waitcnt lgkmcnt(4)\n"
    "ds_read_b64_tr_b16 v[28:29], v5 offset:34016\n"
    "ds_read_b64_tr_b16 v[30:31], v5 offset:34272\n"
    "v_mfma_f32_16x16x32_bf16 v[192:195], v[16:19], a[28:31], v[192:195]\n"
    "s_add_u32 m0, s78, 0x1080\n"
    "v_rcp_f32 v57, v57\n"
    "v_mfma_f32_16x16x32_bf16 v[196:199], v[16:19], a[84:87], v[196:199]\n"
    "global_load_lds_dwordx4 v60, s[74:75]\n"
    "v_mfma_f32_16x16x32_bf16 v[200:203], v[16:19], a[140:143], v[200:203]\n"
    "v_rcp_f32 v58, v58\n"
    "v_mfma_f32_16x16x32_bf16 v[204:207], v[16:19], a[196:199], v[204:207]\n"
    "s_waitcnt lgkmcnt(4)\n"
    "s_waitcnt vmcnt(13)\n"
    "ds_read_b64_tr_b16 v[16:17], v6\n"
    "ds_read_b64_tr_b16 v[18:19], v6 offset:256\n"
    "v_mfma_f32_16x16x32_bf16 v[208:211], v[20:23], a[28:31], v[208:211]\n"
    "v_rcp_f32 v59, v59\n"
    "v_mfma_f32_16x16x32_bf16 v[212:215], v[20:23], a[84:87], v[212:215]\n"
    "global_load_lds_dwordx4 v61, s[74:75] offset:1056\n"
    "v_mul_f32 v84, v84, v56\n"
    "v_mul_f32 v85, v85, v57\n"
    "v_mfma_f32_16x16x32_bf16 v[216:219], v[20:23], a[140:143], v[216:219]\n"
    "v_mul_f32 v86, v86, v58\n"
    "v_mul_f32 v87, v87, v59\n"
    "v_mfma_f32_16x16x32_bf16 v[220:223], v[20:23], a[196:199], v[220:223]\n"
    "s_waitcnt lgkmcnt(4)\n"
    "ds_read_b64_tr_b16 v[20:21], v6 offset:32\n"
    "ds_read_b64_tr_b16 v[22:23], v6 offset:288\n"
    "v_mfma_f32_16x16x32_bf16 v[224:227], v[24:27], a[28:31], v[224:227]\n"
    "v_cvt_pk_bf16_f32 v84, v84, v85\n"
    "v_cvt_pk_bf16_f32 v85, v86, v87\n"
    "v_mfma_f32_16x16x32_bf16 v[228:231], v[24:27], a[84:87], v[228:231]\n"
    "global_load_lds_dwordx4 v62, s[74:75] offset:2112\n"
    "v_mfma_f32_16x16x32_bf16 v[232:235], v[24:27], a[140:143], v[232:235]\n"
    "ds_write_b64 v121, v[84:85] offset:1024\n"
    "v_mfma_f32_16x16x32_bf16 v[236:239], v[24:27], a[196:199], v[236:239]\n"
    "s_waitcnt lgkmcnt(5)\n"
    "ds_read_b64_tr_b16 v[24:25], v6 offset:64\n"
    "ds_read_b64_tr_b16 v[26:27], v6 offset:320\n"
    "v_mfma_f32_16x16x32_bf16 v[240:243], v[28:31], a[28:31], v[240:243]\n"
    "v_xor_b32 v121, 0x800, v121\n"
    "v_fma_f32 v96, v96, v12, v40\n"
    "v_mfma_f32_16x16x32_bf16 v[244:247], v[28:31], a[84:87], v[244:247]\n"
    "global_load_lds_dwordx4 v63, s[74:75] offset:3168\n"
    "v_fma_f32 v97, v97, v12, v41\n"
    "v_mfma_f32_16x16x32_bf16 v[248:251], v[28:31], a[140:143], v[248:251]\n"
    "ds_write_b64 v121, v[88:89]\n"
    "v_mfma_f32_16x16x32_bf16 v[252:255], v[28:31], a[196:199], v[252:255]\n"
    "s_add_u32 s74, s74, 0x40000\n"
    "s_addc_u32 s75, s75, 0\n"
    "s_waitcnt lgkmcnt(6)\n"
    "ds_read_b64_tr_b16 v[28:29], v6 offset:96\n"
    "ds_read_b64_tr_b16 v[30:31], v6 offset:352\n"
    "v_mfma_f32_16x16x32_bf16 v[128:131], v[16:19], a[32:35], v[128:131]\n"
    "s_add_u32 m0, s78, 0x8400\n"
    "ds_write_b64 v121, v[92:93] offset:1024\n"
    "v_mfma_f32_16x16x32_bf16 v[132:135], v[16:19], a[88:91], v[132:135]\n"
    "global_load_lds_dwordx4 v60, s[72:73]\n"
    "v_accvgpr_read_b32 v32, a224\n"
    "v_mfma_f32_16x16x32_bf16 v[136:139], v[16:19], a[144:147], v[136:139]\n"
    "v_accvgpr_read_b32 v33, a225\n"
    "v_accvgpr_read_b32 v34, a226\n"
    "v_mfma_f32_16x16x32_bf16 v[140:143], v[16:19], a[200:203], v[140:143]\n"
    "s_waitcnt lgkmcnt(7)\n"
    "ds_read_b64_tr_b16 v[16:17], v6 offset:128\n"
    "ds_read_b64_tr_b16 v[18:19], v6 offset:384\n"
    "v_mfma_f32_16x16x32_bf16 v[144:147], v[20:23], a[32:35], v[144:147]\n"
    "v_accvgpr_read_b32 v35, a227\n"
    "v_accvgpr_read_b32 v36, a228\n"
    "v_accvgpr_read_b32 v37, a229\n"
    "v_mfma_f32_16x16x32_bf16 v[148:151], v[20:23], a[88:91], v[148:151]\n"
    "global_load_lds_dwordx4 v61, s[72:73] offset:1056\n"
    "v_accvgpr_read_b32 v38, a230\n"
    "v_mfma_f32_16x16x32_bf16 v[152:155], v[20:23], a[144:147], v[152:155]\n"
    "v_fma_f32 v100, v100, v13, v40\n"
    "v_fma_f32 v101, v101, v13, v41\n"
    "v_mfma_f32_16x16x32_bf16 v[156:159], v[20:23], a[200:203], v[156:159]\n"
    "s_waitcnt lgkmcnt(6)\n"
    "ds_read_b64_tr_b16 v[20:21], v6 offset:160\n"
    "ds_read_b64_tr_b16 v[22:23], v6 offset:416\n"
    "v_mfma_f32_16x16x32_bf16 v[160:163], v[24:27], a[32:35], v[160:163]\n"
    "v_fma_f32 v102, v102, v13, v42\n"
    "v_fma_f32 v103, v103, v13, v43\n"
    "v_mfma_f32_16x16x32_bf16 v[164:167], v[24:27], a[88:91], v[164:167]\n"
    "global_load_lds_dwordx4 v62, s[72:73] offset:2112\n"
    "v_mul_f32 v48, v100, v100\n"
    "v_mul_f32 v49, v101, v101\n"
    "v_mfma_f32_16x16x32_bf16 v[168:171], v[24:27], a[144:147], v[168:171]\n"
    "v_mul_f32 v50, v102, v102\n"
    "v_mul_f32 v51, v103, v103\n"
    "v_mfma_f32_16x16x32_bf16 v[172:175], v[24:27], a[200:203], v[172:175]\n"
    "s_waitcnt lgkmcnt(5)\n"
    "ds_read_b64_tr_b16 v[24:25], v6 offset:192\n"
    "ds_read_b64_tr_b16 v[26:27], v6 offset:448\n"
    "v_mfma_f32_16x16x32_bf16 v[176:179], v[28:31], a[32:35], v[176:179]\n"
    "v_fmaak_f32 v48, v48, v127, 0xc0135761\n"
    "v_fmaak_f32 v49, v49, v127, 0xc0135761\n"
    "v_mfma_f32_16x16x32_bf16 v[180:183], v[28:31], a[88:91], v[180:183]\n"
    "global_load_lds_dwordx4 v63, s[72:73] offset:3168\n"
    "v_fmaak_f32 v50, v50, v127, 0xc0135761\n"
    "v_mfma_f32_16x16x32_bf16 v[184:187], v[28:31], a[144:147], v[184:187]\n"
    "v_fmaak_f32 v51, v51, v127, 0xc0135761\n"
    "v_mul_f32 v48, v100, v48\n"
    "v_mul_f32 v49, v101, v49\n"
    "v_mfma_f32_16x16x32_bf16 v[188:191], v[28:31], a[200:203], v[188:191]\n"
    "s_add_u32 s72, s72, 0x40000\n"
    "s_addc_u32 s73, s73, 0\n"
    "s_waitcnt lgkmcnt(4)\n"
    "ds_read_b64_tr_b16 v[28:29], v6 offset:224\n"
    "ds_read_b64_tr_b16 v[30:31], v6 offset:480\n"
    "v_mfma_f32_16x16x32_bf16 v[192:195], v[16:19], a[32:35], v[192:195]\n"
    "s_add_u32 m0, s78, 0x9480\n"
    "v_mul_f32 v50, v102, v50\n"
    "v_mul_f32 v51, v103, v51\n"
    "v_mfma_f32_16x16x32_bf16 v[196:199], v[16:19], a[88:91], v[196:199]\n"
    "global_load_lds_dwordx4 v60, s[74:75]\n"
    "v_fma_f32 v104, v104, v14, v40\n"
    "v_mfma_f32_16x16x32_bf16 v[200:203], v[16:19], a[144:147], v[200:203]\n"
    "v_exp_f32 v48, v48\n"
    "v_mfma_f32_16x16x32_bf16 v[204:207], v[16:19], a[200:203], v[204:207]\n"
    "s_waitcnt lgkmcnt(4)\n"
    "s_waitcnt vmcnt(13)\n"
    "ds_read_b64_tr_b16 v[16:17], v6 offset:33792\n"
    "ds_read_b64_tr_b16 v[18:19], v6 offset:34048\n"
    "v_fma_f32 v105, v105, v14, v41\n"
    "v_mfma_f32_16x16x32_bf16 v[208:211], v[20:23], a[32:35], v[208:211]\n"
    "v_exp_f32 v49, v49\n"
    "v_mfma_f32_16x16x32_bf16 v[212:215], v[20:23], a[88:91], v[212:215]\n"
    "global_load_lds_dwordx4 v61, s[74:75] offset:1056\n"
    "v_fma_f32 v106, v106, v14, v42\n"
    "v_mfma_f32_16x16x32_bf16 v[216:219], v[20:23], a[144:147], v[216:219]\n"
    "v_exp_f32 v50, v50\n"
    "v_mfma_f32_16x16x32_bf16 v[220:223], v[20:23], a[200:203], v[220:223]\n"
    "s_waitcnt lgkmcnt(4)\n"
    "ds_read_b64_tr_b16 v[20:21], v6 offset:33824\n"
    "ds_read_b64_tr_b16 v[22:23], v6 offset:34080\n"
    "v_mfma_f32_16x16x32_bf16 v[224:227], v[24:27], a[32:35], v[224:227]\n"
    "v_exp_f32 v51, v51\n"
    "v_add_f32 v48, 1.0, v48\n"
    "v_mfma_f32_16x16x32_bf16 v[228:231], v[24:27], a[88:91], v[228:231]\n"
    "global_load_lds_dwordx4 v62, s[74:75] offset:2112\n"
    "v_add_f32 v49, 1.0, v49\n"
    "v_mfma_f32_16x16x32_bf16 v[232:235], v[24:27], a[144:147], v[232:235]\n"
    "v_add_f32 v50, 1.0, v50\n"
    "v_add_f32 v51, 1.0, v51\n"
    "v_mfma_f32_16x16x32_bf16 v[236:239], v[24:27], a[200:203], v[236:239]\n"
    "s_waitcnt lgkmcnt(4)\n"
    "ds_read_b64_tr_b16 v[24:25], v6 offset:33856\n"
    "ds_read_b64_tr_b16 v[26:27], v6 offset:34112\n"
    "v_mfma_f32_16x16x32_bf16 v[240:243], v[28:31], a[32:35], v[240:243]\n"
    "v_rcp_f32 v48, v48\n"
    "v_mfma_f32_16x16x32_bf16 v[244:247], v[28:31], a[88:91], v[244:247]\n"
    "global_load_lds_dwordx4 v63, s[74:75] offset:3168\n"
    "v_rcp_f32 v49, v49\n"
    "v_mfma_f32_16x16x32_bf16 v[248:251], v[28:31], a[144:147], v[248:251]\n"
    "v_rcp_f32 v50, v50\n"
    "v_mfma_f32_16x16x32_bf16 v[252:255], v[28:31], a[200:203], v[252:255]\n"
    "s_add_u32 s74, s74, 0x40000\n"
    "s_addc_u32 s75, s75, 0\n"
    "s_waitcnt lgkmcnt(4)\n"
    "ds_read_b64_tr_b16 v[28:29], v6 offset:33888\n"
    "ds_read_b64_tr_b16 v[30:31], v6 offset:34144\n"
    "v_mfma_f32_16x16x32_bf16 v[128:131], v[16:19], a[36:39], v[128:131]\n"
    "s_add_u32 m0, s78, 0x10800\n"
    "v_rcp_f32 v51, v51\n"
    "v_mfma_f32_16x16x32_bf16 v[132:135], v[16:19], a[92:95], v[132:135]\n"
    "global_load_lds_dwordx4 v60, s[72:73]\n"
    "v_mul_f32 v100, v100, v48\n"
    "v_mfma_f32_16x16x32_bf16 v[136:139], v[16:19], a[148:151], v[136:139]\n"
    "v_mul_f32 v101, v101, v49\n"
    "v_mul_f32 v102, v102, v50\n"
    "v_mul_f32 v103, v103, v51\n"
    "v_mfma_f32_16x16x32_bf16 v[140:143], v[16:19], a[204:207], v[140:143]\n"
    "s_waitcnt lgkmcnt(4)\n"
    "ds_read_b64_tr_b16 v[16:17], v6 offset:33920\n"
    "ds_read_b64_tr_b16 v[18:19], v6 offset:34176\n"
    "v_mfma_f32_16x16x32_bf16 v[144:147], v[20:23], a[36:39], v[144:147]\n"
    "v_cvt_pk_bf16_f32 v100, v100, v101\n"
    "v_cvt_pk_bf16_f32 v101, v102, v103\n"
    "v_mfma_f32_16x16x32_bf16 v[148:151], v[20:23], a[92:95], v[148:151]\n"
    "global_load_lds_dwordx4 v61, s[72:73] offset:1056\n"
    "v_fma_f32 v107, v107, v14, v43\n"
    "v_mfma_f32_16x16x32_bf16 v[152:155], v[20:23], a[148:151], v[152:155]\n"
    "ds_write_b64 v122, v[100:101] offset:1024\n"
    "v_mfma_f32_16x16x32_bf16 v[156:159], v[20:23], a[204:207], v[156:159]\n"
    "s_waitcnt lgkmcnt(5)\n"
    "ds_read_b64_tr_b16 v[20:21], v6 offset:33952\n"
    "ds_read_b64_tr_b16 v[22:23], v6 offset:34208\n"
    "v_fma_f32 v112, v112, v12, v44\n"
    "v_mfma_f32_16x16x32_bf16 v[160:163], v[24:27], a[36:39], v[160:163]\n"
    "v_fma_f32 v113, v113, v12, v45\n"
    "v_fma_f32 v114, v114, v12, v46\n"
    "v_mfma_f32_16x16x32_bf16 v[164:167], v[24:27], a[92:95], v[164:167]\n"
    "global_load_lds_dwordx4 v62, s[72:73] offset:2112\n"
    "v_fma_f32 v115, v115, v12, v47\n"
    "v_mfma_f32_16x16x32_bf16 v[168:171], v[24:27], a[148:151], v[168:171]\n"
    "v_mul_f32 v48, v112, v112\n"
    "v_mul_f32 v49, v113, v113\n"
    "v_mfma_f32_16x16x32_bf16 v[172:175], v[24:27], a[204:207], v[172:175]\n"
    "s_waitcnt lgkmcnt(5)\n"
    "ds_read_b64_tr_b16 v[24:25], v6 offset:33984\n"
    "ds_read_b64_tr_b16 v[26:27], v6 offset:34240\n"
    "v_mfma_f32_16x16x32_bf16 v[176:179], v[28:31], a[36:39], v[176:179]\n"
    "v_mul_f32 v50, v114, v114\n"
    "v_mul_f32 v51, v115, v115\n"
    "v_mfma_f32_16x16x32_bf16 v[180:183], v[28:31], a[92:95], v[180:183]\n"
    "global_load_lds_dwordx4 v63, s[72:73] offset:3168\n"
    "v_fmaak_f32 v48, v48, v127, 0xc0135761\n"
    "v_fmaak_f32 v49, v49, v127, 0xc0135761\n"
    "v_mfma_f32_16x16x32_bf16 v[184:187], v[28:31], a[148:151], v[184:187]\n"
    "v_fmaak_f32 v50, v50, v127, 0xc0135761\n"
    "v_fmaak_f32 v51, v51, v127, 0xc0135761\n"
    "v_mfma_f32_16x16x32_bf16 v[188:191], v[28:31], a[204:207], v[188:191]\n"
    "s_add_u32 s72, s72, 0x40000\n"
    "s_addc_u32 s73, s73, 0\n"
    "s_waitcnt lgkmcnt(5)\n"
    "ds_read_b64_tr_b16 v[28:29], v6 offset:34016\n"
    "ds_read_b64_tr_b16 v[30:31], v6 offset:34272\n"
    "v_mfma_f32_16x16x32_bf16 v[192:195], v[16:19], a[36:39], v[192:195]\n"
    "s_add_u32 m0, s78, 0x11880\n"
    "v_mul_f32 v48, v112, v48\n"
    "v_mul_f32 v49, v113, v49\n"
    "v_mfma_f32_16x16x32_bf16 v[196:199], v[16:19], a[92:95], v[196:199]\n"
    "global_load_lds_dwordx4 v60, s[74:75]\n"
    "v_mul_f32 v50, v114, v50\n"
    "v_mfma_f32_16x16x32_bf16 v[200:203], v[16:19], a[148:151], v[200:203]\n"
    "v_mul_f32 v51, v115, v51\n"
    "v_exp_f32 v48, v48\n"
    "v_mfma_f32_16x16x32_bf16 v[204:207], v[16:19], a[204:207], v[204:207]\n"
    "s_waitcnt lgkmcnt(4)\n"
    "s_waitcnt vmcnt(13)\n"
    "ds_read_b64_tr_b16 v[16:17], v5\n"
    "ds_read_b64_tr_b16 v[18:19], v5 offset:256\n"
    "v_mfma_f32_16x16x32_bf16 v[208:211], v[20:23], a[36:39], v[208:211]\n"
    "v_exp_f32 v49, v49\n"
    "v_mfma_f32_16x16x32_bf16 v[212:215], v[20:23], a[92:95], v[212:215]\n"
    "global_load_lds_dwordx4 v61, s[74:75] offset:1056\n"
    "v_mul_f32 v52, v104, v104\n"
    "v_mfma_f32_16x16x32_bf16 v[216:219], v[20:23], a[148:151], v[216:219]\n"
    "v_exp_f32 v50, v50\n"
    "v_mfma_f32_16x16x32_bf16 v[220:223], v[20:23], a[204:207], v[220:223]\n"
    "s_waitcnt lgkmcnt(4)\n"
    "ds_read_b64_tr_b16 v[20:21], v5 offset:32\n"
    "ds_read_b64_tr_b16 v[22:23], v5 offset:288\n"
    "v_mul_f32 v53, v105, v105\n"
    "v_mfma_f32_16x16x32_bf16 v[224:227], v[24:27], a[36:39], v[224:227]\n"
    "v_exp_f32 v51, v51\n"
    "v_mfma_f32_16x16x32_bf16 v[228:231], v[24:27], a[92:95], v[228:231]\n"
    "global_load_lds_dwordx4 v62, s[74:75] offset:2112\n"
    "v_add_f32 v48, 1.0, v48\n"
    "v_mfma_f32_16x16x32_bf16 v[232:235], v[24:27], a[148:151], v[232:235]\n"
    "v_add_f32 v49, 1.0, v49\n"
    "v_add_f32 v50, 1.0, v50\n"
    "v_mfma_f32_16x16x32_bf16 v[236:239], v[24:27], a[204:207], v[236:239]\n"
    "s_waitcnt lgkmcnt(4)\n"
    "ds_read_b64_tr_b16 v[24:25], v5 offset:64\n"
    "ds_read_b64_tr_b16 v[26:27], v5 offset:320\n"
    "v_mfma_f32_16x16x32_bf16 v[240:243], v[28:31], a[36:39], v[240:243]\n"
    "v_add_f32 v51, 1.0, v51\n"
    "v_rcp_f32 v48, v48\n"
    "v_mfma_f32_16x16x32_bf16 v[244:247], v[28:31], a[92:95], v[244:247]\n"
    "global_load_lds_dwordx4 v63, s[74:75] offset:3168\n"
    "v_mul_f32 v54, v106, v106\n"
    "v_mfma_f32_16x16x32_bf16 v[248:251], v[28:31], a[148:151], v[248:251]\n"
    "v_rcp_f32 v49, v49\n"
    "v_mfma_f32_16x16x32_bf16 v[252:255], v[28:31], a[204:207], v[252:255]\n"
    "s_add_u32 s74, s74, 0x40000\n"
    "s_addc_u32 s75, s75, 0\n"
    "s_waitcnt lgkmcnt(4)\n"
    "ds_read_b64_tr_b16 v[28:29], v5 offset:96\n"
    "ds_read_b64_tr_b16 v[30:31], v5 offset:352\n"
    "v_mfma_f32_16x16x32_bf16 v[128:131], v[16:19], a[40:43], v[128:131]\n"
    "s_add_u32 m0, s78, 0x18c00\n"
    "v_rcp_f32 v50, v50\n"
    "v_mfma_f32_16x16x32_bf16 v[132:135], v[16:19], a[96:99], v[132:135]\n"
    "global_load_lds_dwordx4 v60, s[72:73]\n"
    "v_rcp_f32 v51, v51\n"
    "v_mfma_f32_16x16x32_bf16 v[136:139], v[16:19], a[152:155], v[136:139]\n"
    "v_mul_f32 v112, v112, v48\n"
    "v_mul_f32 v113, v113, v49\n"
    "v_mfma_f32_16x16x32_bf16 v[140:143], v[16:19], a[208:211], v[140:143]\n"
    "s_waitcnt lgkmcnt(4)\n"
    "ds_read_b64_tr_b16 v[16:17], v5 offset:128\n"
    "ds_read_b64_tr_b16 v[18:19], v5 offset:384\n"
    "v_mfma_f32_16x16x32_bf16 v[144:147], v[20:23], a[40:43], v[144:147]\n"
    "v_mul_f32 v114, v114, v50\n"
    "v_mul_f32 v115, v115, v51\n"
    "v_mfma_f32_16x16x32_bf16 v[148:151], v[20:23], a[96:99], v[148:151]\n"
    "global_load_lds_dwordx4 v61, s[72:73] offset:1056\n"
    "v_cvt_pk_bf16_f32 v112, v112, v113\n"
    "v_mfma_f32_16x16x32_bf16 v[152:155], v[20:23], a[152:155], v[152:155]\n"
    "v_cvt_pk_bf16_f32 v113, v114, v115\n"
    "ds_write_b64 v123, v[112:113]\n"
    "v_mfma_f32_16x16x32_bf16 v[156:159], v[20:23], a[208:211], v[156:159]\n"
    "s_waitcnt lgkmcnt(5)\n"
    "ds_read_b64_tr_b16 v[20:21], v5 offset:160\n"
    "ds_read_b64_tr_b16 v[22:23], v5 offset:416\n"
    "v_mfma_f32_16x16x32_bf16 v[160:163], v[24:27], a[40:43], v[160:163]\n"
    "v_mul_f32 v55, v107, v107\n"
    "v_fmaak_f32 v52, v52, v127, 0xc0135761\n"
    "v_mfma_f32_16x16x32_bf16 v[164:167], v[24:27], a[96:99], v[164:167]\n"
    "global_load_lds_dwordx4 v62, s[72:73] offset:2112\n"
    "v_fmaak_f32 v53, v53, v127, 0xc0135761\n"
    "v_mfma_f32_16x16x32_bf16 v[168:171], v[24:27], a[152:155], v[168:171]\n"
    "v_fmaak_f32 v54, v54, v127, 0xc0135761\n"
    "v_fmaak_f32 v55, v55, v127, 0xc0135761\n"
    "v_mfma_f32_16x16x32_bf16 v[172:175], v[24:27], a[208:211], v[172:175]\n"
    "s_waitcnt lgkmcnt(5)\n"
    "ds_read_b64_tr_b16 v[24:25], v5 offset:192\n"
    "ds_read_b64_tr_b16 v[26:27], v5 offset:448\n"
    "v_mul_f32 v52, v104, v52\n"
    "v_mfma_f32_16x16x32_bf16 v[176:179], v[28:31], a[40:43], v[176:179]\n"
    "v_mul_f32 v53, v105, v53\n"
    "v_mul_f32 v54, v106, v54\n"
    "v_mfma_f32_16x16x32_bf16 v[180:183], v[28:31], a[96:99], v[180:183]\n"
    "global_load_lds_dwordx4 v63, s[72:73] offset:3168\n"
    "v_mul_f32 v55, v107, v55\n"
    "v_mfma_f32_16x16x32_bf16 v[184:187], v[28:31], a[152:155], v[184:187]\n"
    "v_exp_f32 v52, v52\n"
    "v_mfma_f32_16x16x32_bf16 v[188:191], v[28:31], a[208:211], v[188:191]\n"
    "s_add_u32 s72, s72, 0x40000\n"
    "s_addc_u32 s73, s73, 0\n"
    "s_waitcnt lgkmcnt(5)\n"
    "ds_read_b64_tr_b16 v[28:29], v5 offset:224\n"
    "ds_read_b64_tr_b16 v[30:31], v5 offset:480\n"
    "v_mfma_f32_16x16x32_bf16 v[192:195], v[16:19], a[40:43], v[192:195]\n"
    "s_add_u32 m0, s78, 0x19c80\n"
    "v_exp_f32 v53, v53\n"
    "v_fma_f32 v98, v98, v12, v42\n"
    "v_mfma_f32_16x16x32_bf16 v[196:199], v[16:19], a[96:99], v[196:199]\n"
    "global_load_lds_dwordx4 v60, s[74:75]\n"
    "v_fma_f32 v99, v99, v12, v43\n"
    "v_mfma_f32_16x16x32_bf16 v[200:203], v[16:19], a[152:155], v[200:203]\n"
    "v_exp_f32 v54, v54\n"
    "v_mfma_f32_16x16x32_bf16 v[204:207], v[16:19], a[208:211], v[204:207]\n"
    "s_waitcnt lgkmcnt(4)\n"
    "s_waitcnt vmcnt(13)\n"
    "ds_read_b64_tr_b16 v[16:17], v5 offset:33792\n"
    "ds_read_b64_tr_b16 v[18:19], v5 offset:34048\n"
    "v_mfma_f32_16x16x32_bf16 v[208:211], v[20:23], a[40:43], v[208:211]\n"
    "v_exp_f32 v55, v55\n"
    "v_mfma_f32_16x16x32_bf16 v[212:215], v[20:23], a[96:99], v[212:215]\n"
    "global_load_lds_dwordx4 v61, s[74:75] offset:1056\n"
    "v_add_f32 v52, 1.0, v52\n"
    "v_add_f32 v53, 1.0, v53\n"
    "v_mfma_f32_16x16x32_bf16 v[216:219], v[20:23], a[152:155], v[216:219]\n"
    "v_add_f32 v54, 1.0, v54\n"
    "v_add_f32 v55, 1.0, v55\n"
    "v_mfma_f32_16x16x32_bf16 v[220:223], v[20:23], a[208:211], v[220:223]\n"
    "s_waitcnt lgkmcnt(4)\n"
    "ds_read_b64_tr_b16 v[20:21], v5 offset:33824\n"
    "ds_read_b64_tr_b16 v[22:23], v5 offset:34080\n"
    "v_mfma_f32_16x16x32_bf16 v[224:227], v[24:27], a[40:43], v[224:227]\n"
    "v_rcp_f32 v52, v52\n"
    "v_mfma_f32_16x16x32_bf16 v[228:231], v[24:27], a[96:99], v[228:231]\n"
    "global_load_lds_dwordx4 v62, s[74:75] offset:2112\n"
    "v_mul_f32 v56, v96, v96\n"
    "v_mfma_f32_16x16x32_bf16 v[232:235], v[24:27], a[152:155], v[232:235]\n"
    "v_rcp_f32 v53, v53\n"
    "v_mul_f32 v57, v97, v97\n"
    "v_mfma_f32_16x16x32_bf16 v[236:239], v[24:27], a[208:211], v[236:239]\n"
    "s_waitcnt lgkmcnt(4)\n"
    "ds_read_b64_tr_b16 v[24:25], v5 offset:33856\n"
    "ds_read_b64_tr_b16 v[26:27], v5 offset:34112\n"
    "v_mfma_f32_16x16x32_bf16 v[240:243], v[28:31], a[40:43], v[240:243]\n"
    "v_rcp_f32 v54, v54\n"
    "v_mfma_f32_16x16x32_bf16 v[244:247], v[28:31], a[96:99], v[244:247]\n"
    "global_load_lds_dwordx4 v63, s[74:75] offset:3168\n"
    "v_mul_f32 v58, v98, v98\n"
    "v_mfma_f32_16x16x32_bf16 v[248:251], v[28:31], a[152:155], v[248:251]\n"
    "v_rcp_f32 v55, v55\n"
    "v_mfma_f32_16x16x32_bf16 v[252:255], v[28:31], a[208:211], v[252:255]\n"
    "s_add_u32 s74, s74, 0x40000\n"
    "s_addc_u32 s75, s75, 0\n"
    "s_waitcnt lgkmcnt(4)\n"
    "ds_read_b64_tr_b16 v[28:29], v5 offset:33888\n"
    "ds_read_b64_tr_b16 v[30:31], v5 offset:34144\n"
    "v_mfma_f32_16x16x32_bf16 v[128:131], v[16:19], a[44:47], v[128:131]\n"
    "s_add_u32 m0, s78, 0\n"
    "v_mul_f32 v104, v104, v52\n"
    "v_mul_f32 v105, v105, v53\n"
    "v_mfma_f32_16x16x32_bf16 v[132:135], v[16:19], a[100:103], v[132:135]\n"
    "global_load_lds_dwordx4 v32, s[76:77]\n"
    "v_mul_f32 v106, v106, v54\n"
    "v_mfma_f32_16x16x32_bf16 v[136:139], v[16:19], a[156:159], v[136:139]\n"
    "v_mul_f32 v107, v107, v55\n"
    "v_cvt_pk_bf16_f32 v104, v104, v105\n"
    "v_mfma_f32_16x16x32_bf16 v[140:143], v[16:19], a[212:215], v[140:143]\n"
    "s_waitcnt lgkmcnt(4)\n"
    "ds_read_b64_tr_b16 v[16:17], v5 offset:33920\n"
    "ds_read_b64_tr_b16 v[18:19], v5 offset:34176\n"
    "v_mfma_f32_16x16x32_bf16 v[144:147], v[20:23], a[44:47], v[144:147]\n"
    "v_cvt_pk_bf16_f32 v105, v106, v107\n"
    "v_mul_f32 v59, v99, v99\n"
    "v_fmaak_f32 v56, v56, v127, 0xc0135761\n"
    "v_mfma_f32_16x16x32_bf16 v[148:151], v[20:23], a[100:103], v[148:151]\n"
    "global_load_lds_dwordx4 v33, s[76:77] offset:1024\n"
    "v_fmaak_f32 v57, v57, v127, 0xc0135761\n"
    "v_mfma_f32_16x16x32_bf16 v[152:155], v[20:23], a[156:159], v[152:155]\n"
    "v_fmaak_f32 v58, v58, v127, 0xc0135761\n"
    "v_fmaak_f32 v59, v59, v127, 0xc0135761\n"
    "v_mfma_f32_16x16x32_bf16 v[156:159], v[20:23], a[212:215], v[156:159]\n"
    "s_waitcnt lgkmcnt(4)\n"
    "ds_read_b64_tr_b16 v[20:21], v5 offset:33952\n"
    "ds_read_b64_tr_b16 v[22:23], v5 offset:34208\n"
    "v_mfma_f32_16x16x32_bf16 v[160:163], v[24:27], a[44:47], v[160:163]\n"
    "v_mul_f32 v56, v96, v56\n"
    "v_mul_f32 v57, v97, v57\n"
    "v_mfma_f32_16x16x32_bf16 v[164:167], v[24:27], a[100:103], v[164:167]\n"
    "global_load_lds_dwordx4 v34, s[76:77] offset:2048\n"
    "v_mul_f32 v58, v98, v58\n"
    "v_mul_f32 v59, v99, v59\n"
    "v_mfma_f32_16x16x32_bf16 v[168:171], v[24:27], a[156:159], v[168:171]\n"
    "v_exp_f32 v56, v56\n"
    "v_mfma_f32_16x16x32_bf16 v[172:175], v[24:27], a[212:215], v[172:175]\n"
    "s_waitcnt lgkmcnt(4)\n"
    "ds_read_b64_tr_b16 v[24:25], v5 offset:33984\n"
    "ds_read_b64_tr_b16 v[26:27], v5 offset:34240\n"
    "v_mfma_f32_16x16x32_bf16 v[176:179], v[28:31], a[44:47], v[176:179]\n"
    "v_exp_f32 v57, v57\n"
    "v_mfma_f32_16x16x32_bf16 v[180:183], v[28:31], a[100:103], v[180:183]\n"
    "global_load_lds_dwordx4 v35, s[76:77] offset:3072\n"
    "v_mfma_f32_16x16x32_bf16 v[184:187], v[28:31], a[156:159], v[184:187]\n"
    "v_exp_f32 v58, v58\n"
    "v_mfma_f32_16x16x32_bf16 v[188:191], v[28:31], a[212:215], v[188:191]\n"
    "s_waitcnt lgkmcnt(4)\n"
    "ds_read_b64_tr_b16 v[28:29], v5 offset:34016\n"
    "ds_read_b64_tr_b16 v[30:31], v5 offset:34272\n"
    "v_mfma_f32_16x16x32_bf16 v[192:195], v[16:19], a[44:47], v[192:195]\n"
    "s_add_u32 m0, s78, 0x1000\n"
    "v_exp_f32 v59, v59\n"
    "v_mfma_f32_16x16x32_bf16 v[196:199], v[16:19], a[100:103], v[196:199]\n"
    "global_load_lds_dwordx4 v36, s[76:77]\n"
    "v_add_f32 v56, 1.0, v56\n"
    "v_mfma_f32_16x16x32_bf16 v[200:203], v[16:19], a[156:159], v[200:203]\n"
    "v_add_f32 v57, 1.0, v57\n"
    "v_add_f32 v58, 1.0, v58\n"
    "v_mfma_f32_16x16x32_bf16 v[204:207], v[16:19], a[212:215], v[204:207]\n"
    "s_waitcnt lgkmcnt(4)\n"
    "s_waitcnt vmcnt(13)\n"
    "ds_read_b64_tr_b16 v[16:17], v6\n"
    "ds_read_b64_tr_b16 v[18:19], v6 offset:256\n"
    "v_add_f32 v59, 1.0, v59\n"
    "v_mfma_f32_16x16x32_bf16 v[208:211], v[20:23], a[44:47], v[208:211]\n"
    "v_rcp_f32 v56, v56\n"
    "v_mfma_f32_16x16x32_bf16 v[212:215], v[20:23], a[100:103], v[212:215]\n"
    "global_load_lds_dwordx4 v37, s[76:77] offset:1024\n"
    "v_mfma_f32_16x16x32_bf16 v[216:219], v[20:23], a[156:159], v[216:219]\n"
    "v_rcp_f32 v57, v57\n"
    "v_mfma_f32_16x16x32_bf16 v[220:223], v[20:23], a[212:215], v[220:223]\n"
    "s_waitcnt lgkmcnt(4)\n"
    "ds_read_b64_tr_b16 v[20:21], v6 offset:32\n"
    "ds_read_b64_tr_b16 v[22:23], v6 offset:288\n"
    "v_mfma_f32_16x16x32_bf16 v[224:227], v[24:27], a[44:47], v[224:227]\n"
    "v_rcp_f32 v58, v58\n"
    "v_mfma_f32_16x16x32_bf16 v[228:231], v[24:27], a[100:103], v[228:231]\n"
    "global_load_lds_dwordx4 v38, s[76:77] offset:2048\n"
    "v_rcp_f32 v59, v59\n"
    "v_mfma_f32_16x16x32_bf16 v[232:235], v[24:27], a[156:159], v[232:235]\n"
    "v_mul_f32 v96, v96, v56\n"
    "v_mul_f32 v97, v97, v57\n"
    "v_mfma_f32_16x16x32_bf16 v[236:239], v[24:27], a[212:215], v[236:239]\n"
    "s_add_u32 s76, s76, 0x7000\n"
    "s_addc_u32 s77, s77, 0\n"
    "s_waitcnt lgkmcnt(4)\n"
    "ds_read_b64_tr_b16 v[24:25], v6 offset:64\n"
    "ds_read_b64_tr_b16 v[26:27], v6 offset:320\n"
    "v_mfma_f32_16x16x32_bf16 v[240:243], v[28:31], a[44:47], v[240:243]\n"
    "v_mul_f32 v98, v98, v58\n"
    "v_mul_f32 v99, v99, v59\n"
    "v_mfma_f32_16x16x32_bf16 v[244:247], v[28:31], a[100:103], v[244:247]\n"
    "v_cvt_pk_bf16_f32 v96, v96, v97\n"
    "v_cvt_pk_bf16_f32 v97, v98, v99\n"
    "v_mfma_f32_16x16x32_bf16 v[248:251], v[28:31], a[156:159], v[248:251]\n"
    "ds_write_b64 v122, v[96:97]\n"
    "v_mfma_f32_16x16x32_bf16 v[252:255], v[28:31], a[212:215], v[252:255]\n"
    "s_waitcnt lgkmcnt(5)\n"
    "ds_read_b64_tr_b16 v[28:29], v6 offset:96\n"
    "ds_read_b64_tr_b16 v[30:31], v6 offset:352\n"
    "v_mfma_f32_16x16x32_bf16 v[128:131], v[16:19], a[48:51], v[128:131]\n"
    "s_add_u32 m0, s78, 0x8400\n"
    "v_xor_b32 v122, 0x800, v122\n"
    "v_fma_f32 v108, v108, v15, v40\n"
    "v_mfma_f32_16x16x32_bf16 v[132:135], v[16:19], a[104:107], v[132:135]\n"
    "global_load_lds_dwordx4 v32, s[76:77]\n"
    "v_fma_f32 v109, v109, v15, v41\n"
    "v_mfma_f32_16x16x32_bf16 v[136:139], v[16:19], a[160:163], v[136:139]\n"
    "ds_write_b64 v122, v[104:105]\n"
    "v_mfma_f32_16x16x32_bf16 v[140:143], v[16:19], a[216:219], v[140:143]\n"
    "s_waitcnt lgkmcnt(6)\n"
    "ds_read_b64_tr_b16 v[16:17], v6 offset:128\n"
    "ds_read_b64_tr_b16 v[18:19], v6 offset:384\n"
    "v_fma_f32 v116, v116, v13, v44\n"
    "v_mfma_f32_16x16x32_bf16 v[144:147], v[20:23], a[48:51], v[144:147]\n"
    "v_fma_f32 v117, v117, v13, v45\n"
    "v_fma_f32 v118, v118, v13, v46\n"
    "v_mfma_f32_16x16x32_bf16 v[148:151], v[20:23], a[104:107], v[148:151]\n"
    "global_load_lds_dwordx4 v33, s[76:77] offset:1024\n"
    "v_fma_f32 v119, v119, v13, v47\n"
    "v_mfma_f32_16x16x32_bf16 v[152:155], v[20:23], a[160:163], v[152:155]\n"
    "v_mul_f32 v52, v116, v116\n"
    "v_mul_f32 v53, v117, v117\n"
    "v_mfma_f32_16x16x32_bf16 v[156:159], v[20:23], a[216:219], v[156:159]\n"
    "s_waitcnt lgkmcnt(6)\n"
    "ds_read_b64_tr_b16 v[20:21], v6 offset:160\n"
    "ds_read_b64_tr_b16 v[22:23], v6 offset:416\n"
    "v_mfma_f32_16x16x32_bf16 v[160:163], v[24:27], a[48:51], v[160:163]\n"
    "v_mul_f32 v54, v118, v118\n"
    "v_mul_f32 v55, v119, v119\n"
    "v_fmaak_f32 v52, v52, v127, 0xc0135761\n"
    "v_mfma_f32_16x16x32_bf16 v[164:167], v[24:27], a[104:107], v[164:167]\n"
    "global_load_lds_dwordx4 v34, s[76:77] offset:2048\n"
    "v_fmaak_f32 v53, v53, v127, 0xc0135761\n"
    "v_mfma_f32_16x16x32_bf16 v[168:171], v[24:27], a[160:163], v[168:171]\n"
    "v_fmaak_f32 v54, v54, v127, 0xc0135761\n"
    "v_fmaak_f32 v55, v55, v127, 0xc0135761\n"
    "v_mfma_f32_16x16x32_bf16 v[172:175], v[24:27], a[216:219], v[172:175]\n"
    "s_waitcnt lgkmcnt(5)\n"
    "ds_read_b64_tr_b16 v[24:25], v6 offset:192\n"
    "ds_read_b64_tr_b16 v[26:27], v6 offset:448\n"
    "v_mfma_f32_16x16x32_bf16 v[176:179], v[28:31], a[48:51], v[176:179]\n"
    "v_mul_f32 v52, v116, v52\n"
    "v_mul_f32 v53, v117, v53\n"
    "v_mfma_f32_16x16x32_bf16 v[180:183], v[28:31], a[104:107], v[180:183]\n"
    "global_load_lds_dwordx4 v35, s[76:77] offset:3072\n"
    "v_mul_f32 v54, v118, v54\n"
    "v_mul_f32 v55, v119, v55\n"
    "v_mfma_f32_16x16x32_bf16 v[184:187], v[28:31], a[160:163], v[184:187]\n"
    "v_exp_f32 v52, v52\n"
    "v_mfma_f32_16x16x32_bf16 v[188:191], v[28:31], a[216:219], v[188:191]\n"
    "s_waitcnt lgkmcnt(4)\n"
    "ds_read_b64_tr_b16 v[28:29], v6 offset:224\n"
    "ds_read_b64_tr_b16 v[30:31], v6 offset:480\n"
    "v_mfma_f32_16x16x32_bf16 v[192:195], v[16:19], a[48:51], v[192:195]\n"
    "s_add_u32 m0, s78, 0x9400\n"
    "v_exp_f32 v53, v53\n"
    "v_mfma_f32_16x16x32_bf16 v[196:199], v[16:19], a[104:107], v[196:199]\n"
    "global_load_lds_dwordx4 v36, s[76:77]\n"
    "v_fma_f32 v110, v110, v15, v42\n"
    "v_mfma_f32_16x16x32_bf16 v[200:203], v[16:19], a[160:163], v[200:203]\n"
    "v_exp_f32 v54, v54\n"
    "v_fma_f32 v111, v111, v15, v43\n"
    "v_mfma_f32_16x16x32_bf16 v[204:207], v[16:19], a[216:219], v[204:207]\n"
    "s_waitcnt lgkmcnt(4)\n"
    "s_waitcnt vmcnt(12)\n"
    "ds_read_b64_tr_b16 v[16:17], v6 offset:33792\n"
    "ds_read_b64_tr_b16 v[18:19], v6 offset:34048\n"
    "v_mfma_f32_16x16x32_bf16 v[208:211], v[20:23], a[48:51], v[208:211]\n"
    "v_exp_f32 v55, v55\n"
    "v_mfma_f32_16x16x32_bf16 v[212:215], v[20:23], a[104:107], v[212:215]\n"
    "global_load_lds_dwordx4 v37, s[76:77] offset:1024\n"
    "v_add_f32 v52, 1.0, v52\n"
    "v_mfma_f32_16x16x32_bf16 v[216:219], v[20:23], a[160:163], v[216:219]\n"
    "v_add_f32 v53, 1.0, v53\n"
    "v_add_f32 v54, 1.0, v54\n"
    "v_mfma_f32_16x16x32_bf16 v[220:223], v[20:23], a[216:219], v[220:223]\n"
    "s_waitcnt lgkmcnt(4)\n"
    "ds_read_b64_tr_b16 v[20:21], v6 offset:33824\n"
    "ds_read_b64_tr_b16 v[22:23], v6 offset:34080\n"
    "v_add_f32 v55, 1.0, v55\n"
    "v_mfma_f32_16x16x32_bf16 v[224:227], v[24:27], a[48:51], v[224:227]\n"
    "v_rcp_f32 v52, v52\n"
    "v_mfma_f32_16x16x32_bf16 v[228:231], v[24:27], a[104:107], v[228:231]\n"
    "global_load_lds_dwordx4 v38, s[76:77] offset:2048\n"
    "v_mul_f32 v56, v108, v108\n"
    "v_mfma_f32_16x16x32_bf16 v[232:235], v[24:27], a[160:163], v[232:235]\n"
    "v_rcp_f32 v53, v53\n"
    "v_mfma_f32_16x16x32_bf16 v[236:239], v[24:27], a[216:219], v[236:239]\n"
    "s_add_u32 s76, s76, 0x7000\n"
    "s_addc_u32 s77, s77, 0\n"
    "s_waitcnt lgkmcnt(4)\n"
    "ds_read_b64_tr_b16 v[24:25], v6 offset:33856\n"
    "ds_read_b64_tr_b16 v[26:27], v6 offset:34112\n"
    "v_mfma_f32_16x16x32_bf16 v[240:243], v[28:31], a[48:51], v[240:243]\n"
    "v_rcp_f32 v54, v54\n"
    "v_mul_f32 v57, v109, v109\n"
    "v_mfma_f32_16x16x32_bf16 v[244:247], v[28:31], a[104:107], v[244:247]\n"
    "v_rcp_f32 v55, v55\n"
    "v_mfma_f32_16x16x32_bf16 v[248:251], v[28:31], a[160:163], v[248:251]\n"
    "v_mul_f32 v116, v116, v52\n"
    "v_mul_f32 v117, v117, v53\n"
    "v_mfma_f32_16x16x32_bf16 v[252:255], v[28:31], a[216:219], v[252:255]\n"
    "s_waitcnt lgkmcnt(4)\n"
    "ds_read_b64_tr_b16 v[28:29], v6 offset:33888\n"
    "ds_read_b64_tr_b16 v[30:31], v6 offset:34144\n"
    "v_mfma_f32_16x16x32_bf16 v[128:131], v[16:19], a[52:55], v[128:131]\n"
    "s_add_u32 m0, s78, 0x10800\n"
    "v_mul_f32 v118, v118, v54\n"
    "v_mul_f32 v119, v119, v55\n"
    "v_mfma_f32_16x16x32_bf16 v[132:135], v[16:19], a[108:111], v[132:135]\n"
    "global_load_lds_dwordx4 v32, s[76:77]\n"
    "v_cvt_pk_bf16_f32 v116, v116, v117\n"
    "v_cvt_pk_bf16_f32 v117, v118, v119\n"
    "v_mfma_f32_16x16x32_bf16 v[136:139], v[16:19], a[164:167], v[136:139]\n"
    "ds_write_b64 v123, v[116:117] offset:1024\n"
    "v_mfma_f32_16x16x32_bf16 v[140:143], v[16:19], a[220:223], v[140:143]\n"
    "s_waitcnt lgkmcnt(5)\n"
    "ds_read_b64_tr_b16 v[16:17], v6 offset:33920\n"
    "ds_read_b64_tr_b16 v[18:19], v6 offset:34176\n"
    "v_mfma_f32_16x16x32_bf16 v[144:147], v[20:23], a[52:55], v[144:147]\n"
    "v_mul_f32 v58, v110, v110\n"
    "v_mul_f32 v59, v111, v111\n"
    "v_mfma_f32_16x16x32_bf16 v[148:151], v[20:23], a[108:111], v[148:151]\n"
    "global_load_lds_dwordx4 v33, s[76:77] offset:1024\n"
    "v_fmaak_f32 v56, v56, v127, 0xc0135761\n"
    "v_mfma_f32_16x16x32_bf16 v[152:155], v[20:23], a[164:167], v[152:155]\n"
    "v_fmaak_f32 v57, v57, v127, 0xc0135761\n"
    "v_fmaak_f32 v58, v58, v127, 0xc0135761\n"
    "v_fmaak_f32 v59, v59, v127, 0xc0135761\n"
    "v_mfma_f32_16x16x32_bf16 v[156:159], v[20:23], a[220:223], v[156:159]\n"
    "s_waitcnt lgkmcnt(5)\n"
    "ds_read_b64_tr_b16 v[20:21], v6 offset:33952\n"
    "ds_read_b64_tr_b16 v[22:23], v6 offset:34208\n"
    "v_mfma_f32_16x16x32_bf16 v[160:163], v[24:27], a[52:55], v[160:163]\n"
    "v_mul_f32 v56, v108, v56\n"
    "v_mul_f32 v57, v109, v57\n"
    "v_mfma_f32_16x16x32_bf16 v[164:167], v[24:27], a[108:111], v[164:167]\n"
    "global_load_lds_dwordx4 v34, s[76:77] offset:2048\n"
    "v_mul_f32 v58, v110, v58\n"
    "v_mfma_f32_16x16x32_bf16 v[168:171], v[24:27], a[164:167], v[168:171]\n"
    "v_mul_f32 v59, v111, v59\n"
    "v_mfma_f32_16x16x32_bf16 v[172:175], v[24:27], a[220:223], v[172:175]\n"
    "s_waitcnt lgkmcnt(5)\n"
    "ds_read_b64_tr_b16 v[24:25], v6 offset:33984\n"
    "ds_read_b64_tr_b16 v[26:27], v6 offset:34240\n"
    "v_mfma_f32_16x16x32_bf16 v[176:179], v[28:31], a[52:55], v[176:179]\n"
    "v_exp_f32 v56, v56\n"
    "v_mfma_f32_16x16x32_bf16 v[180:183], v[28:31], a[108:111], v[180:183]\n"
    "global_load_lds_dwordx4 v35, s[76:77] offset:3072\n"
    "v_mfma_f32_16x16x32_bf16 v[184:187], v[28:31], a[164:167], v[184:187]\n"
    "v_exp_f32 v57, v57\n"
    "v_mfma_f32_16x16x32_bf16 v[188:191], v[28:31], a[220:223], v[188:191]\n"
    "s_waitcnt lgkmcnt(4)\n"
    "ds_read_b64_tr_b16 v[28:29], v6 offset:34016\n"
    "ds_read_b64_tr_b16 v[30:31], v6 offset:34272\n"
    "v_mfma_f32_16x16x32_bf16 v[192:195], v[16:19], a[52:55], v[192:195]\n"
    "s_add_u32 m0, s78, 0x11800\n"
    "v_exp_f32 v58, v58\n"
    "v_mfma_f32_16x16x32_bf16 v[196:199], v[16:19], a[108:111], v[196:199]\n"
    "global_load_lds_dwordx4 v36, s[76:77]\n"
    "v_mfma_f32_16x16x32_bf16 v[200:203], v[16:19], a[164:167], v[200:203]\n"
    "v_exp_f32 v59, v59\n"
    "v_mfma_f32_16x16x32_bf16 v[204:207], v[16:19], a[220:223], v[204:207]\n"
    "s_waitcnt lgkmcnt(4)\n"
    "v_add_f32 v56, 1.0, v56\n"
    "v_add_f32 v57, 1.0, v57\n"
    "v_mfma_f32_16x16x32_bf16 v[208:211], v[20:23], a[52:55], v[208:211]\n"
    "v_add_f32 v58, 1.0, v58\n"
    "v_add_f32 v59, 1.0, v59\n"
    "v_mfma_f32_16x16x32_bf16 v[212:215], v[20:23], a[108:111], v[212:215]\n"
    "global_load_lds_dwordx4 v37, s[76:77] offset:1024\n"
    "v_rcp_f32 v56, v56\n"
    "v_mfma_f32_16x16x32_bf16 v[216:219], v[20:23], a[164:167], v[216:219]\n"
    "v_rcp_f32 v57, v57\n"
    "v_mfma_f32_16x16x32_bf16 v[220:223], v[20:23], a[220:223], v[220:223]\n"
    "s_waitcnt lgkmcnt(2)\n"
    "v_rcp_f32 v58, v58\n"
    "v_mfma_f32_16x16x32_bf16 v[224:227], v[24:27], a[52:55], v[224:227]\n"
    "v_rcp_f32 v59, v59\n"
    "v_mfma_f32_16x16x32_bf16 v[228:231], v[24:27], a[108:111], v[228:231]\n"
    "global_load_lds_dwordx4 v38, s[76:77] offset:2048\n"
    "v_mul_f32 v108, v108, v56\n"
    "v_mfma_f32_16x16x32_bf16 v[232:235], v[24:27], a[164:167], v[232:235]\n"
    "v_mul_f32 v109, v109, v57\n"
    "v_mul_f32 v110, v110, v58\n"
    "v_mul_f32 v111, v111, v59\n"
    "v_mfma_f32_16x16x32_bf16 v[236:239], v[24:27], a[220:223], v[236:239]\n"
    "s_add_u32 s76, s76, 0x7000\n"
    "s_addc_u32 s77, s77, 0\n"
    "s_waitcnt lgkmcnt(0)\n"
    "v_mfma_f32_16x16x32_bf16 v[240:243], v[28:31], a[52:55], v[240:243]\n"
    "v_cvt_pk_bf16_f32 v108, v108, v109\n"
    "v_cvt_pk_bf16_f32 v109, v110, v111\n"
    "v_mfma_f32_16x16x32_bf16 v[244:247], v[28:31], a[108:111], v[244:247]\n"
    "ds_write_b64 v122, v[108:109] offset:1024\n"
    "v_mfma_f32_16x16x32_bf16 v[248:251], v[28:31], a[164:167], v[248:251]\n"
    "v_mfma_f32_16x16x32_bf16 v[252:255], v[28:31], a[220:223], v[252:255]\n"
    "s_waitcnt lgkmcnt(0)\n"
    "s_barrier\n"
    "ds_read_b128 v[64:67], v11\n"
    "ds_read_b128 v[68:71], v11 offset:1024\n"
    "ds_read_b128 v[72:75], v11 offset:2048\n"
    "ds_read_b128 v[76:79], v11 offset:3072\n"
    "s_waitcnt vmcnt(14)\n"
    "ds_read_b64_tr_b16 v[16:17], v9\n"
    "ds_read_b64_tr_b16 v[18:19], v9 offset:3584\n"
    "ds_read_b64_tr_b16 v[20:21], v9 offset:32\n"
    "ds_read_b64_tr_b16 v[22:23], v9 offset:3616\n"
    "ds_read_b64_tr_b16 v[24:25], v9 offset:64\n"
    "ds_read_b64_tr_b16 v[26:27], v9 offset:3648\n"
    "ds_read_b64_tr_b16 v[28:29], v9 offset:96\n"
    "ds_read_b64_tr_b16 v[30:31], v9 offset:3680\n"
    "s_waitcnt lgkmcnt(6)\n"
    "v_mfma_f32_16x16x32_bf16 a[0:3], v[16:19], v[64:67], 0\n"
    "v_mfma_f32_16x16x32_bf16 a[4:7], v[16:19], v[68:71], 0\n"
    "ds_read_b128 v[80:83], v11 offset:4096\n"
    "v_lshlrev_b32 v0, 4, v126\n"
    "v_mfma_f32_16x16x32_bf16 a[8:11], v[16:19], v[72:75], 0\n"
    "v_add_u32 v0, 0x60, v0\n"
    "v_lshlrev_b32 v1, 4, v126\n"
    "v_mfma_f32_16x16x32_bf16 a[12:15], v[16:19], v[76:79], 0\n"
    "s_waitcnt lgkmcnt(5)\n"
    "ds_read_b64_tr_b16 v[16:17], v9 offset:128\n"
    "ds_read_b64_tr_b16 v[18:19], v9 offset:3712\n"
    "v_mfma_f32_16x16x32_bf16 a[16:19], v[20:23], v[64:67], 0\n"
    "v_add_u32 v1, 64, v1\n"
    "v_lshlrev_b32 v2, 4, v126\n"
    "v_mfma_f32_16x16x32_bf16 a[20:23], v[20:23], v[68:71], 0\n"
    "ds_read_b128 v[84:87], v11 offset:5120\n"
    "v_add_u32 v2, 32, v2\n"
    "v_mfma_f32_16x16x32_bf16 a[24:27], v[20:23], v[72:75], 0\n"
    "v_lshlrev_b32 v3, 4, v126\n"
    "v_add_u32 v3, 0, v3\n"
    "v_mfma_f32_16x16x32_bf16 a[28:31], v[20:23], v[76:79], 0\n"
    "s_waitcnt lgkmcnt(6)\n"
    "ds_read_b64_tr_b16 v[20:21], v9 offset:160\n"
    "ds_read_b64_tr_b16 v[22:23], v9 offset:3744\n"
    "v_mfma_f32_16x16x32_bf16 a[32:35], v[24:27], v[64:67], 0\n"
    "v_lshlrev_b32 v60, 4, v126\n"
    "v_add_u32 v60, 0x1060, v60\n"
    "v_mfma_f32_16x16x32_bf16 a[36:39], v[24:27], v[68:71], 0\n"
    "ds_read_b128 v[88:91], v11 offset:6144\n"
    "v_lshlrev_b32 v61, 4, v126\n"
    "v_mfma_f32_16x16x32_bf16 a[40:43], v[24:27], v[72:75], 0\n"
    "v_add_u32 v61, 0x1040, v61\n"
    "v_lshlrev_b32 v62, 4, v126\n"
    "v_mfma_f32_16x16x32_bf16 a[44:47], v[24:27], v[76:79], 0\n"
    "s_waitcnt lgkmcnt(7)\n"
    "ds_read_b64_tr_b16 v[24:25], v9 offset:192\n"
    "ds_read_b64_tr_b16 v[26:27], v9 offset:3776\n"
    "v_mfma_f32_16x16x32_bf16 a[48:51], v[28:31], v[64:67], 0\n"
    "v_add_u32 v62, 0x1020, v62\n"
    "v_lshlrev_b32 v63, 4, v126\n"
    "v_mfma_f32_16x16x32_bf16 a[52:55], v[28:31], v[68:71], 0\n"
    "ds_read_b128 v[92:95], v11 offset:7168\n"
    "v_add_u32 v63, 0x1000, v63\n"
    "v_mfma_f32_16x16x32_bf16 a[56:59], v[28:31], v[72:75], 0\n"
    "v_and_b32 v49, 15, v126\n"
    "v_lshrrev_b32 v50, 4, v126\n"
    "v_mfma_f32_16x16x32_bf16 a[60:63], v[28:31], v[76:79], 0\n"
    "s_waitcnt lgkmcnt(7)\n"
    "s_waitcnt vmcnt(7)\n"
    "ds_read_b64_tr_b16 v[28:29], v9 offset:33792\n"
    "ds_read_b64_tr_b16 v[30:31], v9 offset:37376\n"
    "v_mfma_f32_16x16x32_bf16 a[64:67], v[16:19], v[64:67], 0\n"
    "s_mul_i32 s29, s15, 0x4200\n"
    "v_mul_u32_u24 v4, 0x420, v49\n"
    "v_lshl_add_u32 v4, v50, 4, v4\n"
    "v_mfma_f32_16x16x32_bf16 a[68:71], v[16:19], v[68:71], 0\n"
    "v_add_u32 v4, s29, v4\n"
    "v_lshrrev_b32 v51, 2, v126\n"
    "v_mfma_f32_16x16x32_bf16 a[72:75], v[16:19], v[72:75], 0\n"
    "v_mul_u32_u24 v5, 0x420, v51\n"
    "v_and_b32 v51, 3, v126\n"
    "v_mfma_f32_16x16x32_bf16 a[76:79], v[16:19], v[76:79], 0\n"
    "s_waitcnt lgkmcnt(6)\n"
    "ds_read_b64_tr_b16 v[16:17], v9 offset:33824\n"
    "ds_read_b64_tr_b16 v[18:19], v9 offset:37408\n"
    "v_mfma_f32_16x16x32_bf16 a[80:83], v[20:23], v[64:67], 0\n"
    "v_lshl_add_u32 v5, v51, 3, v5\n"
    "v_add_u32 v5, s29, v5\n"
    "v_mfma_f32_16x16x32_bf16 a[84:87], v[20:23], v[68:71], 0\n"
    "v_add_u32 v5, 0x10800, v5\n"
    "s_lshl_b32 s28, s15, 8\n"
    "v_mfma_f32_16x16x32_bf16 a[88:91], v[20:23], v[72:75], 0\n"
    "v_subrev_u32 v6, s28, v5\n"
    "v_add_u32 v5, s28, v5\n"
    "v_mfma_f32_16x16x32_bf16 a[92:95], v[20:23], v[76:79], 0\n"
    "s_waitcnt lgkmcnt(5)\n"
    "ds_read_b64_tr_b16 v[20:21], v9 offset:33856\n"
    "ds_read_b64_tr_b16 v[22:23], v9 offset:37440\n"
    "v_mfma_f32_16x16x32_bf16 a[96:99], v[24:27], v[64:67], 0\n"
    "s_add_u32 m0, s78, 0\n"
    "s_mul_i32 s28, s14, 0x4200\n"
    "v_lshlrev_b32 v59, 4, v126\n"
    "v_mfma_f32_16x16x32_bf16 a[100:103], v[24:27], v[68:71], 0\n"
    "global_load_lds_dwordx4 v32, s[76:77]\n"
    "v_add_u32 v59, s28, v59\n"
    "v_mfma_f32_16x16x32_bf16 a[104:107], v[24:27], v[72:75], 0\n"
    "v_add_u32 v59, 0x10800, v59\n"
    "v_mov_b32 v58, s78\n"
    "v_mfma_f32_16x16x32_bf16 a[108:111], v[24:27], v[76:79], 0\n"
    "s_waitcnt lgkmcnt(4)\n"
    "ds_read_b64_tr_b16 v[24:25], v9 offset:33888\n"
    "ds_read_b64_tr_b16 v[26:27], v9 offset:37472\n"
    "v_mfma_f32_16x16x32_bf16 a[0:3], v[28:31], v[80:83], a[0:3]\n"
    "v_add_u32 v58, 0x20e0, v58\n"
    "v_cvt_pk_bf16_f32 v128, v128, v129\n"
    "v_mfma_f32_16x16x32_bf16 a[4:7], v[28:31], v[84:87], a[4:7]\n"
    "ds_read_b128 v[64:67], v11 offset:8192\n"
    "global_load_lds_dwordx4 v33, s[76:77] offset:1024\n"
    "v_mfma_f32_16x16x32_bf16 a[8:11], v[28:31], v[88:91], a[8:11]\n"
    "v_cvt_pk_bf16_f32 v129, v130, v131\n"
    "v_cvt_pk_bf16_f32 v136, v136, v137\n"
    "v_mfma_f32_16x16x32_bf16 a[12:15], v[28:31], v[92:95], a[12:15]\n"
    "s_waitcnt lgkmcnt(5)\n"
    "ds_read_b64_tr_b16 v[28:29], v9 offset:33920\n"
    "ds_read_b64_tr_b16 v[30:31], v9 offset:37504\n"
    "v_mfma_f32_16x16x32_bf16 a[16:19], v[16:19], v[80:83], a[16:19]\n"
    "ds_write_b64 v124, v[128:129]\n"
    "v_mfma_f32_16x16x32_bf16 a[20:23], v[16:19], v[84:87], a[20:23]\n"
    "ds_read_b128 v[68:71], v11 offset:9216\n"
    "global_load_lds_dwordx4 v34, s[76:77] offset:2048\n"
    "v_mfma_f32_16x16x32_bf16 a[24:27], v[16:19], v[88:91], a[24:27]\n"
    "v_cvt_pk_bf16_f32 v144, v144, v145\n"
    "v_cvt_pk_bf16_f32 v145, v146, v147\n"
    "v_mfma_f32_16x16x32_bf16 a[28:31], v[16:19], v[92:95], a[28:31]\n"
    "s_waitcnt lgkmcnt(7)\n"
    "ds_read_b64_tr_b16 v[16:17], v9 offset:33952\n"
    "ds_read_b64_tr_b16 v[18:19], v9 offset:37536\n"
    "v_mfma_f32_16x16x32_bf16 a[32:35], v[20:23], v[80:83], a[32:35]\n"
    "ds_write_b64 v124, v[144:145] offset:512\n"
    "v_mfma_f32_16x16x32_bf16 a[36:39], v[20:23], v[84:87], a[36:39]\n"
    "ds_read_b128 v[72:75], v11 offset:10240\n"
    "global_load_lds_dwordx4 v35, s[76:77] offset:3072\n"
    "v_mfma_f32_16x16x32_bf16 a[40:43], v[20:23], v[88:91], a[40:43]\n"
    "v_cvt_pk_bf16_f32 v160, v160, v161\n"
    "v_cvt_pk_bf16_f32 v161, v162, v163\n"
    "v_mfma_f32_16x16x32_bf16 a[44:47], v[20:23], v[92:95], a[44:47]\n"
    "s_waitcnt lgkmcnt(9)\n"
    "ds_read_b64_tr_b16 v[20:21], v9 offset:33984\n"
    "ds_read_b64_tr_b16 v[22:23], v9 offset:37568\n"
    "v_mfma_f32_16x16x32_bf16 a[48:51], v[24:27], v[80:83], a[48:51]\n"
    "s_add_u32 m0, s78, 0x1000\n"
    "ds_write_b64 v124, v[160:161] offset:1024\n"
    "v_mfma_f32_16x16x32_bf16 a[52:55], v[24:27], v[84:87], a[52:55]\n"
    "ds_read_b128 v[76:79], v11 offset:11264\n"
    "global_load_lds_dwordx4 v36, s[76:77]\n"
    "v_mfma_f32_16x16x32_bf16 a[56:59], v[24:27], v[88:91], a[56:59]\n"
    "v_cvt_pk_bf16_f32 v176, v176, v177\n"
    "v_cvt_pk_bf16_f32 v177, v178, v179\n"
    "v_mfma_f32_16x16x32_bf16 a[60:63], v[24:27], v[92:95], a[60:63]\n"
    "s_waitcnt lgkmcnt(10)\n"
    "s_waitcnt vmcnt(5)\n"
    "ds_read_b64_tr_b16 v[24:25], v10\n"
    "ds_read_b64_tr_b16 v[26:27], v10 offset:3584\n"
    "v_mfma_f32_16x16x32_bf16 a[64:67], v[28:31], v[80:83], a[64:67]\n"
    "ds_write_b64 v124, v[176:177] offset:1536\n"
    "v_mfma_f32_16x16x32_bf16 a[68:71], v[28:31], v[84:87], a[68:71]\n"
    "global_load_lds_dwordx4 v37, s[76:77] offset:1024\n"
    "v_cvt_pk_bf16_f32 v192, v192, v193\n"
    "v_mfma_f32_16x16x32_bf16 a[72:75], v[28:31], v[88:91], a[72:75]\n"
    "v_cvt_pk_bf16_f32 v193, v194, v195\n"
    "v_cvt_pk_bf16_f32 v137, v138, v139\n"
    "v_mfma_f32_16x16x32_bf16 a[76:79], v[28:31], v[92:95], a[76:79]\n"
    "s_waitcnt lgkmcnt(9)\n"
    "ds_read_b64_tr_b16 v[28:29], v10 offset:32\n"
    "ds_read_b64_tr_b16 v[30:31], v10 offset:3616\n"
    "v_mfma_f32_16x16x32_bf16 a[80:83], v[16:19], v[80:83], a[80:83]\n"
    "ds_write_b64 v124, v[192:193] offset:2048\n"
    "v_mfma_f32_16x16x32_bf16 a[84:87], v[16:19], v[84:87], a[84:87]\n"
    "global_load_lds_dwordx4 v38, s[76:77] offset:2048\n"
    "v_cvt_pk_bf16_f32 v208, v208, v209\n"
    "v_mfma_f32_16x16x32_bf16 a[88:91], v[16:19], v[88:91], a[88:91]\n"
    "s_add_u32 s76, s76, 0x7000\n"
    "s_addc_u32 s77, s77, 0\n"
    "v_cvt_pk_bf16_f32 v209, v210, v211\n"
    "v_mfma_f32_16x16x32_bf16 a[92:95], v[16:19], v[92:95], a[92:95]\n"
    "s_waitcnt lgkmcnt(8)\n"
    "ds_read_b64_tr_b16 v[16:17], v10 offset:64\n"
    "ds_read_b64_tr_b16 v[18:19], v10 offset:3648\n"
    "v_mfma_f32_16x16x32_bf16 a[96:99], v[20:23], v[80:83], a[96:99]\n"
    "s_add_u32 m0, s78, 0x8400\n"
    "ds_write_b64 v124, v[208:209] offset:2560\n"
    "v_mfma_f32_16x16x32_bf16 a[100:103], v[20:23], v[84:87], a[100:103]\n"
    "global_load_lds_dwordx4 v32, s[76:77]\n"
    "v_cvt_pk_bf16_f32 v224, v224, v225\n"
    "v_mfma_f32_16x16x32_bf16 a[104:107], v[20:23], v[88:91], a[104:107]\n"
    "v_cvt_pk_bf16_f32 v225, v226, v227\n"
    "v_cvt_pk_bf16_f32 v152, v152, v153\n"
    "v_mfma_f32_16x16x32_bf16 a[108:111], v[20:23], v[92:95], a[108:111]\n"
    "s_waitcnt lgkmcnt(7)\n"
    "ds_read_b64_tr_b16 v[20:21], v10 offset:96\n"
    "ds_read_b64_tr_b16 v[22:23], v10 offset:3680\n"
    "v_mfma_f32_16x16x32_bf16 a[0:3], v[24:27], v[64:67], a[0:3]\n"
    "ds_write_b64 v124, v[224:225] offset:3072\n"
    "v_mfma_f32_16x16x32_bf16 a[4:7], v[24:27], v[68:71], a[4:7]\n"
    "ds_read_b128 v[80:83], v11 offset:12288\n"
    "global_load_lds_dwordx4 v33, s[76:77] offset:1024\n"
    "v_mfma_f32_16x16x32_bf16 a[8:11], v[24:27], v[72:75], a[8:11]\n"
    "v_cvt_pk_bf16_f32 v240, v240, v241\n"
    "v_cvt_pk_bf16_f32 v241, v242, v243\n"
    "v_mfma_f32_16x16x32_bf16 a[12:15], v[24:27], v[76:79], a[12:15]\n"
    "s_waitcnt lgkmcnt(8)\n"
    "ds_read_b64_tr_b16 v[24:25], v10 offset:128\n"
    "ds_read_b64_tr_b16 v[26:27], v10 offset:3712\n"
    "v_mfma_f32_16x16x32_bf16 a[16:19], v[28:31], v[64:67], a[16:19]\n"
    "ds_write_b64 v124, v[240:241] offset:3584\n"
    "v_mfma_f32_16x16x32_bf16 a[20:23], v[28:31], v[68:71], a[20:23]\n"
    "ds_read_b128 v[84:87], v11 offset:13312\n"
    "global_load_lds_dwordx4 v34, s[76:77] offset:2048\n"
    "v_mfma_f32_16x16x32_bf16 a[24:27], v[28:31], v[72:75], a[24:27]\n"
    "v_cvt_pk_bf16_f32 v132, v132, v133\n"
    "v_cvt_pk_bf16_f32 v133, v134, v135\n"
    "v_mfma_f32_16x16x32_bf16 a[28:31], v[28:31], v[76:79], a[28:31]\n"
    "s_waitcnt lgkmcnt(9)\n"
    "ds_read_b64_tr_b16 v[28:29], v10 offset:160\n"
    "ds_read_b64_tr_b16 v[30:31], v10 offset:3744\n"
    "v_mfma_f32_16x16x32_bf16 a[32:35], v[16:19], v[64:67], a[32:35]\n"
    "ds_write_b64 v124, v[132:133] offset:4096\n"
    "v_mfma_f32_16x16x32_bf16 a[36:39], v[16:19], v[68:71], a[36:39]\n"
    "ds_read_b128 v[88:91], v11 offset:14336\n"
    "global_load_lds_dwordx4 v35, s[76:77] offset:3072\n"
    "v_mfma_f32_16x16x32_bf16 a[40:43], v[16:19], v[72:75], a[40:43]\n"
    "v_cvt_pk_bf16_f32 v148, v148, v149\n"
    "v_cvt_pk_bf16_f32 v149, v150, v151\n"
    "v_mfma_f32_16x16x32_bf16 a[44:47], v[16:19], v[76:79], a[44:47]\n"
    "s_waitcnt lgkmcnt(10)\n"
    "ds_read_b64_tr_b16 v[16:17], v10 offset:192\n"
    "ds_read_b64_tr_b16 v[18:19], v10 offset:3776\n"
    "v_mfma_f32_16x16x32_bf16 a[48:51], v[20:23], v[64:67], a[48:51]\n"
    "s_add_u32 m0, s78, 0x9400\n"
    "ds_write_b64 v124, v[148:149] offset:4608\n"
    "v_mfma_f32_16x16x32_bf16 a[52:55], v[20:23], v[68:71], a[52:55]\n"
    "ds_read_b128 v[92:95], v11 offset:15360\n"
    "global_load_lds_dwordx4 v36, s[76:77]\n"
    "v_mfma_f32_16x16x32_bf16 a[56:59], v[20:23], v[72:75], a[56:59]\n"
    "v_cvt_pk_bf16_f32 v164, v164, v165\n"
    "v_cvt_pk_bf16_f32 v165, v166, v167\n"
    "v_mfma_f32_16x16x32_bf16 a[60:63], v[20:23], v[76:79], a[60:63]\n"
    "s_waitcnt lgkmcnt(10)\n"
    "s_waitcnt vmcnt(5)\n"
    "ds_read_b64_tr_b16 v[20:21], v9\n"
    "ds_read_b64_tr_b16 v[22:23], v9 offset:3584\n"
    "v_mfma_f32_16x16x32_bf16 a[64:67], v[24:27], v[64:67], a[64:67]\n"
    "ds_write_b64 v124, v[164:165] offset:5120\n"
    "v_mfma_f32_16x16x32_bf16 a[68:71], v[24:27], v[68:71], a[68:71]\n"
    "global_load_lds_dwordx4 v37, s[76:77] offset:1024\n"
    "v_cvt_pk_bf16_f32 v180, v180, v181\n"
    "v_mfma_f32_16x16x32_bf16 a[72:75], v[24:27], v[72:75], a[72:75]\n"
    "v_cvt_pk_bf16_f32 v181, v182, v183\n"
    "v_cvt_pk_bf16_f32 v153, v154, v155\n"
    "v_mfma_f32_16x16x32_bf16 a[76:79], v[24:27], v[76:79], a[76:79]\n"
    "s_waitcnt lgkmcnt(9)\n"
    "ds_read_b64_tr_b16 v[24:25], v9 offset:32\n"
    "ds_read_b64_tr_b16 v[26:27], v9 offset:3616\n"
    "v_mfma_f32_16x16x32_bf16 a[80:83], v[28:31], v[64:67], a[80:83]\n"
    "ds_write_b64 v124, v[180:181] offset:5632\n"
    "v_mfma_f32_16x16x32_bf16 a[84:87], v[28:31], v[68:71], a[84:87]\n"
    "global_load_lds_dwordx4 v38, s[76:77] offset:2048\n"
    "v_cvt_pk_bf16_f32 v196, v196, v197\n"
    "v_mfma_f32_16x16x32_bf16 a[88:91], v[28:31], v[72:75], a[88:91]\n"
    "s_add_u32 s76, s76, 0x7000\n"
    "s_addc_u32 s77, s77, 0\n"
    "v_cvt_pk_bf16_f32 v197, v198, v199\n"
    "v_mfma_f32_16x16x32_bf16 a[92:95], v[28:31], v[76:79], a[92:95]\n"
    "s_waitcnt lgkmcnt(8)\n"
    "ds_read_b64_tr_b16 v[28:29], v9 offset:64\n"
    "ds_read_b64_tr_b16 v[30:31], v9 offset:3648\n"
    "v_mfma_f32_16x16x32_bf16 a[96:99], v[16:19], v[64:67], a[96:99]\n"
    "s_add_u32 m0, s78, 0x10800\n"
    "ds_write_b64 v124, v[196:197] offset:6144\n"
    "v_mfma_f32_16x16x32_bf16 a[100:103], v[16:19], v[68:71], a[100:103]\n"
    "global_load_lds_dwordx4 v32, s[76:77]\n"
    "v_cvt_pk_bf16_f32 v212, v212, v213\n"
    "v_mfma_f32_16x16x32_bf16 a[104:107], v[16:19], v[72:75], a[104:107]\n"
    "v_cvt_pk_bf16_f32 v213, v214, v215\n"
    "v_cvt_pk_bf16_f32 v168, v168, v169\n"
    "v_mfma_f32_16x16x32_bf16 a[108:111], v[16:19], v[76:79], a[108:111]\n"
    "s_waitcnt lgkmcnt(7)\n"
    "ds_read_b64_tr_b16 v[16:17], v9 offset:96\n"
    "ds_read_b64_tr_b16 v[18:19], v9 offset:3680\n"
    "v_mfma_f32_16x16x32_bf16 a[0:3], v[20:23], v[80:83], a[0:3]\n"
    "ds_write_b64 v124, v[212:213] offset:6656\n"
    "v_mfma_f32_16x16x32_bf16 a[4:7], v[20:23], v[84:87], a[4:7]\n"
    "ds_read_b128 v[64:67], v11 offset:16384\n"
    "global_load_lds_dwordx4 v33, s[76:77] offset:1024\n"
    "v_mfma_f32_16x16x32_bf16 a[8:11], v[20:23], v[88:91], a[8:11]\n"
    "v_cvt_pk_bf16_f32 v228, v228, v229\n"
    "v_cvt_pk_bf16_f32 v229, v230, v231\n"
    "v_mfma_f32_16x16x32_bf16 a[12:15], v[20:23], v[92:95], a[12:15]\n"
    "s_waitcnt lgkmcnt(8)\n"
    "ds_read_b64_tr_b16 v[20:21], v9 offset:128\n"
    "ds_read_b64_tr_b16 v[22:23], v9 offset:3712\n"
    "v_mfma_f32_16x16x32_bf16 a[16:19], v[24:27], v[80:83], a[16:19]\n"
    "ds_write_b64 v124, v[228:229] offset:7168\n"
    "v_mfma_f32_16x16x32_bf16 a[20:23], v[24:27], v[84:87], a[20:23]\n"
    "ds_read_b128 v[68:71], v11 offset:17408\n"
    "global_load_lds_dwordx4 v34, s[76:77] offset:2048\n"
    "v_mfma_f32_16x16x32_bf16 a[24:27], v[24:27], v[88:91], a[24:27]\n"
    "v_cvt_pk_bf16_f32 v244, v244, v245\n"
    "v_cvt_pk_bf16_f32 v245, v246, v247\n"
    "v_mfma_f32_16x16x32_bf16 a[28:31], v[24:27], v[92:95], a[28:31]\n"
    "s_waitcnt lgkmcnt(9)\n"
    "ds_read_b64_tr_b16 v[24:25], v9 offset:160\n"
    "ds_read_b64_tr_b16 v[26:27], v9 offset:3744\n"
    "v_mfma_f32_16x16x32_bf16 a[32:35], v[28:31], v[80:83], a[32:35]\n"
    "ds_write_b64 v124, v[244:245] offset:7680\n"
    "v_mfma_f32_16x16x32_bf16 a[36:39], v[28:31], v[84:87], a[36:39]\n"
    "ds_read_b128 v[72:75], v11 offset:18432\n"
    "global_load_lds_dwordx4 v35, s[76:77] offset:3072\n"
    "v_mfma_f32_16x16x32_bf16 a[40:43], v[28:31], v[88:91], a[40:43]\n"
    "v_cvt_pk_bf16_f32 v169, v170, v171\n"
    "v_cvt_pk_bf16_f32 v184, v184, v185\n"
    "v_mfma_f32_16x16x32_bf16 a[44:47], v[28:31], v[92:95], a[44:47]\n"
    "s_waitcnt lgkmcnt(10)\n"
    "ds_read_b64_tr_b16 v[28:29], v9 offset:192\n"
    "ds_read_b64_tr_b16 v[30:31], v9 offset:3776\n"
    "v_mfma_f32_16x16x32_bf16 a[48:51], v[16:19], v[80:83], a[48:51]\n"
    "s_add_u32 m0, s78, 0x11800\n"
    "v_cvt_pk_bf16_f32 v185, v186, v187\n"
    "v_cvt_pk_bf16_f32 v200, v200, v201\n"
    "v_mfma_f32_16x16x32_bf16 a[52:55], v[16:19], v[84:87], a[52:55]\n"
    "ds_read_b128 v[76:79], v11 offset:19456\n"
    "global_load_lds_dwordx4 v36, s[76:77]\n"
    "v_mfma_f32_16x16x32_bf16 a[56:59], v[16:19], v[88:91], a[56:59]\n"
    "v_cvt_pk_bf16_f32 v201, v202, v203\n"
    "v_cvt_pk_bf16_f32 v216, v216, v217\n"
    "v_mfma_f32_16x16x32_bf16 a[60:63], v[16:19], v[92:95], a[60:63]\n"
    "s_waitcnt lgkmcnt(9)\n"
    "s_waitcnt vmcnt(5)\n"
    "ds_read_b64_tr_b16 v[16:17], v9 offset:33792\n"
    "ds_read_b64_tr_b16 v[18:19], v9 offset:37376\n"
    "v_mfma_f32_16x16x32_bf16 a[64:67], v[20:23], v[80:83], a[64:67]\n"
    "v_cvt_pk_bf16_f32 v217, v218, v219\n"
    "v_cvt_pk_bf16_f32 v232, v232, v233\n"
    "v_mfma_f32_16x16x32_bf16 a[68:71], v[20:23], v[84:87], a[68:71]\n"
    "global_load_lds_dwordx4 v37, s[76:77] offset:1024\n"
    "v_cvt_pk_bf16_f32 v233, v234, v235\n"
    "v_mfma_f32_16x16x32_bf16 a[72:75], v[20:23], v[88:91], a[72:75]\n"
    "v_cvt_pk_bf16_f32 v248, v248, v249\n"
    "v_cvt_pk_bf16_f32 v249, v250, v251\n"
    "v_mfma_f32_16x16x32_bf16 a[76:79], v[20:23], v[92:95], a[76:79]\n"
    "s_waitcnt lgkmcnt(7)\n"
    "ds_read_b64_tr_b16 v[20:21], v9 offset:33824\n"
    "ds_read_b64_tr_b16 v[22:23], v9 offset:37408\n"
    "v_mfma_f32_16x16x32_bf16 a[80:83], v[24:27], v[80:83], a[80:83]\n"
    "v_cvt_pk_bf16_f32 v140, v140, v141\n"
    "v_cvt_pk_bf16_f32 v141, v142, v143\n"
    "v_mfma_f32_16x16x32_bf16 a[84:87], v[24:27], v[84:87], a[84:87]\n"
    "global_load_lds_dwordx4 v38, s[76:77] offset:2048\n"
    "v_cvt_pk_bf16_f32 v156, v156, v157\n"
    "v_mfma_f32_16x16x32_bf16 a[88:91], v[24:27], v[88:91], a[88:91]\n"
    "s_add_u32 s76, s76, 0x7000\n"
    "s_addc_u32 s77, s77, 0\n"
    "v_cvt_pk_bf16_f32 v157, v158, v159\n"
    "v_mfma_f32_16x16x32_bf16 a[92:95], v[24:27], v[92:95], a[92:95]\n"
    "s_waitcnt lgkmcnt(5)\n"
    "ds_read_b64_tr_b16 v[24:25], v9 offset:33856\n"
    "ds_read_b64_tr_b16 v[26:27], v9 offset:37440\n"
    "v_mfma_f32_16x16x32_bf16 a[96:99], v[28:31], v[80:83], a[96:99]\n"
    "s_add_u32 m0, s78, 0\n"
    "v_cvt_pk_bf16_f32 v172, v172, v173\n"
    "v_cvt_pk_bf16_f32 v173, v174, v175\n"
    "v_mfma_f32_16x16x32_bf16 a[100:103], v[28:31], v[84:87], a[100:103]\n"
    "global_load_lds_dwordx4 v32, s[76:77]\n"
    "v_cvt_pk_bf16_f32 v188, v188, v189\n"
    "v_mfma_f32_16x16x32_bf16 a[104:107], v[28:31], v[88:91], a[104:107]\n"
    "v_cvt_pk_bf16_f32 v189, v190, v191\n"
    "v_cvt_pk_bf16_f32 v204, v204, v205\n"
    "v_mfma_f32_16x16x32_bf16 a[108:111], v[28:31], v[92:95], a[108:111]\n"
    "s_waitcnt lgkmcnt(4)\n"
    "ds_read_b64_tr_b16 v[28:29], v9 offset:33888\n"
    "ds_read_b64_tr_b16 v[30:31], v9 offset:37472\n"
    "v_mfma_f32_16x16x32_bf16 a[0:3], v[16:19], v[64:67], a[0:3]\n"
    "v_cvt_pk_bf16_f32 v205, v206, v207\n"
    "v_cvt_pk_bf16_f32 v220, v220, v221\n"
    "v_mfma_f32_16x16x32_bf16 a[4:7], v[16:19], v[68:71], a[4:7]\n"
    "ds_read_b128 v[80:83], v11 offset:20480\n"
    "global_load_lds_dwordx4 v33, s[76:77] offset:1024\n"
    "v_mfma_f32_16x16x32_bf16 a[8:11], v[16:19], v[72:75], a[8:11]\n"
    "v_cvt_pk_bf16_f32 v221, v222, v223\n"
    "v_cvt_pk_bf16_f32 v236, v236, v237\n"
    "v_mfma_f32_16x16x32_bf16 a[12:15], v[16:19], v[76:79], a[12:15]\n"
    "s_waitcnt lgkmcnt(5)\n"
    "ds_read_b64_tr_b16 v[16:17], v9 offset:33920\n"
    "ds_read_b64_tr_b16 v[18:19], v9 offset:37504\n"
    "v_mfma_f32_16x16x32_bf16 a[16:19], v[20:23], v[64:67], a[16:19]\n"
    "v_cvt_pk_bf16_f32 v237, v238, v239\n"
    "v_cvt_pk_bf16_f32 v252, v252, v253\n"
    "v_mfma_f32_16x16x32_bf16 a[20:23], v[20:23], v[68:71], a[20:23]\n"
    "ds_read_b128 v[84:87], v11 offset:21504\n"
    "global_load_lds_dwordx4 v34, s[76:77] offset:2048\n"
    "v_mfma_f32_16x16x32_bf16 a[24:27], v[20:23], v[72:75], a[24:27]\n"
    "v_cvt_pk_bf16_f32 v253, v254, v255\n"
    "ds_read_b128 a[112:115], v58\n"
    "v_mfma_f32_16x16x32_bf16 a[28:31], v[20:23], v[76:79], a[28:31]\n"
    "s_waitcnt lgkmcnt(7)\n"
    "ds_read_b64_tr_b16 v[20:21], v9 offset:33952\n"
    "ds_read_b64_tr_b16 v[22:23], v9 offset:37536\n"
    "v_mfma_f32_16x16x32_bf16 a[32:35], v[24:27], v[64:67], a[32:35]\n"
    "ds_read_b128 a[116:119], v58\n"
    "ds_read_b128 a[120:123], v58\n"
    "v_mfma_f32_16x16x32_bf16 a[36:39], v[24:27], v[68:71], a[36:39]\n"
    "ds_read_b128 v[88:91], v11 offset:22528\n"
    "global_load_lds_dwordx4 v35, s[76:77] offset:3072\n"
    "v_mfma_f32_16x16x32_bf16 a[40:43], v[24:27], v[72:75], a[40:43]\n"
    "ds_read_b128 a[124:127], v58\n"
    "ds_read_b128 a[128:131], v58\n"
    "v_mfma_f32_16x16x32_bf16 a[44:47], v[24:27], v[76:79], a[44:47]\n"
    "s_waitcnt lgkmcnt(12)\n"
    "ds_read_b64_tr_b16 v[24:25], v9 offset:33984\n"
    "ds_read_b64_tr_b16 v[26:27], v9 offset:37568\n"
    "v_mfma_f32_16x16x32_bf16 a[48:51], v[28:31], v[64:67], a[48:51]\n"
    "s_add_u32 m0, s78, 0x1000\n"
    "ds_read_b128 a[132:135], v58\n"
    "ds_read_b128 a[136:139], v58\n"
    "v_mfma_f32_16x16x32_bf16 a[52:55], v[28:31], v[68:71], a[52:55]\n"
    "ds_read_b128 v[92:95], v11 offset:23552\n"
    "global_load_lds_dwordx4 v36, s[76:77]\n"
    "v_mfma_f32_16x16x32_bf16 a[56:59], v[28:31], v[72:75], a[56:59]\n"
    "ds_read_b128 a[140:143], v58\n"
    "ds_read_b128 a[144:147], v58\n"
    "v_mfma_f32_16x16x32_bf16 a[60:63], v[28:31], v[76:79], a[60:63]\n"
    "s_waitcnt lgkmcnt(15)\n"
    "s_waitcnt vmcnt(5)\n"
    "ds_read_b64_tr_b16 v[28:29], v10\n"
    "ds_read_b64_tr_b16 v[30:31], v10 offset:3584\n"
    "v_mfma_f32_16x16x32_bf16 a[64:67], v[16:19], v[64:67], a[64:67]\n"
    "ds_read_b128 a[148:151], v58\n"
    "ds_read_b128 a[152:155], v58\n"
    "v_mfma_f32_16x16x32_bf16 a[68:71], v[16:19], v[68:71], a[68:71]\n"
    "global_load_lds_dwordx4 v37, s[76:77] offset:1024\n"
    "ds_read_b128 a[156:159], v58\n"
    "v_mfma_f32_16x16x32_bf16 a[72:75], v[16:19], v[72:75], a[72:75]\n"
    "ds_read_b128 a[160:163], v58\n"
    "ds_read_b128 a[164:167], v58\n"
    "v_mfma_f32_16x16x32_bf16 a[76:79], v[16:19], v[76:79], a[76:79]\n"
    "s_waitcnt lgkmcnt(15)\n"
    "ds_read_b64_tr_b16 v[16:17], v10 offset:32\n"
    "ds_read_b64_tr_b16 v[18:19], v10 offset:3616\n"
    "v_mfma_f32_16x16x32_bf16 a[80:83], v[20:23], v[64:67], a[80:83]\n"
    "ds_read_b128 a[168:171], v58\n"
    "ds_read_b128 a[172:175], v58\n"
    "v_mfma_f32_16x16x32_bf16 a[84:87], v[20:23], v[68:71], a[84:87]\n"
    "global_load_lds_dwordx4 v38, s[76:77] offset:2048\n"
    "ds_read_b128 a[176:179], v58\n"
    "v_mfma_f32_16x16x32_bf16 a[88:91], v[20:23], v[72:75], a[88:91]\n"
    "s_add_u32 s76, s76, 0x7000\n"
    "s_addc_u32 s77, s77, 0\n"
    "ds_read_b128 a[180:183], v58\n"
    "v_mfma_f32_16x16x32_bf16 a[92:95], v[20:23], v[76:79], a[92:95]\n"
    "s_waitcnt lgkmcnt(15)\n"
    "ds_read_b64_tr_b16 v[20:21], v10 offset:64\n"
    "ds_read_b64_tr_b16 v[22:23], v10 offset:3648\n"
    "v_mfma_f32_16x16x32_bf16 a[96:99], v[24:27], v[64:67], a[96:99]\n"
    "s_mov_b32 m0, s22\n"
    "ds_read_b128 a[184:187], v58\n"
    "ds_read_b128 a[188:191], v58\n"
    "v_mfma_f32_16x16x32_bf16 a[100:103], v[24:27], v[68:71], a[100:103]\n"
    "global_load_lds_dwordx4 v0, s[16:17]\n"
    "ds_read_b128 a[192:195], v58\n"
    "v_mfma_f32_16x16x32_bf16 a[104:107], v[24:27], v[72:75], a[104:107]\n"
    "ds_read_b128 a[196:199], v58\n"
    "ds_read_b128 a[200:203], v58\n"
    "v_mfma_f32_16x16x32_bf16 a[108:111], v[24:27], v[76:79], a[108:111]\n"
    "s_waitcnt lgkmcnt(15)\n"
    "ds_read_b64_tr_b16 v[24:25], v10 offset:96\n"
    "ds_read_b64_tr_b16 v[26:27], v10 offset:3680\n"
    "v_mfma_f32_16x16x32_bf16 a[0:3], v[28:31], v[80:83], a[0:3]\n"
    "ds_read_b128 a[204:207], v58\n"
    "ds_read_b128 a[208:211], v58\n"
    "v_mfma_f32_16x16x32_bf16 a[4:7], v[28:31], v[84:87], a[4:7]\n"
    "ds_read_b128 v[64:67], v11 offset:24576\n"
    "global_load_lds_dwordx4 v1, s[16:17] offset:1056\n"
    "v_mfma_f32_16x16x32_bf16 a[8:11], v[28:31], v[88:91], a[8:11]\n"
    "ds_read_b128 a[212:215], v58\n"
    "ds_read_b128 a[216:219], v58\n"
    "v_mfma_f32_16x16x32_bf16 a[12:15], v[28:31], v[92:95], a[12:15]\n"
    "s_waitcnt lgkmcnt(15)\n"
    "ds_read_b64_tr_b16 v[28:29], v10 offset:128\n"
    "ds_read_b64_tr_b16 v[30:31], v10 offset:3712\n"
    "v_mfma_f32_16x16x32_bf16 a[16:19], v[16:19], v[80:83], a[16:19]\n"
    "ds_read_b128 a[220:223], v58\n"
    "ds_read_b128 a[224:227], v58\n"
    "v_mfma_f32_16x16x32_bf16 a[20:23], v[16:19], v[84:87], a[20:23]\n"
    "ds_read_b128 v[68:71], v11 offset:25600\n"
    "global_load_lds_dwordx4 v2, s[16:17] offset:2112\n"
    "v_mfma_f32_16x16x32_bf16 a[24:27], v[16:19], v[88:91], a[24:27]\n"
    "ds_read_b128 a[228:231], v58\n"
    "ds_read_b128 a[232:235], v58\n"
    "v_mfma_f32_16x16x32_bf16 a[28:31], v[16:19], v[92:95], a[28:31]\n"
    "s_waitcnt lgkmcnt(15)\n"
    "ds_read_b64_tr_b16 v[16:17], v10 offset:160\n"
    "ds_read_b64_tr_b16 v[18:19], v10 offset:3744\n"
    "v_mfma_f32_16x16x32_bf16 a[32:35], v[20:23], v[80:83], a[32:35]\n"
    "ds_read_b128 a[236:239], v58\n"
    "ds_read_b128 a[240:243], v58\n"
    "v_mfma_f32_16x16x32_bf16 a[36:39], v[20:23], v[84:87], a[36:39]\n"
    "ds_read_b128 v[72:75], v11 offset:26624\n"
    "global_load_lds_dwordx4 v3, s[16:17] offset:3168\n"
    "v_mfma_f32_16x16x32_bf16 a[40:43], v[20:23], v[88:91], a[40:43]\n"
    "ds_read_b128 a[244:247], v58\n"
    "ds_read_b128 a[248:251], v58\n"
    "v_mfma_f32_16x16x32_bf16 a[44:47], v[20:23], v[92:95], a[44:47]\n"
    "s_waitcnt lgkmcnt(15)\n"
    "ds_read_b64_tr_b16 v[20:21], v10 offset:192\n"
    "ds_read_b64_tr_b16 v[22:23], v10 offset:3776\n"
    "v_mfma_f32_16x16x32_bf16 a[48:51], v[24:27], v[80:83], a[48:51]\n"
    "s_mov_b32 m0, s23\n"
    "ds_read_b128 a[252:255], v58\n"
    "v_mfma_f32_16x16x32_bf16 a[52:55], v[24:27], v[84:87], a[52:55]\n"
    "ds_read_b128 v[76:79], v11 offset:27648\n"
    "global_load_lds_dwordx4 v60, s[16:17]\n"
    "v_mfma_f32_16x16x32_bf16 a[56:59], v[24:27], v[88:91], a[56:59]\n"
    "v_mfma_f32_16x16x32_bf16 a[60:63], v[24:27], v[92:95], a[60:63]\n"
    "s_waitcnt lgkmcnt(15)\n"
    "s_waitcnt vmcnt(5)\n"
    "ds_read_b64_tr_b16 v[24:25], v9\n"
    "ds_read_b64_tr_b16 v[26:27], v9 offset:3584\n"
    "v_mfma_f32_16x16x32_bf16 a[64:67], v[28:31], v[80:83], a[64:67]\n"
    "v_mfma_f32_16x16x32_bf16 a[68:71], v[28:31], v[84:87], a[68:71]\n"
    "global_load_lds_dwordx4 v61, s[16:17] offset:1056\n"
    "v_mfma_f32_16x16x32_bf16 a[72:75], v[28:31], v[88:91], a[72:75]\n"
    "v_mfma_f32_16x16x32_bf16 a[76:79], v[28:31], v[92:95], a[76:79]\n"
    "s_waitcnt lgkmcnt(11)\n"
    "ds_read_b64_tr_b16 v[28:29], v9 offset:32\n"
    "ds_read_b64_tr_b16 v[30:31], v9 offset:3616\n"
    "v_mfma_f32_16x16x32_bf16 a[80:83], v[16:19], v[80:83], a[80:83]\n"
    "v_mfma_f32_16x16x32_bf16 a[84:87], v[16:19], v[84:87], a[84:87]\n"
    "global_load_lds_dwordx4 v62, s[16:17] offset:2112\n"
    "v_mfma_f32_16x16x32_bf16 a[88:91], v[16:19], v[88:91], a[88:91]\n"
    "v_mfma_f32_16x16x32_bf16 a[92:95], v[16:19], v[92:95], a[92:95]\n"
    "s_waitcnt lgkmcnt(6)\n"
    "ds_read_b64_tr_b16 v[16:17], v9 offset:64\n"
    "ds_read_b64_tr_b16 v[18:19], v9 offset:3648\n"
    "v_mfma_f32_16x16x32_bf16 a[96:99], v[20:23], v[80:83], a[96:99]\n"
    "ds_write_b64 v125, v[136:137]\n"
    "v_mfma_f32_16x16x32_bf16 a[100:103], v[20:23], v[84:87], a[100:103]\n"
    "global_load_lds_dwordx4 v63, s[16:17] offset:3168\n"
    "v_mfma_f32_16x16x32_bf16 a[104:107], v[20:23], v[88:91], a[104:107]\n"
    "s_add_u32 s16, s16, 0x8000\n"
    "s_addc_u32 s17, s17, 0\n"
    "v_mfma_f32_16x16x32_bf16 a[108:111], v[20:23], v[92:95], a[108:111]\n"
    "s_waitcnt lgkmcnt(5)\n"
    "ds_read_b64_tr_b16 v[20:21], v9 offset:96\n"
    "ds_read_b64_tr_b16 v[22:23], v9 offset:3680\n"
    "v_mfma_f32_16x16x32_bf16 a[0:3], v[24:27], v[64:67], a[0:3]\n"
    "ds_write_b64 v125, v[152:153] offset:512\n"
    "v_mfma_f32_16x16x32_bf16 a[4:7], v[24:27], v[68:71], a[4:7]\n"
    "ds_write_b64 v125, v[168:169] offset:1024\n"
    "v_mfma_f32_16x16x32_bf16 a[8:11], v[24:27], v[72:75], a[8:11]\n"
    "ds_write_b64 v125, v[184:185] offset:1536\n"
    "v_mfma_f32_16x16x32_bf16 a[12:15], v[24:27], v[76:79], a[12:15]\n"
    "s_waitcnt lgkmcnt(8)\n"
    "ds_read_b64_tr_b16 v[24:25], v9 offset:128\n"
    "ds_read_b64_tr_b16 v[26:27], v9 offset:3712\n"
    "v_mfma_f32_16x16x32_bf16 a[16:19], v[28:31], v[64:67], a[16:19]\n"
    "ds_write_b64 v125, v[200:201] offset:2048\n"
    "v_mfma_f32_16x16x32_bf16 a[20:23], v[28:31], v[68:71], a[20:23]\n"
    "ds_write_b64 v125, v[216:217] offset:2560\n"
    "v_mfma_f32_16x16x32_bf16 a[24:27], v[28:31], v[72:75], a[24:27]\n"
    "ds_write_b64 v125, v[232:233] offset:3072\n"
    "v_mfma_f32_16x16x32_bf16 a[28:31], v[28:31], v[76:79], a[28:31]\n"
    "s_waitcnt lgkmcnt(11)\n"
    "ds_read_b64_tr_b16 v[28:29], v9 offset:160\n"
    "ds_read_b64_tr_b16 v[30:31], v9 offset:3744\n"
    "v_mfma_f32_16x16x32_bf16 a[32:35], v[16:19], v[64:67], a[32:35]\n"
    "ds_write_b64 v125, v[248:249] offset:3584\n"
    "v_mfma_f32_16x16x32_bf16 a[36:39], v[16:19], v[68:71], a[36:39]\n"
    "ds_write_b64 v125, v[140:141] offset:4096\n"
    "v_mfma_f32_16x16x32_bf16 a[40:43], v[16:19], v[72:75], a[40:43]\n"
    "ds_write_b64 v125, v[156:157] offset:4608\n"
    "v_mfma_f32_16x16x32_bf16 a[44:47], v[16:19], v[76:79], a[44:47]\n"
    "s_waitcnt lgkmcnt(13)\n"
    "ds_read_b64_tr_b16 v[16:17], v9 offset:192\n"
    "ds_read_b64_tr_b16 v[18:19], v9 offset:3776\n"
    "v_mfma_f32_16x16x32_bf16 a[48:51], v[20:23], v[64:67], a[48:51]\n"
    "ds_write_b64 v125, v[172:173] offset:5120\n"
    "v_mfma_f32_16x16x32_bf16 a[52:55], v[20:23], v[68:71], a[52:55]\n"
    "ds_write_b64 v125, v[188:189] offset:5632\n"
    "v_mfma_f32_16x16x32_bf16 a[56:59], v[20:23], v[72:75], a[56:59]\n"
    "ds_write_b64 v125, v[204:205] offset:6144\n"
    "v_mfma_f32_16x16x32_bf16 a[60:63], v[20:23], v[76:79], a[60:63]\n"
    "s_waitcnt lgkmcnt(13)\n"
    "ds_write_b64 v125, v[220:221] offset:6656\n"
    "v_mfma_f32_16x16x32_bf16 a[64:67], v[24:27], v[64:67], a[64:67]\n"
    "ds_write_b64 v125, v[236:237] offset:7168\n"
    "v_mfma_f32_16x16x32_bf16 a[68:71], v[24:27], v[68:71], a[68:71]\n"
    "ds_write_b64 v125, v[252:253] offset:7680\n"
    "v_mfma_f32_16x16x32_bf16 a[72:75], v[24:27], v[72:75], a[72:75]\n"
    "v_mfma_f32_16x16x32_bf16 a[76:79], v[24:27], v[76:79], a[76:79]\n"
    "s_waitcnt lgkmcnt(11)\n"
    "v_mfma_f32_16x16x32_bf16 a[80:83], v[28:31], v[64:67], a[80:83]\n"
    "v_mfma_f32_16x16x32_bf16 a[84:87], v[28:31], v[68:71], a[84:87]\n"
    "v_mfma_f32_16x16x32_bf16 a[88:91], v[28:31], v[72:75], a[88:91]\n"
    "v_mfma_f32_16x16x32_bf16 a[92:95], v[28:31], v[76:79], a[92:95]\n"
    "s_waitcnt lgkmcnt(6)\n"
    "v_mfma_f32_16x16x32_bf16 a[96:99], v[16:19], v[64:67], a[96:99]\n"
    "v_mfma_f32_16x16x32_bf16 a[100:103], v[16:19], v[68:71], a[100:103]\n"
    "v_mfma_f32_16x16x32_bf16 a[104:107], v[16:19], v[72:75], a[104:107]\n"
    "v_mfma_f32_16x16x32_bf16 a[108:111], v[16:19], v[76:79], a[108:111]\n"
    "s_getpc_b64 s[40:41]\n"
    "L_pc0:\n"
    "s_add_u32 s40, s40, L_rareB-L_pc0\n"
    "s_addc_u32 s41, s41, 0\n"
    "s_mul_i32 s44, s13, 0x70000\n"
    "s_mul_i32 s45, s11, 0xe0\n"
    "s_add_u32 s44, s44, s45\n"
    "s_add_u32 s70, s56, s44\n"
    "s_addc_u32 s71, s57, 0\n"
    "s_sub_u32 s70, s70, 0xc00\n"
    "s_subb_u32 s71, s71, 0\n"
    "s_lshl_b32 s44, s13, 5\n"
    "s_add_u32 s44, s44, s12\n"
    "s_lshl_b32 s44, s44, 2\n"
    "s_add_u32 s44, s44, s11\n"
    "s_mul_i32 s44, s44, 0x3800\n"
    "s_add_u32 s44, s44, 0x1000000\n"
    "s_add_u32 s72, s68, s44\n"
    "s_addc_u32 s73, s69, 0\n"
    "s_mov_b32 s74, 0x2a0\n"
    "s_mul_i32 s75, s11, 0x1c00\n"
    "s_lshl_b32 s44, s12, 2\n"
    "s_add_u32 s44, s44, s11\n"
    "s_lshl_b32 s45, s44, 9\n"
    "s_lshl_b32 s46, s13, 20\n"
    "s_add_u32 s45, s45, s46\n"
    "s_add_u32 s45, s45, 0x40000\n"
    "s_add_u32 s34, s50, s45\n"
    "s_addc_u32 s35, s51, 0\n"
    "s_add_u32 s76, s52, s45\n"
    "s_addc_u32 s77, s53, 0\n"
    "s_min_u32 s44, s44, 0x6f\n"
    "s_lshl_b32 s45, s44, 12\n"
    "s_mul_i32 s46, s13, 0x70000\n"
    "s_add_u32 s45, s45, s46\n"
    "s_add_u32 s28, s56, s45\n"
    "s_addc_u32 s29, s57, 0\n"
    "v_and_b32 v56, 3, v126\n"
    "v_lshlrev_b32 v56, 7, v56\n"
    "s_lshl_b32 s44, s13, 5\n"
    "s_add_u32 s44, s44, s12\n"
    "s_mul_i32 s44, s44, 56\n"
    "s_mul_i32 s45, s11, 14\n"
    "s_add_u32 s44, s44, s45\n"
    "s_cmp_eq_u32 s11, 3\n"
    "s_cselect_b32 s45, 2, 1\n"
    "s_add_u32 s44, s44, 0x4000\n"
    "s_sub_u32 s44, s44, s45\n"
    "s_lshl_b32 s44, s44, 10\n"
    "s_add_u32 s60, s68, s44\n"
    "s_addc_u32 s61, s69, 0\n"
    "s_and_b32 s61, s61, 0xffff\n"
    "s_mov_b32 s62, 0x4000\n"
    "s_mov_b32 s63, 0x20000\n"
    "s_mov_b32 m0, s20\n"
    "s_nop 0\n"
    "global_load_lds_dwordx4 v0, s[16:17]\n"
    "global_load_lds_dwordx4 v1, s[16:17] offset:1056\n"
    "global_load_lds_dwordx4 v2, s[16:17] offset:2112\n"
    "global_load_lds_dwordx4 v3, s[16:17] offset:3168\n"
    "s_mov_b32 m0, s21\n"
    "s_nop 0\n"
    "global_load_lds_dwordx4 v60, s[16:17]\n"
    "global_load_lds_dwordx4 v61, s[16:17] offset:1056\n"
    "global_load_lds_dwordx4 v62, s[16:17] offset:2112\n"
    "global_load_lds_dwordx4 v63, s[16:17] offset:3168\n"
    "s_add_u32 s16, s16, 0x8000\n"
    "s_addc_u32 s17, s17, 0\n"
    "v_mov_b32 v8, 0\n"
    "v_mov_b32 v32, 0\n"
    "v_mov_b32 v33, 0\n"
    "v_mov_b32 v34, 0\n"
    "v_mov_b32 v35, 0\n"
    "v_mov_b32 v9, 0\n"
    "v_mov_b32 v36, 0\n"
    "v_mov_b32 v37, 0\n"
    "v_mov_b32 v38, 0\n"
    "v_mov_b32 v39, 0\n"
    "v_mov_b32 v10, 0\n"
    "v_mov_b32 v40, 0\n"
    "v_mov_b32 v41, 0\n"
    "v_mov_b32 v42, 0\n"
    "v_mov_b32 v43, 0\n"
    "v_mov_b32 v11, 0\n"
    "v_mov_b32 v44, 0\n"
    "v_mov_b32 v45, 0\n"
    "v_mov_b32 v46, 0\n"
    "v_mov_b32 v47, 0\n"
    "v_lshlrev_b32 v57, 4, v126\n"
    "s_waitcnt vmcnt(8) lgkmcnt(0)\n"
    "s_barrier\n"
    "ds_read_b128 v[128:131], v59 offset:33792\n"
    "ds_read_b128 v[160:163], v59 offset:37888\n"
    "ds_read_b128 v[192:195], v59\n"
    "ds_read_b128 v[224:227], v59 offset:4096\n"
    "ds_read_b128 v[16:19], v4 offset:33792\n"
    "ds_read_b128 v[20:23], v4 offset:34304\n"
    "ds_read_b128 v[132:135], v59 offset:34816\n"
    "ds_read_b128 v[164:167], v59 offset:38912\n"
    "ds_read_b128 v[196:199], v59 offset:1024\n"
    "ds_read_b128 v[228:231], v59 offset:5120\n"
    "ds_read_b128 v[24:27], v4 offset:33856\n"
    "ds_read_b128 v[28:31], v4 offset:34368\n"
    "s_waitcnt lgkmcnt(7)\n"
    "v_mfma_f32_16x16x32_bf16 v[64:67], v[16:19], v[128:131], v[32:35]\n"
    "ds_read_b128 v[136:139], v59 offset:35840\n"
    "ds_read_b128 v[168:171], v59 offset:39936\n"
    "v_mfma_f32_16x16x32_bf16 v[72:75], v[16:19], v[160:163], v[36:39]\n"
    "ds_read_b128 v[200:203], v59 offset:2048\n"
    "ds_read_b128 v[232:235], v59 offset:6144\n"
    "v_mfma_f32_16x16x32_bf16 v[80:83], v[16:19], v[192:195], v[40:43]\n"
    "ds_read_b128 v[140:143], v59 offset:36864\n"
    "ds_read_b128 v[172:175], v59 offset:40960\n"
    "v_mfma_f32_16x16x32_bf16 v[88:91], v[16:19], v[224:227], v[44:47]\n"
    "s_waitcnt lgkmcnt(12)\n"
    "ds_read_b128 v[204:207], v59 offset:3072\n"
    "ds_read_b128 v[236:239], v59 offset:7168\n"
    "ds_read_b128 v[16:19], v4 offset:33920\n"
    "v_mfma_f32_16x16x32_bf16 v[68:71], v[20:23], v[128:131], v[32:35]\n"
    "ds_read_b128 v[144:147], v59 offset:8448\n"
    "ds_read_b128 v[176:179], v59 offset:12544\n"
    "v_mfma_f32_16x16x32_bf16 v[76:79], v[20:23], v[160:163], v[36:39]\n"
    "ds_read_b128 v[208:211], v59 offset:42240\n"
    "ds_read_b128 v[240:243], v59 offset:46336\n"
    "v_mfma_f32_16x16x32_bf16 v[84:87], v[20:23], v[192:195], v[40:43]\n"
    "ds_read_b128 v[148:151], v59 offset:9472\n"
    "ds_read_b128 v[180:183], v59 offset:13568\n"
    "v_mfma_f32_16x16x32_bf16 v[92:95], v[20:23], v[224:227], v[44:47]\n"
    "s_waitcnt lgkmcnt(15)\n"
    "ds_read_b128 v[212:215], v59 offset:43264\n"
    "ds_read_b128 v[244:247], v59 offset:47360\n"
    "ds_read_b128 v[20:23], v4 offset:34432\n"
    "v_mfma_f32_16x16x32_bf16 v[64:67], v[24:27], v[132:135], v[64:67]\n"
    "ds_read_b128 v[152:155], v59 offset:10496\n"
    "ds_read_b128 v[184:187], v59 offset:14592\n"
    "v_accvgpr_read_b32 v96, a0\n"
    "v_accvgpr_read_b32 v97, a1\n"
    "v_accvgpr_read_b32 v98, a2\n"
    "v_accvgpr_read_b32 v99, a3\n"
    "v_accvgpr_read_b32 v100, a4\n"
    "v_mfma_f32_16x16x32_bf16 v[72:75], v[24:27], v[164:167], v[72:75]\n"
    "ds_read_b128 v[216:219], v59 offset:44288\n"
    "ds_read_b128 v[248:251], v59 offset:48384\n"
    "v_accvgpr_read_b32 v101, a5\n"
    "v_accvgpr_read_b32 v102, a6\n"
    "v_accvgpr_read_b32 v103, a7\n"
    "v_cvt_pk_bf16_f32 v112, v96, v97\n"
    "v_cvt_pk_bf16_f32 v113, v98, v99\n"
    "v_mfma_f32_16x16x32_bf16 v[80:83], v[24:27], v[196:199], v[80:83]\n"
    "ds_read_b128 v[156:159], v59 offset:11520\n"
    "ds_read_b128 v[188:191], v59 offset:15616\n"
    "v_cvt_pk_bf16_f32 v114, v100, v101\n"
    "v_cvt_pk_bf16_f32 v115, v102, v103\n"
    "v_mfma_f32_16x16x32_bf16 v[88:91], v[24:27], v[228:231], v[88:91]\n"
    "ds_read_b128 v[220:223], v59 offset:45312\n"
    "ds_read_b128 v[252:255], v59 offset:49408\n"
    "ds_read_b128 v[24:27], v4 offset:33984\n"
    "v_mfma_f32_16x16x32_bf16 v[68:71], v[28:31], v[132:135], v[68:71]\n"
    "v_mfma_f32_16x16x32_bf16 v[76:79], v[28:31], v[164:167], v[76:79]\n"
    "v_mfma_f32_16x16x32_bf16 v[84:87], v[28:31], v[196:199], v[84:87]\n"
    "s_waitcnt lgkmcnt(1)\n"
    "s_barrier\n"
    "v_mfma_f32_16x16x32_bf16 v[92:95], v[28:31], v[228:231], v[92:95]\n"
    "s_mov_b32 m0, s26\n"
    "ds_read_b128 v[28:31], v4 offset:34496\n"
    "v_mfma_f32_16x16x32_bf16 v[64:67], v[16:19], v[136:139], v[64:67]\n"
    "global_load_lds_dwordx4 v0, s[18:19]\n"
    "v_mfma_f32_16x16x32_bf16 v[72:75], v[16:19], v[168:171], v[72:75]\n"
    "global_load_lds_dwordx4 v1, s[18:19] offset:1056\n"
    "v_mfma_f32_16x16x32_bf16 v[80:83], v[16:19], v[200:203], v[80:83]\n"
    "global_load_lds_dwordx4 v2, s[18:19] offset:2112\n"
    "v_mfma_f32_16x16x32_bf16 v[88:91], v[16:19], v[232:235], v[88:91]\n"
    "ds_read_b128 v[16:19], v4 offset:34048\n"
    "global_load_lds_dwordx4 v3, s[18:19] offset:3168\n"
    "v_mfma_f32_16x16x32_bf16 v[68:71], v[20:23], v[136:139], v[68:71]\n"
    "s_mov_b32 m0, s27\n"
    "v_mfma_f32_16x16x32_bf16 v[76:79], v[20:23], v[168:171], v[76:79]\n"
    "global_load_lds_dwordx4 v60, s[18:19]\n"
    "v_mfma_f32_16x16x32_bf16 v[84:87], v[20:23], v[200:203], v[84:87]\n"
    "global_load_lds_dwordx4 v61, s[18:19] offset:1056\n"
    "v_mfma_f32_16x16x32_bf16 v[92:95], v[20:23], v[232:235], v[92:95]\n"
    "s_waitcnt lgkmcnt(2)\n"
    "ds_read_b128 v[20:23], v4 offset:34560\n"
    "global_load_lds_dwordx4 v62, s[18:19] offset:2112\n"
    "v_mfma_f32_16x16x32_bf16 v[64:67], v[24:27], v[140:143], v[64:67]\n"
    "global_load_lds_dwordx4 v63, s[18:19] offset:3168\n"
    "v_mfma_f32_16x16x32_bf16 v[72:75], v[24:27], v[172:175], v[72:75]\n"
    "s_add_u32 s18, s18, 0x8000\n"
    "s_addc_u32 s19, s19, 0\n"
    "global_store_dwordx4 v57, v[112:115], s[32:33] sc1\n"
    "v_accvgpr_read_b32 v104, a8\n"
    "v_mfma_f32_16x16x32_bf16 v[80:83], v[24:27], v[204:207], v[80:83]\n"
    "v_accvgpr_read_b32 v105, a9\n"
    "v_accvgpr_read_b32 v106, a10\n"
    "v_accvgpr_read_b32 v107, a11\n"
    "v_accvgpr_read_b32 v108, a12\n"
    "v_accvgpr_read_b32 v109, a13\n"
    "v_accvgpr_read_b32 v110, a14\n"
    "v_accvgpr_read_b32 v111, a15\n"
    "v_mfma_f32_16x16x32_bf16 v[88:91], v[24:27], v[236:239], v[88:91]\n"
    "s_waitcnt lgkmcnt(2)\n"
    "ds_read_b128 v[24:27], v4 offset:34112\n"
    "v_cvt_pk_bf16_f32 v116, v104, v105\n"
    "v_cvt_pk_bf16_f32 v117, v106, v107\n"
    "v_cvt_pk_bf16_f32 v118, v108, v109\n"
    "v_cvt_pk_bf16_f32 v119, v110, v111\n"
    "global_store_dwordx4 v57, v[116:119], s[32:33] offset:1024 sc1\n"
    "v_mfma_f32_16x16x32_bf16 v[68:71], v[28:31], v[140:143], v[68:71]\n"
    "v_accvgpr_read_b32 v96, a16\n"
    "v_accvgpr_read_b32 v97, a17\n"
    "v_accvgpr_read_b32 v98, a18\n"
    "v_accvgpr_read_b32 v99, a19\n"
    "v_accvgpr_read_b32 v100, a20\n"
    "v_accvgpr_read_b32 v101, a21\n"
    "v_accvgpr_read_b32 v102, a22\n"
    "v_mfma_f32_16x16x32_bf16 v[76:79], v[28:31], v[172:175], v[76:79]\n"
    "v_accvgpr_read_b32 v103, a23\n"
    "v_cvt_pk_bf16_f32 v112, v96, v97\n"
    "v_cvt_pk_bf16_f32 v113, v98, v99\n"
    "v_cvt_pk_bf16_f32 v114, v100, v101\n"
    "v_cvt_pk_bf16_f32 v115, v102, v103\n"
    "global_store_dwordx4 v57, v[112:115], s[32:33] offset:2048 sc1\n"
    "v_mfma_f32_16x16x32_bf16 v[84:87], v[28:31], v[204:207], v[84:87]\n"
    "v_accvgpr_read_b32 v104, a24\n"
    "v_accvgpr_read_b32 v105, a25\n"
    "v_accvgpr_read_b32 v106, a26\n"
    "v_accvgpr_read_b32 v107, a27\n"
    "v_accvgpr_read_b32 v108, a28\n"
    "v_accvgpr_read_b32 v109, a29\n"
    "v_accvgpr_read_b32 v110, a30\n"
    "v_mfma_f32_16x16x32_bf16 v[92:95], v[28:31], v[236:239], v[92:95]\n"
    "s_waitcnt lgkmcnt(2)\n"
    "ds_read_b128 v[28:31], v4 offset:34624\n"
    "v_accvgpr_read_b32 v111, a31\n"
    "v_cvt_pk_bf16_f32 v116, v104, v105\n"
    "v_cvt_pk_bf16_f32 v117, v106, v107\n"
    "v_cvt_pk_bf16_f32 v118, v108, v109\n"
    "v_cvt_pk_bf16_f32 v119, v110, v111\n"
    "v_mfma_f32_16x16x32_bf16 v[64:67], v[16:19], v[144:147], v[64:67]\n"
    "global_store_dwordx4 v57, v[116:119], s[32:33] offset:3072 sc1\n"
    "s_add_u32 s32, s32, 0x1000\n"
    "s_addc_u32 s33, s33, 0\n"
    "v_mfma_f32_16x16x32_bf16 v[72:75], v[16:19], v[176:179], v[72:75]\n"
    "v_accvgpr_read_b32 v96, a32\n"
    "v_accvgpr_read_b32 v97, a33\n"
    "v_accvgpr_read_b32 v98, a34\n"
    "v_accvgpr_read_b32 v99, a35\n"
    "v_accvgpr_read_b32 v100, a36\n"
    "v_accvgpr_read_b32 v101, a37\n"
    "v_accvgpr_read_b32 v102, a38\n"
    "v_mfma_f32_16x16x32_bf16 v[80:83], v[16:19], v[208:211], v[80:83]\n"
    "v_accvgpr_read_b32 v103, a39\n"
    "v_cvt_pk_bf16_f32 v112, v96, v97\n"
    "v_cvt_pk_bf16_f32 v113, v98, v99\n"
    "v_cvt_pk_bf16_f32 v114, v100, v101\n"
    "v_cvt_pk_bf16_f32 v115, v102, v103\n"
    "global_store_dwordx4 v57, v[112:115], s[32:33] sc1\n"
    "v_mfma_f32_16x16x32_bf16 v[88:91], v[16:19], v[240:243], v[88:91]\n"
    "s_waitcnt lgkmcnt(2)\n"
    "ds_read_b128 v[16:19], v4 offset:34176\n"
    "v_accvgpr_read_b32 v104, a40\n"
    "v_accvgpr_read_b32 v105, a41\n"
    "v_accvgpr_read_b32 v106, a42\n"
    "v_accvgpr_read_b32 v107, a43\n"
    "v_accvgpr_read_b32 v108, a44\n"
    "v_accvgpr_read_b32 v109, a45\n"
    "v_mfma_f32_16x16x32_bf16 v[68:71], v[20:23], v[144:147], v[68:71]\n"
    "v_accvgpr_read_b32 v110, a46\n"
    "v_accvgpr_read_b32 v111, a47\n"
    "v_cvt_pk_bf16_f32 v116, v104, v105\n"
    "v_cvt_pk_bf16_f32 v117, v106, v107\n"
    "v_cvt_pk_bf16_f32 v118, v108, v109\n"
    "v_cvt_pk_bf16_f32 v119, v110, v111\n"
    "v_mfma_f32_16x16x32_bf16 v[76:79], v[20:23], v[176:179], v[76:79]\n"
    "global_store_dwordx4 v57, v[116:119], s[32:33] offset:1024 sc1\n"
    "v_accvgpr_read_b32 v96, a48\n"
    "v_accvgpr_read_b32 v97, a49\n"
    "v_mfma_f32_16x16x32_bf16 v[84:87], v[20:23], v[208:211], v[84:87]\n"
    "v_accvgpr_read_b32 v98, a50\n"
    "v_accvgpr_read_b32 v99, a51\n"
    "v_accvgpr_read_b32 v100, a52\n"
    "v_accvgpr_read_b32 v101, a53\n"
    "v_accvgpr_read_b32 v102, a54\n"
    "v_accvgpr_read_b32 v103, a55\n"
    "v_cvt_pk_bf16_f32 v112, v96, v97\n"
    "v_mfma_f32_16x16x32_bf16 v[92:95], v[20:23], v[240:243], v[92:95]\n"
    "s_waitcnt lgkmcnt(2)\n"
    "ds_read_b128 v[20:23], v4 offset:34688\n"
    "v_cvt_pk_bf16_f32 v113, v98, v99\n"
    "v_cvt_pk_bf16_f32 v114, v100, v101\n"
    "v_cvt_pk_bf16_f32 v115, v102, v103\n"
    "global_store_dwordx4 v57, v[112:115], s[32:33] offset:2048 sc1\n"
    "v_mfma_f32_16x16x32_bf16 v[64:67], v[24:27], v[148:151], v[64:67]\n"
    "v_accvgpr_read_b32 v104, a56\n"
    "v_accvgpr_read_b32 v105, a57\n"
    "v_accvgpr_read_b32 v106, a58\n"
    "v_accvgpr_read_b32 v107, a59\n"
    "v_accvgpr_read_b32 v108, a60\n"
    "v_accvgpr_read_b32 v109, a61\n"
    "v_accvgpr_read_b32 v110, a62\n"
    "v_mfma_f32_16x16x32_bf16 v[72:75], v[24:27], v[180:183], v[72:75]\n"
    "v_accvgpr_read_b32 v111, a63\n"
    "v_cvt_pk_bf16_f32 v116, v104, v105\n"
    "v_cvt_pk_bf16_f32 v117, v106, v107\n"
    "v_cvt_pk_bf16_f32 v118, v108, v109\n"
    "v_cvt_pk_bf16_f32 v119, v110, v111\n"
    "global_store_dwordx4 v57, v[116:119], s[32:33] offset:3072 sc1\n"
    "v_mfma_f32_16x16x32_bf16 v[80:83], v[24:27], v[212:215], v[80:83]\n"
    "s_add_u32 s32, s32, 0x1000\n"
    "s_addc_u32 s33, s33, 0\n"
    "v_accvgpr_read_b32 v96, a64\n"
    "v_accvgpr_read_b32 v97, a65\n"
    "v_accvgpr_read_b32 v98, a66\n"
    "v_accvgpr_read_b32 v99, a67\n"
    "v_accvgpr_read_b32 v100, a68\n"
    "v_accvgpr_read_b32 v101, a69\n"
    "v_mfma_f32_16x16x32_bf16 v[88:91], v[24:27], v[244:247], v[88:91]\n"
    "s_waitcnt lgkmcnt(2)\n"
    "ds_read_b128 v[24:27], v4 offset:34240\n"
    "v_accvgpr_read_b32 v102, a70\n"
    "v_accvgpr_read_b32 v103, a71\n"
    "v_cvt_pk_bf16_f32 v112, v96, v97\n"
    "v_cvt_pk_bf16_f32 v113, v98, v99\n"
    "v_cvt_pk_bf16_f32 v114, v100, v101\n"
    "v_cvt_pk_bf16_f32 v115, v102, v103\n"
    "v_mfma_f32_16x16x32_bf16 v[68:71], v[28:31], v[148:151], v[68:71]\n"
    "global_store_dwordx4 v57, v[112:115], s[32:33] sc1\n"
    "v_accvgpr_read_b32 v104, a72\n"
    "v_accvgpr_read_b32 v105, a73\n"
    "v_mfma_f32_16x16x32_bf16 v[76:79], v[28:31], v[180:183], v[76:79]\n"
    "v_accvgpr_read_b32 v106, a74\n"
    "v_accvgpr_read_b32 v107, a75\n"
    "v_accvgpr_read_b32 v108, a76\n"
    "v_accvgpr_read_b32 v109, a77\n"
    "v_accvgpr_read_b32 v110, a78\n"
    "v_accvgpr_read_b32 v111, a79\n"
    "v_mfma_f32_16x16x32_bf16 v[84:87], v[28:31], v[212:215], v[84:87]\n"
    "v_cvt_pk_bf16_f32 v116, v104, v105\n"
    "v_cvt_pk_bf16_f32 v117, v106, v107\n"
    "v_cvt_pk_bf16_f32 v118, v108, v109\n"
    "v_cvt_pk_bf16_f32 v119, v110, v111\n"
    "global_store_dwordx4 v57, v[116:119], s[32:33] offset:1024 sc1\n"
    "v_mfma_f32_16x16x32_bf16 v[92:95], v[28:31], v[244:247], v[92:95]\n"
    "s_waitcnt lgkmcnt(2)\n"
    "ds_read_b128 v[28:31], v4 offset:34752\n"
    "v_accvgpr_read_b32 v96, a80\n"
    "v_accvgpr_read_b32 v97, a81\n"
    "v_accvgpr_read_b32 v98, a82\n"
    "v_accvgpr_read_b32 v99, a83\n"
    "v_accvgpr_read_b32 v100, a84\n"
    "v_mfma_f32_16x16x32_bf16 v[64:67], v[16:19], v[152:155], v[64:67]\n"
    "v_accvgpr_read_b32 v101, a85\n"
    "v_accvgpr_read_b32 v102, a86\n"
    "v_accvgpr_read_b32 v103, a87\n"
    "v_cvt_pk_bf16_f32 v112, v96, v97\n"
    "v_cvt_pk_bf16_f32 v113, v98, v99\n"
    "v_cvt_pk_bf16_f32 v114, v100, v101\n"
    "v_mfma_f32_16x16x32_bf16 v[72:75], v[16:19], v[184:187], v[72:75]\n"
    "v_cvt_pk_bf16_f32 v115, v102, v103\n"
    "global_store_dwordx4 v57, v[112:115], s[32:33] offset:2048 sc1\n"
    "v_mfma_f32_16x16x32_bf16 v[80:83], v[16:19], v[216:219], v[80:83]\n"
    "v_accvgpr_read_b32 v104, a88\n"
    "v_accvgpr_read_b32 v105, a89\n"
    "v_accvgpr_read_b32 v106, a90\n"
    "v_accvgpr_read_b32 v107, a91\n"
    "v_accvgpr_read_b32 v108, a92\n"
    "v_accvgpr_read_b32 v109, a93\n"
    "v_mfma_f32_16x16x32_bf16 v[88:91], v[16:19], v[248:251], v[88:91]\n"
    "s_waitcnt lgkmcnt(2)\n"
    "v_accvgpr_read_b32 v110, a94\n"
    "v_accvgpr_read_b32 v111, a95\n"
    "v_cvt_pk_bf16_f32 v116, v104, v105\n"
    "v_cvt_pk_bf16_f32 v117, v106, v107\n"
    "v_cvt_pk_bf16_f32 v118, v108, v109\n"
    "v_cvt_pk_bf16_f32 v119, v110, v111\n"
    "v_mfma_f32_16x16x32_bf16 v[68:71], v[20:23], v[152:155], v[68:71]\n"
    "global_store_dwordx4 v57, v[116:119], s[32:33] offset:3072 sc1\n"
    "s_add_u32 s32, s32, 0x1000\n"
    "v_mfma_f32_16x16x32_bf16 v[76:79], v[20:23], v[184:187], v[76:79]\n"
    "s_addc_u32 s33, s33, 0\n"
    "v_accvgpr_read_b32 v96, a96\n"
    "v_accvgpr_read_b32 v97, a97\n"
    "v_accvgpr_read_b32 v98, a98\n"
    "v_accvgpr_read_b32 v99, a99\n"
    "v_accvgpr_read_b32 v100, a100\n"
    "v_accvgpr_read_b32 v101, a101\n"
    "v_mfma_f32_16x16x32_bf16 v[84:87], v[20:23], v[216:219], v[84:87]\n"
    "v_accvgpr_read_b32 v102, a102\n"
    "v_accvgpr_read_b32 v103, a103\n"
    "v_cvt_pk_bf16_f32 v112, v96, v97\n"
    "v_cvt_pk_bf16_f32 v113, v98, v99\n"
    "v_cvt_pk_bf16_f32 v114, v100, v101\n"
    "v_cvt_pk_bf16_f32 v115, v102, v103\n"
    "v_mfma_f32_16x16x32_bf16 v[92:95], v[20:23], v[248:251], v[92:95]\n"
    "s_waitcnt lgkmcnt(1)\n"
    "global_store_dwordx4 v57, v[112:115], s[32:33] sc1\n"
    "v_accvgpr_read_b32 v104, a104\n"
    "v_mfma_f32_16x16x32_bf16 v[64:67], v[24:27], v[156:159], v[64:67]\n"
    "v_accvgpr_read_b32 v105, a105\n"
    "v_accvgpr_read_b32 v106, a106\n"
    "v_accvgpr_read_b32 v107, a107\n"
    "v_accvgpr_read_b32 v108, a108\n"
    "v_accvgpr_read_b32 v109, a109\n"
    "v_accvgpr_read_b32 v110, a110\n"
    "v_mfma_f32_16x16x32_bf16 v[72:75], v[24:27], v[188:191], v[72:75]\n"
    "v_accvgpr_read_b32 v111, a111\n"
    "v_cvt_pk_bf16_f32 v116, v104, v105\n"
    "v_cvt_pk_bf16_f32 v117, v106, v107\n"
    "v_cvt_pk_bf16_f32 v118, v108, v109\n"
    "v_cvt_pk_bf16_f32 v119, v110, v111\n"
    "v_mfma_f32_16x16x32_bf16 v[80:83], v[24:27], v[220:223], v[80:83]\n"
    "global_store_dwordx4 v57, v[116:119], s[32:33] offset:1024 sc1\n"
    "ds_read_b128 a[0:3], v58\n"
    "v_mfma_f32_16x16x32_bf16 v[88:91], v[24:27], v[252:255], v[88:91]\n"
    "s_waitcnt lgkmcnt(1)\n"
    "ds_read_b128 a[4:7], v58\n"
    "ds_read_b128 a[8:11], v58\n"
    "ds_read_b128 a[12:15], v58\n"
    "ds_read_b128 a[16:19], v58\n"
    "ds_read_b128 a[20:23], v58\n"
    "ds_read_b128 a[24:27], v58\n"
    "v_mfma_f32_16x16x32_bf16 v[68:71], v[28:31], v[156:159], v[68:71]\n"
    "ds_read_b128 a[28:31], v58\n"
    "ds_read_b128 a[32:35], v58\n"
    "ds_read_b128 a[36:39], v58\n"
    "ds_read_b128 a[40:43], v58\n"
    "ds_read_b128 a[44:47], v58\n"
    "ds_read_b128 a[48:51], v58\n"
    "v_mfma_f32_16x16x32_bf16 v[76:79], v[28:31], v[188:191], v[76:79]\n"
    "ds_read_b128 a[52:55], v58\n"
    "ds_read_b128 a[56:59], v58\n"
    "ds_read_b128 a[60:63], v58\n"
    "ds_read_b128 a[64:67], v58\n"
    "ds_read_b128 a[68:71], v58\n"
    "ds_read_b128 a[72:75], v58\n"
    "v_mfma_f32_16x16x32_bf16 v[84:87], v[28:31], v[220:223], v[84:87]\n"
    "ds_read_b128 a[76:79], v58\n"
    "ds_read_b128 a[80:83], v58\n"
    "ds_read_b128 a[84:87], v58\n"
    "ds_read_b128 a[88:91], v58\n"
    "ds_read_b128 a[92:95], v58\n"
    "ds_read_b128 a[96:99], v58\n"
    "v_mfma_f32_16x16x32_bf16 v[92:95], v[28:31], v[252:255], v[92:95]\n"
    "ds_read_b128 a[100:103], v58\n"
    "ds_read_b128 a[104:107], v58\n"
    "ds_read_b128 a[108:111], v58\n"
    "v_max3_f32 v48, v64, v65, v66\n"
    "v_max3_f32 v49, v72, v73, v74\n"
    "v_max3_f32 v50, v80, v81, v82\n"
    "v_max3_f32 v51, v88, v89, v90\n"
    "v_max3_f32 v48, v48, v67, v68\n"
    "v_max3_f32 v49, v49, v75, v76\n"
    "v_max3_f32 v50, v50, v83, v84\n"
    "v_max3_f32 v51, v51, v91, v92\n"
    "v_max3_f32 v48, v48, v69, v70\n"
    "v_max3_f32 v49, v49, v77, v78\n"
    "v_max3_f32 v50, v50, v85, v86\n"
    "v_max3_f32 v51, v51, v93, v94\n"
    "v_max_f32 v48, v48, v71\n"
    "v_max_f32 v49, v49, v79\n"
    "v_max_f32 v50, v50, v87\n"
    "v_max_f32 v51, v51, v95\n"
    "v_max3_f32 v7, v48, v49, v50\n"
    "v_min3_f32 v52, v48, v49, v50\n"
    "v_max_f32 v7, v7, v51\n"
    "v_min_f32 v52, v52, v51\n"
    "v_cmp_lt_f32 vcc, s38, v7\n"
    "v_cmp_gt_f32_e64 s[46:47], s39, v52\n"
    "s_or_b64 vcc, vcc, s[46:47]\n"
    "s_cbranch_vccnz L_rareA_p0\n"
    "L_rareA_ret_p0:\n"
    "s_waitcnt vmcnt(14) lgkmcnt(0)\n"
    "s_barrier\n"
    "ds_read_b128 v[16:19], v4\n"
    "s_mov_b32 m0, s22\n"
    "ds_read_b128 v[20:23], v4 offset:512\n"
    "global_load_lds_dwordx4 v0, s[16:17]\n"
    "ds_read_b128 v[24:27], v4 offset:64\n"
    "global_load_lds_dwordx4 v1, s[16:17] offset:1056\n"
    "s_waitcnt lgkmcnt(2)\n"
    "ds_read_b128 v[28:31], v4 offset:576\n"
    "v_mfma_f32_16x16x32_bf16 v[96:99], v[16:19], v[128:131], v[32:35]\n"
    "v_exp_f32 v64, v64\n"
    "v_mfma_f32_16x16x32_bf16 v[104:107], v[16:19], v[160:163], v[36:39]\n"
    "global_load_lds_dwordx4 v2, s[16:17] offset:2112\n"
    "v_add_f32 v8, v8, v64\n"
    "v_mfma_f32_16x16x32_bf16 v[112:115], v[16:19], v[192:195], v[40:43]\n"
    "v_exp_f32 v65, v65\n"
    "v_mfma_f32_16x16x32_bf16 v[120:123], v[16:19], v[224:227], v[44:47]\n"
    "s_waitcnt lgkmcnt(2)\n"
    "ds_read_b128 v[16:19], v4 offset:128\n"
    "v_add_f32 v8, v8, v65\n"
    "v_mfma_f32_16x16x32_bf16 v[100:103], v[20:23], v[128:131], v[32:35]\n"
    "v_exp_f32 v66, v66\n"
    "v_mfma_f32_16x16x32_bf16 v[108:111], v[20:23], v[160:163], v[36:39]\n"
    "global_load_lds_dwordx4 v3, s[16:17] offset:3168\n"
    "v_cvt_pk_bf16_f32 v64, v64, v65\n"
    "v_mfma_f32_16x16x32_bf16 v[116:119], v[20:23], v[192:195], v[40:43]\n"
    "v_exp_f32 v67, v67\n"
    "v_mfma_f32_16x16x32_bf16 v[124:127], v[20:23], v[224:227], v[44:47]\n"
    "s_waitcnt lgkmcnt(2)\n"
    "ds_read_b128 v[20:23], v4 offset:640\n"
    "v_add_f32 v8, v8, v66\n"
    "v_mfma_f32_16x16x32_bf16 v[96:99], v[24:27], v[132:135], v[96:99]\n"
    "s_mov_b32 m0, s23\n"
    "v_exp_f32 v68, v68\n"
    "v_mfma_f32_16x16x32_bf16 v[104:107], v[24:27], v[164:167], v[104:107]\n"
    "global_load_lds_dwordx4 v60, s[16:17]\n"
    "v_add_f32 v8, v8, v67\n"
    "v_mfma_f32_16x16x32_bf16 v[112:115], v[24:27], v[196:199], v[112:115]\n"
    "v_exp_f32 v69, v69\n"
    "v_mfma_f32_16x16x32_bf16 v[120:123], v[24:27], v[228:231], v[120:123]\n"
    "s_waitcnt lgkmcnt(2)\n"
    "ds_read_b128 v[24:27], v4 offset:192\n"
    "v_cvt_pk_bf16_f32 v65, v66, v67\n"
    "v_mfma_f32_16x16x32_bf16 v[100:103], v[28:31], v[132:135], v[100:103]\n"
    "v_exp_f32 v70, v70\n"
    "v_mfma_f32_16x16x32_bf16 v[108:111], v[28:31], v[164:167], v[108:111]\n"
    "global_load_lds_dwordx4 v61, s[16:17] offset:1056\n"
    "v_add_f32 v8, v8, v68\n"
    "v_mfma_f32_16x16x32_bf16 v[116:119], v[28:31], v[196:199], v[116:119]\n"
    "v_exp_f32 v71, v71\n"
    "v_mfma_f32_16x16x32_bf16 v[124:127], v[28:31], v[228:231], v[124:127]\n"
    "s_waitcnt lgkmcnt(2)\n"
    "ds_read_b128 v[28:31], v4 offset:704\n"
    "v_add_f32 v8, v8, v69\n"
    "v_mfma_f32_16x16x32_bf16 v[96:99], v[16:19], v[136:139], v[96:99]\n"
    "v_exp_f32 v72, v72\n"
    "v_mfma_f32_16x16x32_bf16 v[104:107], v[16:19], v[168:171], v[104:107]\n"
    "global_load_lds_dwordx4 v62, s[16:17] offset:2112\n"
    "v_cvt_pk_bf16_f32 v66, v68, v69\n"
    "v_mfma_f32_16x16x32_bf16 v[112:115], v[16:19], v[200:203], v[112:115]\n"
    "v_exp_f32 v73, v73\n"
    "v_mfma_f32_16x16x32_bf16 v[120:123], v[16:19], v[232:235], v[120:123]\n"
    "s_waitcnt lgkmcnt(2)\n"
    "ds_read_b128 v[16:19], v4 offset:256\n"
    "v_add_f32 v8, v8, v70\n"
    "v_mfma_f32_16x16x32_bf16 v[100:103], v[20:23], v[136:139], v[100:103]\n"
    "v_exp_f32 v74, v74\n"
    "v_mfma_f32_16x16x32_bf16 v[108:111], v[20:23], v[168:171], v[108:111]\n"
    "global_load_lds_dwordx4 v63, s[16:17] offset:3168\n"
    "v_add_f32 v8, v8, v71\n"
    "v_mfma_f32_16x16x32_bf16 v[116:119], v[20:23], v[200:203], v[116:119]\n"
    "s_add_u32 s16, s16, 0x8000\n"
    "s_addc_u32 s17, s17, 0\n"
    "v_cvt_pk_bf16_f32 v67, v70, v71\n"
    "v_mfma_f32_16x16x32_bf16 v[124:127], v[20:23], v[232:235], v[124:127]\n"
    "s_waitcnt lgkmcnt(2)\n"
    "ds_read_b128 v[20:23], v4 offset:768\n"
    "v_add_f32 v9, v9, v72\n"
    "v_mfma_f32_16x16x32_bf16 v[96:99], v[24:27], v[140:143], v[96:99]\n"
    "s_mov_b32 m0, s24\n"
    "v_exp_f32 v75, v75\n"
    "v_mfma_f32_16x16x32_bf16 v[104:107], v[24:27], v[172:175], v[104:107]\n"
    "global_load_lds_dwordx4 v0, s[18:19]\n"
    "v_add_f32 v9, v9, v73\n"
    "v_mfma_f32_16x16x32_bf16 v[112:115], v[24:27], v[204:207], v[112:115]\n"
    "v_exp_f32 v76, v76\n"
    "v_mfma_f32_16x16x32_bf16 v[120:123], v[24:27], v[236:239], v[120:123]\n"
    "s_waitcnt lgkmcnt(2)\n"
    "ds_read_b128 v[24:27], v4 offset:320\n"
    "v_cvt_pk_bf16_f32 v72, v72, v73\n"
    "v_mfma_f32_16x16x32_bf16 v[100:103], v[28:31], v[140:143], v[100:103]\n"
    "v_exp_f32 v77, v77\n"
    "v_mfma_f32_16x16x32_bf16 v[108:111], v[28:31], v[172:175], v[108:111]\n"
    "global_load_lds_dwordx4 v1, s[18:19] offset:1056\n"
    "v_add_f32 v9, v9, v74\n"
    "v_mfma_f32_16x16x32_bf16 v[116:119], v[28:31], v[204:207], v[116:119]\n"
    "v_exp_f32 v78, v78\n"
    "v_mfma_f32_16x16x32_bf16 v[124:127], v[28:31], v[236:239], v[124:127]\n"
    "s_waitcnt lgkmcnt(2)\n"
    "ds_read_b128 v[28:31], v4 offset:832\n"
    "v_add_f32 v9, v9, v75\n"
    "v_mfma_f32_16x16x32_bf16 v[96:99], v[16:19], v[144:147], v[96:99]\n"
    "v_exp_f32 v79, v79\n"
    "v_mfma_f32_16x16x32_bf16 v[104:107], v[16:19], v[176:179], v[104:107]\n"
    "global_load_lds_dwordx4 v2, s[18:19] offset:2112\n"
    "v_cvt_pk_bf16_f32 v73, v74, v75\n"
    "v_mfma_f32_16x16x32_bf16 v[112:115], v[16:19], v[208:211], v[112:115]\n"
    "v_exp_f32 v80, v80\n"
    "v_mfma_f32_16x16x32_bf16 v[120:123], v[16:19], v[240:243], v[120:123]\n"
    "s_waitcnt lgkmcnt(2)\n"
    "ds_read_b128 v[16:19], v4 offset:384\n"
    "v_add_f32 v9, v9, v76\n"
    "v_mfma_f32_16x16x32_bf16 v[100:103], v[20:23], v[144:147], v[100:103]\n"
    "v_mbcnt_lo_u32_b32 v58, -1, 0\n"
    "v_add_f32 v9, v9, v77\n"
    "v_mfma_f32_16x16x32_bf16 v[108:111], v[20:23], v[176:179], v[108:111]\n"
    "global_load_lds_dwordx4 v3, s[18:19] offset:3168\n"
    "v_cvt_pk_bf16_f32 v74, v76, v77\n"
    "v_mfma_f32_16x16x32_bf16 v[116:119], v[20:23], v[208:211], v[116:119]\n"
    "v_mbcnt_hi_u32_b32 v58, -1, v58\n"
    "v_add_f32 v9, v9, v78\n"
    "v_mfma_f32_16x16x32_bf16 v[124:127], v[20:23], v[240:243], v[124:127]\n"
    "s_waitcnt lgkmcnt(2)\n"
    "ds_read_b128 v[20:23], v4 offset:896\n"
    "v_add_f32 v9, v9, v79\n"
    "v_mfma_f32_16x16x32_bf16 v[96:99], v[24:27], v[148:151], v[96:99]\n"
    "s_mov_b32 m0, s25\n"
    "v_and_b32 v58, 31, v58\n"
    "v_cvt_pk_bf16_f32 v75, v78, v79\n"
    "v_mfma_f32_16x16x32_bf16 v[104:107], v[24:27], v[180:183], v[104:107]\n"
    "global_load_lds_dwordx4 v60, s[18:19]\n"
    "v_add_f32 v10, v10, v80\n"
    "v_mfma_f32_16x16x32_bf16 v[112:115], v[24:27], v[212:215], v[112:115]\n"
    "v_lshlrev_b32 v58, 7, v58\n"
    "v_mfma_f32_16x16x32_bf16 v[120:123], v[24:27], v[244:247], v[120:123]\n"
    "s_waitcnt lgkmcnt(2)\n"
    "ds_read_b128 v[24:27], v4 offset:448\n"
    "v_mfma_f32_16x16x32_bf16 v[100:103], v[28:31], v[148:151], v[100:103]\n"
    "v_exp_f32 v81, v81\n"
    "v_mfma_f32_16x16x32_bf16 v[108:111], v[28:31], v[180:183], v[108:111]\n"
    "global_load_lds_dwordx4 v61, s[18:19] offset:1056\n"
    "v_add_f32 v10, v10, v81\n"
    "v_mfma_f32_16x16x32_bf16 v[116:119], v[28:31], v[212:215], v[116:119]\n"
    "v_exp_f32 v82, v82\n"
    "v_mfma_f32_16x16x32_bf16 v[124:127], v[28:31], v[244:247], v[124:127]\n"
    "s_waitcnt lgkmcnt(2)\n"
    "ds_read_b128 v[28:31], v4 offset:960\n"
    "v_cvt_pk_bf16_f32 v80, v80, v81\n"
    "v_mfma_f32_16x16x32_bf16 v[96:99], v[16:19], v[152:155], v[96:99]\n"
    "v_exp_f32 v83, v83\n"
    "v_mfma_f32_16x16x32_bf16 v[104:107], v[16:19], v[184:187], v[104:107]\n"
    "global_load_lds_dwordx4 v62, s[18:19] offset:2112\n"
    "v_add_f32 v10, v10, v82\n"
    "v_mfma_f32_16x16x32_bf16 v[112:115], v[16:19], v[216:219], v[112:115]\n"
    "v_exp_f32 v84, v84\n"
    "v_mfma_f32_16x16x32_bf16 v[120:123], v[16:19], v[248:251], v[120:123]\n"
    "s_waitcnt lgkmcnt(2)\n"
    "ds_read_b64_tr_b16 v[16:17], v5 offset:33792\n"
    "ds_read_b64_tr_b16 v[18:19], v5 offset:34304\n"
    "v_mfma_f32_16x16x32_bf16 v[100:103], v[20:23], v[152:155], v[100:103]\n"
    "v_exp_f32 v85, v85\n"
    "v_mfma_f32_16x16x32_bf16 v[108:111], v[20:23], v[184:187], v[108:111]\n"
    "global_load_lds_dwordx4 v63, s[18:19] offset:3168\n"
    "v_add_f32 v10, v10, v83\n"
    "v_mfma_f32_16x16x32_bf16 v[116:119], v[20:23], v[216:219], v[116:119]\n"
    "s_add_u32 s18, s18, 0x8000\n"
    "s_addc_u32 s19, s19, 0\n"
    "v_cvt_pk_bf16_f32 v81, v82, v83\n"
    "v_mfma_f32_16x16x32_bf16 v[124:127], v[20:23], v[248:251], v[124:127]\n"
    "s_waitcnt lgkmcnt(3)\n"
    "ds_read_b64_tr_b16 v[20:21], v5 offset:33824\n"
    "ds_read_b64_tr_b16 v[22:23], v5 offset:34336\n"
    "v_mfma_f32_16x16x32_bf16 v[96:99], v[24:27], v[156:159], v[96:99]\n"
    "global_load_dword v57, v58, s[28:29]\n"
    "v_add_f32 v10, v10, v84\n"
    "v_mfma_f32_16x16x32_bf16 v[104:107], v[24:27], v[188:191], v[104:107]\n"
    "v_exp_f32 v86, v86\n"
    "v_mfma_f32_16x16x32_bf16 v[112:115], v[24:27], v[220:223], v[112:115]\n"
    "v_exp_f32 v87, v87\n"
    "v_mfma_f32_16x16x32_bf16 v[120:123], v[24:27], v[252:255], v[120:123]\n"
    "s_waitcnt lgkmcnt(4)\n"
    "ds_read_b64_tr_b16 v[24:25], v5 offset:33856\n"
    "ds_read_b64_tr_b16 v[26:27], v5 offset:34368\n"
    "v_mfma_f32_16x16x32_bf16 v[100:103], v[28:31], v[156:159], v[100:103]\n"
    "v_exp_f32 v88, v88\n"
    "v_mfma_f32_16x16x32_bf16 v[108:111], v[28:31], v[188:191], v[108:111]\n"
    "v_exp_f32 v89, v89\n"
    "v_mfma_f32_16x16x32_bf16 v[116:119], v[28:31], v[220:223], v[116:119]\n"
    "v_mfma_f32_16x16x32_bf16 v[124:127], v[28:31], v[252:255], v[124:127]\n"
    "s_waitcnt lgkmcnt(4)\n"
    "ds_read_b64_tr_b16 v[28:29], v5 offset:33888\n"
    "ds_read_b64_tr_b16 v[30:31], v5 offset:34400\n"
    "v_exp_f32 v90, v90\n"
    "v_exp_f32 v91, v91\n"
    "v_exp_f32 v92, v92\n"
    "v_exp_f32 v93, v93\n"
    "v_exp_f32 v94, v94\n"
    "v_exp_f32 v95, v95\n"
    "v_add_f32 v10, v10, v85\n"
    "v_cvt_pk_bf16_f32 v82, v84, v85\n"
    "v_add_f32 v10, v10, v86\n"
    "v_add_f32 v10, v10, v87\n"
    "v_cvt_pk_bf16_f32 v83, v86, v87\n"
    "v_add_f32 v11, v11, v88\n"
    "v_add_f32 v11, v11, v89\n"
    "v_cvt_pk_bf16_f32 v88, v88, v89\n"
    "v_add_f32 v11, v11, v90\n"
    "v_add_f32 v11, v11, v91\n"
    "v_cvt_pk_bf16_f32 v89, v90, v91\n"
    "v_add_f32 v11, v11, v92\n"
    "v_add_f32 v11, v11, v93\n"
    "v_cvt_pk_bf16_f32 v90, v92, v93\n"
    "v_add_f32 v11, v11, v94\n"
    "v_add_f32 v11, v11, v95\n"
    "v_cvt_pk_bf16_f32 v91, v94, v95\n"
    "s_mov_b32 s36, 0x3c00\n"
    "v_mbcnt_lo_u32_b32 v58, -1, 0\n"
    "v_mbcnt_hi_u32_b32 v58, -1, v58\n"
    "v_lshlrev_b32 v58, 4, v58\n"
    "v_mov_b32 v48, -1\n"
    "v_mov_b32 v49, -1\n"
    "v_mov_b32 v50, -1\n"
    "v_mov_b32 v51, -1\n"
    "L_loop:\n"
    "v_mfma_f32_16x16x32_bf16 a[0:3], v[16:19], v[64:67], a[0:3]\n"
    "v_mfma_f32_16x16x32_bf16 a[4:7], v[16:19], v[72:75], a[4:7]\n"
    "v_max3_f32 v7, v96, v97, v98\n"
    "v_max3_f32 v7, v7, v99, v104\n"
    "v_mfma_f32_16x16x32_bf16 a[8:11], v[16:19], v[80:83], a[8:11]\n"
    "v_max3_f32 v7, v7, v105, v106\n"
    "v_max3_f32 v7, v7, v107, v112\n"
    "v_mfma_f32_16x16x32_bf16 a[12:15], v[16:19], v[88:91], a[12:15]\n"
    "s_waitcnt lgkmcnt(4)\n"
    "ds_read_b64_tr_b16 v[16:17], v5 offset:33920\n"
    "ds_read_b64_tr_b16 v[18:19], v5 offset:34432\n"
    "v_mfma_f32_16x16x32_bf16 a[16:19], v[20:23], v[64:67], a[16:19]\n"
    "v_max3_f32 v7, v7, v113, v114\n"
    "v_max3_f32 v7, v7, v115, v120\n"
    "v_mfma_f32_16x16x32_bf16 a[20:23], v[20:23], v[72:75], a[20:23]\n"
    "v_max3_f32 v7, v7, v121, v122\n"
    "v_max3_f32 v7, v7, v123, v100\n"
    "v_mfma_f32_16x16x32_bf16 a[24:27], v[20:23], v[80:83], a[24:27]\n"
    "v_max3_f32 v7, v7, v101, v102\n"
    "v_max3_f32 v7, v7, v103, v108\n"
    "v_mfma_f32_16x16x32_bf16 a[28:31], v[20:23], v[88:91], a[28:31]\n"
    "s_waitcnt lgkmcnt(4)\n"
    "ds_read_b64_tr_b16 v[20:21], v5 offset:33952\n"
    "ds_read_b64_tr_b16 v[22:23], v5 offset:34464\n"
    "v_mfma_f32_16x16x32_bf16 a[32:35], v[24:27], v[64:67], a[32:35]\n"
    "v_max3_f32 v7, v7, v109, v110\n"
    "v_max3_f32 v7, v7, v111, v116\n"
    "v_mfma_f32_16x16x32_bf16 a[36:39], v[24:27], v[72:75], a[36:39]\n"
    "v_max3_f32 v7, v7, v117, v118\n"
    "v_max3_f32 v7, v7, v119, v124\n"
    "v_mfma_f32_16x16x32_bf16 a[40:43], v[24:27], v[80:83], a[40:43]\n"
    "v_max3_f32 v7, v7, v125, v126\n"
    "v_max_f32 v7, v7, v127\n"
    "v_mfma_f32_16x16x32_bf16 a[44:47], v[24:27], v[88:91], a[44:47]\n"
    "s_waitcnt lgkmcnt(4)\n"
    "ds_read_b64_tr_b16 v[24:25], v5 offset:33984\n"
    "ds_read_b64_tr_b16 v[26:27], v5 offset:34496\n"
    "v_mfma_f32_16x16x32_bf16 a[48:51], v[28:31], v[64:67], a[48:51]\n"
    "v_cmp_lt_f32 vcc, s38, v7\n"
    "buffer_store_dwordx4 v[48:51], v58, s[60:63], s36 offen sc1\n"
    "v_mfma_f32_16x16x32_bf16 a[52:55], v[28:31], v[72:75], a[52:55]\n"
    "s_cbranch_vccnz L_rareA_p1\n"
    "L_rareA_ret_p1:\n"
    "v_exp_f32 v96, v96\n"
    "v_mfma_f32_16x16x32_bf16 a[56:59], v[28:31], v[80:83], a[56:59]\n"
    "v_exp_f32 v97, v97\n"
    "v_mfma_f32_16x16x32_bf16 a[60:63], v[28:31], v[88:91], a[60:63]\n"
    "s_waitcnt lgkmcnt(4)\n"
    "ds_read_b64_tr_b16 v[28:29], v5 offset:34016\n"
    "ds_read_b64_tr_b16 v[30:31], v5 offset:34528\n"
    "v_mfma_f32_16x16x32_bf16 a[64:67], v[16:19], v[64:67], a[64:67]\n"
    "v_exp_f32 v98, v98\n"
    "v_mfma_f32_16x16x32_bf16 a[68:71], v[16:19], v[72:75], a[68:71]\n"
    "v_exp_f32 v99, v99\n"
    "v_mfma_f32_16x16x32_bf16 a[72:75], v[16:19], v[80:83], a[72:75]\n"
    "v_exp_f32 v100, v100\n"
    "v_mfma_f32_16x16x32_bf16 a[76:79], v[16:19], v[88:91], a[76:79]\n"
    "s_waitcnt lgkmcnt(4)\n"
    "ds_read_b64_tr_b16 v[16:17], v6 offset:34048\n"
    "ds_read_b64_tr_b16 v[18:19], v6 offset:34560\n"
    "v_mfma_f32_16x16x32_bf16 a[80:83], v[20:23], v[64:67], a[80:83]\n"
    "v_exp_f32 v101, v101\n"
    "v_mfma_f32_16x16x32_bf16 a[84:87], v[20:23], v[72:75], a[84:87]\n"
    "v_exp_f32 v102, v102\n"
    "v_mfma_f32_16x16x32_bf16 a[88:91], v[20:23], v[80:83], a[88:91]\n"
    "v_exp_f32 v103, v103\n"
    "v_mfma_f32_16x16x32_bf16 a[92:95], v[20:23], v[88:91], a[92:95]\n"
    "s_waitcnt lgkmcnt(4)\n"
    "ds_read_b64_tr_b16 v[20:21], v6 offset:34080\n"
    "ds_read_b64_tr_b16 v[22:23], v6 offset:34592\n"
    "v_mfma_f32_16x16x32_bf16 a[96:99], v[24:27], v[64:67], a[96:99]\n"
    "v_exp_f32 v104, v104\n"
    "v_mfma_f32_16x16x32_bf16 a[100:103], v[24:27], v[72:75], a[100:103]\n"
    "v_exp_f32 v105, v105\n"
    "v_mfma_f32_16x16x32_bf16 a[104:107], v[24:27], v[80:83], a[104:107]\n"
    "v_exp_f32 v106, v106\n"
    "v_mfma_f32_16x16x32_bf16 a[108:111], v[24:27], v[88:91], a[108:111]\n"
    "s_waitcnt lgkmcnt(4)\n"
    "ds_read_b64_tr_b16 v[24:25], v6 offset:34112\n"
    "ds_read_b64_tr_b16 v[26:27], v6 offset:34624\n"
    "v_mfma_f32_16x16x32_bf16 a[112:115], v[28:31], v[64:67], a[112:115]\n"
    "v_exp_f32 v107, v107\n"
    "v_mfma_f32_16x16x32_bf16 a[116:119], v[28:31], v[72:75], a[116:119]\n"
    "v_exp_f32 v108, v108\n"
    "v_mfma_f32_16x16x32_bf16 a[120:123], v[28:31], v[80:83], a[120:123]\n"
    "v_exp_f32 v109, v109\n"
    "v_mfma_f32_16x16x32_bf16 a[124:127], v[28:31], v[88:91], a[124:127]\n"
    "s_waitcnt lgkmcnt(4)\n"
    "ds_read_b64_tr_b16 v[28:29], v6 offset:34144\n"
    "ds_read_b64_tr_b16 v[30:31], v6 offset:34656\n"
    "v_mfma_f32_16x16x32_bf16 a[128:131], v[16:19], v[64:67], a[128:131]\n"
    "v_exp_f32 v110, v110\n"
    "v_mfma_f32_16x16x32_bf16 a[132:135], v[16:19], v[72:75], a[132:135]\n"
    "v_exp_f32 v111, v111\n"
    "v_mfma_f32_16x16x32_bf16 a[136:139], v[16:19], v[80:83], a[136:139]\n"
    "v_exp_f32 v112, v112\n"
    "v_mfma_f32_16x16x32_bf16 a[140:143], v[16:19], v[88:91], a[140:143]\n"
    "s_waitcnt lgkmcnt(4)\n"
    "ds_read_b64_tr_b16 v[16:17], v6 offset:34176\n"
    "ds_read_b64_tr_b16 v[18:19], v6 offset:34688\n"
    "v_mfma_f32_16x16x32_bf16 a[144:147], v[20:23], v[64:67], a[144:147]\n"
    "v_exp_f32 v113, v113\n"
    "v_mfma_f32_16x16x32_bf16 a[148:151], v[20:23], v[72:75], a[148:151]\n"
    "v_exp_f32 v114, v114\n"
    "v_mfma_f32_16x16x32_bf16 a[152:155], v[20:23], v[80:83], a[152:155]\n"
    "v_exp_f32 v115, v115\n"
    "v_mfma_f32_16x16x32_bf16 a[156:159], v[20:23], v[88:91], a[156:159]\n"
    "s_waitcnt lgkmcnt(4)\n"
    "ds_read_b64_tr_b16 v[20:21], v6 offset:34208\n"
    "ds_read_b64_tr_b16 v[22:23], v6 offset:34720\n"
    "v_mfma_f32_16x16x32_bf16 a[160:163], v[24:27], v[64:67], a[160:163]\n"
    "v_exp_f32 v116, v116\n"
    "v_mfma_f32_16x16x32_bf16 a[164:167], v[24:27], v[72:75], a[164:167]\n"
    "v_exp_f32 v117, v117\n"
    "v_mfma_f32_16x16x32_bf16 a[168:171], v[24:27], v[80:83], a[168:171]\n"
    "v_exp_f32 v118, v118\n"
    "v_mfma_f32_16x16x32_bf16 a[172:175], v[24:27], v[88:91], a[172:175]\n"
    "s_waitcnt lgkmcnt(4)\n"
    "ds_read_b64_tr_b16 v[24:25], v6 offset:34240\n"
    "ds_read_b64_tr_b16 v[26:27], v6 offset:34752\n"
    "v_mfma_f32_16x16x32_bf16 a[176:179], v[28:31], v[64:67], a[176:179]\n"
    "v_exp_f32 v119, v119\n"
    "v_mfma_f32_16x16x32_bf16 a[180:183], v[28:31], v[72:75], a[180:183]\n"
    "v_exp_f32 v120, v120\n"
    "v_mfma_f32_16x16x32_bf16 a[184:187], v[28:31], v[80:83], a[184:187]\n"
    "v_exp_f32 v121, v121\n"
    "v_mfma_f32_16x16x32_bf16 a[188:191], v[28:31], v[88:91], a[188:191]\n"
    "s_waitcnt lgkmcnt(4)\n"
    "ds_read_b64_tr_b16 v[28:29], v6 offset:34272\n"
    "ds_read_b64_tr_b16 v[30:31], v6 offset:34784\n"
    "v_mfma_f32_16x16x32_bf16 a[192:195], v[16:19], v[64:67], a[192:195]\n"
    "v_exp_f32 v122, v122\n"
    "v_mfma_f32_16x16x32_bf16 a[196:199], v[16:19], v[72:75], a[196:199]\n"
    "v_exp_f32 v123, v123\n"
    "v_mfma_f32_16x16x32_bf16 a[200:203], v[16:19], v[80:83], a[200:203]\n"
    "v_exp_f32 v124, v124\n"
    "v_mfma_f32_16x16x32_bf16 a[204:207], v[16:19], v[88:91], a[204:207]\n"
    "s_waitcnt lgkmcnt(4)\n"
    "v_exp_f32 v125, v125\n"
    "v_mfma_f32_16x16x32_bf16 a[208:211], v[20:23], v[64:67], a[208:211]\n"
    "v_exp_f32 v126, v126\n"
    "v_mfma_f32_16x16x32_bf16 a[212:215], v[20:23], v[72:75], a[212:215]\n"
    "v_exp_f32 v127, v127\n"
    "v_mfma_f32_16x16x32_bf16 a[216:219], v[20:23], v[80:83], a[216:219]\n"
    "v_add_f32 v8, v8, v96\n"
    "v_add_f32 v8, v8, v97\n"
    "v_mfma_f32_16x16x32_bf16 a[220:223], v[20:23], v[88:91], a[220:223]\n"
    "s_waitcnt vmcnt(2) lgkmcnt(0)\n"
    "s_barrier\n"
    "ds_read_b128 v[16:19], v4 offset:33792\n"
    "v_mfma_f32_16x16x32_bf16 a[224:227], v[24:27], v[64:67], a[224:227]\n"
    "s_mov_b32 m0, s20\n"
    "ds_read_b128 v[20:23], v4 offset:34304\n"
    "v_cvt_pk_bf16_f32 v96, v96, v97\n"
    "v_mfma_f32_16x16x32_bf16 a[228:231], v[24:27], v[72:75], a[228:231]\n"
    "global_load_lds_dwordx4 v0, s[16:17]\n"
    "v_add_f32 v8, v8, v98\n"
    "v_mfma_f32_16x16x32_bf16 a[232:235], v[24:27], v[80:83], a[232:235]\n"
    "v_add_f32 v8, v8, v99\n"
    "v_cvt_pk_bf16_f32 v97, v98, v99\n"
    "v_mfma_f32_16x16x32_bf16 a[236:239], v[24:27], v[88:91], a[236:239]\n"
    "ds_read_b128 v[24:27], v4 offset:33856\n"
    "v_add_f32 v8, v8, v100\n"
    "v_mfma_f32_16x16x32_bf16 a[240:243], v[28:31], v[64:67], a[240:243]\n"
    "v_add_f32 v8, v8, v101\n"
    "v_cvt_pk_bf16_f32 v98, v100, v101\n"
    "v_mfma_f32_16x16x32_bf16 a[244:247], v[28:31], v[72:75], a[244:247]\n"
    "global_load_lds_dwordx4 v1, s[16:17] offset:1056\n"
    "v_add_f32 v8, v8, v102\n"
    "v_mfma_f32_16x16x32_bf16 a[248:251], v[28:31], v[80:83], a[248:251]\n"
    "v_add_f32 v8, v8, v103\n"
    "v_cvt_pk_bf16_f32 v99, v102, v103\n"
    "v_mfma_f32_16x16x32_bf16 a[252:255], v[28:31], v[88:91], a[252:255]\n"
    "s_waitcnt lgkmcnt(2)\n"
    "ds_read_b128 v[28:31], v4 offset:34368\n"
    "v_add_f32 v9, v9, v104\n"
    "v_mfma_f32_16x16x32_bf16 v[64:67], v[16:19], v[128:131], v[32:35]\n"
    "v_add_f32 v9, v9, v105\n"
    "v_cvt_pk_bf16_f32 v104, v104, v105\n"
    "v_mfma_f32_16x16x32_bf16 v[72:75], v[16:19], v[160:163], v[36:39]\n"
    "global_load_lds_dwordx4 v2, s[16:17] offset:2112\n"
    "v_add_f32 v9, v9, v106\n"
    "v_mfma_f32_16x16x32_bf16 v[80:83], v[16:19], v[192:195], v[40:43]\n"
    "s_cmp_lg_u32 s37, 0\n"
    "s_cbranch_scc1 L_rb_p1\n"
    "L_rb_ret_p1:\n"
    "v_add_f32 v9, v9, v107\n"
    "v_mfma_f32_16x16x32_bf16 v[88:91], v[16:19], v[224:227], v[44:47]\n"
    "s_waitcnt lgkmcnt(2)\n"
    "ds_read_b128 v[16:19], v4 offset:33920\n"
    "v_cvt_pk_bf16_f32 v105, v106, v107\n"
    "v_mfma_f32_16x16x32_bf16 v[68:71], v[20:23], v[128:131], v[32:35]\n"
    "v_add_f32 v9, v9, v108\n"
    "v_add_f32 v9, v9, v109\n"
    "v_mfma_f32_16x16x32_bf16 v[76:79], v[20:23], v[160:163], v[36:39]\n"
    "global_load_lds_dwordx4 v3, s[16:17] offset:3168\n"
    "v_cvt_pk_bf16_f32 v106, v108, v109\n"
    "v_mfma_f32_16x16x32_bf16 v[84:87], v[20:23], v[192:195], v[40:43]\n"
    "s_cmp_eq_u32 s36, 0x400\n"
    "s_cselect_b32 s32, 0, 0x8000\n"
    "v_add_f32 v9, v9, v110\n"
    "v_mfma_f32_16x16x32_bf16 v[92:95], v[20:23], v[224:227], v[44:47]\n"
    "s_waitcnt lgkmcnt(2)\n"
    "ds_read_b128 v[20:23], v4 offset:34432\n"
    "v_add_f32 v9, v9, v111\n"
    "v_mfma_f32_16x16x32_bf16 v[64:67], v[24:27], v[132:135], v[64:67]\n"
    "s_mov_b32 m0, s21\n"
    "v_cvt_pk_bf16_f32 v107, v110, v111\n"
    "v_add_f32 v10, v10, v112\n"
    "v_mfma_f32_16x16x32_bf16 v[72:75], v[24:27], v[164:167], v[72:75]\n"
    "global_load_lds_dwordx4 v60, s[16:17]\n"
    "v_add_f32 v10, v10, v113\n"
    "v_mfma_f32_16x16x32_bf16 v[80:83], v[24:27], v[196:199], v[80:83]\n"
    "v_cvt_pk_bf16_f32 v112, v112, v113\n"
    "v_add_f32 v10, v10, v114\n"
    "v_mfma_f32_16x16x32_bf16 v[88:91], v[24:27], v[228:231], v[88:91]\n"
    "s_waitcnt lgkmcnt(2)\n"
    "ds_read_b128 v[24:27], v4 offset:33984\n"
    "v_add_f32 v10, v10, v115\n"
    "v_mfma_f32_16x16x32_bf16 v[68:71], v[28:31], v[132:135], v[68:71]\n"
    "v_cvt_pk_bf16_f32 v113, v114, v115\n"
    "v_add_f32 v10, v10, v116\n"
    "v_mfma_f32_16x16x32_bf16 v[76:79], v[28:31], v[164:167], v[76:79]\n"
    "global_load_lds_dwordx4 v61, s[16:17] offset:1056\n"
    "v_add_f32 v10, v10, v117\n"
    "v_mfma_f32_16x16x32_bf16 v[84:87], v[28:31], v[196:199], v[84:87]\n"
    "v_cvt_pk_bf16_f32 v114, v116, v117\n"
    "v_add_f32 v10, v10, v118\n"
    "v_mfma_f32_16x16x32_bf16 v[92:95], v[28:31], v[228:231], v[92:95]\n"
    "s_waitcnt lgkmcnt(2)\n"
    "ds_read_b128 v[28:31], v4 offset:34496\n"
    "v_add_f32 v10, v10, v119\n"
    "v_mfma_f32_16x16x32_bf16 v[64:67], v[16:19], v[136:139], v[64:67]\n"
    "v_cvt_pk_bf16_f32 v115, v118, v119\n"
    "v_add_f32 v11, v11, v120\n"
    "v_mfma_f32_16x16x32_bf16 v[72:75], v[16:19], v[168:171], v[72:75]\n"
    "global_load_lds_dwordx4 v62, s[16:17] offset:2112\n"
    "v_add_f32 v11, v11, v121\n"
    "v_mfma_f32_16x16x32_bf16 v[80:83], v[16:19], v[200:203], v[80:83]\n"
    "v_cvt_pk_bf16_f32 v120, v120, v121\n"
    "v_add_f32 v11, v11, v122\n"
    "v_mfma_f32_16x16x32_bf16 v[88:91], v[16:19], v[232:235], v[88:91]\n"
    "s_waitcnt lgkmcnt(2)\n"
    "ds_read_b128 v[16:19], v4 offset:34048\n"
    "v_add_f32 v11, v11, v123\n"
    "v_mfma_f32_16x16x32_bf16 v[68:71], v[20:23], v[136:139], v[68:71]\n"
    "v_cvt_pk_bf16_f32 v121, v122, v123\n"
    "v_add_f32 v11, v11, v124\n"
    "v_mfma_f32_16x16x32_bf16 v[76:79], v[20:23], v[168:171], v[76:79]\n"
    "global_load_lds_dwordx4 v63, s[16:17] offset:3168\n"
    "v_add_f32 v11, v11, v125\n"
    "v_mfma_f32_16x16x32_bf16 v[84:87], v[20:23], v[200:203], v[84:87]\n"
    "s_add_u32 s16, s16, s32\n"
    "s_addc_u32 s17, s17, 0\n"
    "v_cvt_pk_bf16_f32 v122, v124, v125\n"
    "v_mfma_f32_16x16x32_bf16 v[92:95], v[20:23], v[232:235], v[92:95]\n"
    "s_waitcnt lgkmcnt(2)\n"
    "ds_read_b128 v[20:23], v4 offset:34560\n"
    "v_add_f32 v11, v11, v126\n"
    "v_mfma_f32_16x16x32_bf16 v[64:67], v[24:27], v[140:143], v[64:67]\n"
    "s_mov_b32 m0, s26\n"
    "v_add_f32 v11, v11, v127\n"
    "v_cvt_pk_bf16_f32 v123, v126, v127\n"
    "v_mfma_f32_16x16x32_bf16 v[72:75], v[24:27], v[172:175], v[72:75]\n"
    "global_load_lds_dwordx4 v0, s[18:19]\n"
    "v_mfma_f32_16x16x32_bf16 v[80:83], v[24:27], v[204:207], v[80:83]\n"
    "v_mfma_f32_16x16x32_bf16 v[88:91], v[24:27], v[236:239], v[88:91]\n"
    "s_waitcnt lgkmcnt(2)\n"
    "ds_read_b128 v[24:27], v4 offset:34112\n"
    "v_mfma_f32_16x16x32_bf16 v[68:71], v[28:31], v[140:143], v[68:71]\n"
    "v_mfma_f32_16x16x32_bf16 v[76:79], v[28:31], v[172:175], v[76:79]\n"
    "global_load_lds_dwordx4 v1, s[18:19] offset:1056\n"
    "v_mfma_f32_16x16x32_bf16 v[84:87], v[28:31], v[204:207], v[84:87]\n"
    "v_mfma_f32_16x16x32_bf16 v[92:95], v[28:31], v[236:239], v[92:95]\n"
    "s_waitcnt lgkmcnt(2)\n"
    "ds_read_b128 v[28:31], v4 offset:34624\n"
    "v_mfma_f32_16x16x32_bf16 v[64:67], v[16:19], v[144:147], v[64:67]\n"
    "v_mfma_f32_16x16x32_bf16 v[72:75], v[16:19], v[176:179], v[72:75]\n"
    "global_load_lds_dwordx4 v2, s[18:19] offset:2112\n"
    "v_mfma_f32_16x16x32_bf16 v[80:83], v[16:19], v[208:211], v[80:83]\n"
    "v_mfma_f32_16x16x32_bf16 v[88:91], v[16:19], v[240:243], v[88:91]\n"
    "s_waitcnt lgkmcnt(2)\n"
    "ds_read_b128 v[16:19], v4 offset:34176\n"
    "v_mfma_f32_16x16x32_bf16 v[68:71], v[20:23], v[144:147], v[68:71]\n"
    "v_mfma_f32_16x16x32_bf16 v[76:79], v[20:23], v[176:179], v[76:79]\n"
    "global_load_lds_dwordx4 v3, s[18:19] offset:3168\n"
    "v_mfma_f32_16x16x32_bf16 v[84:87], v[20:23], v[208:211], v[84:87]\n"
    "v_mfma_f32_16x16x32_bf16 v[92:95], v[20:23], v[240:243], v[92:95]\n"
    "s_waitcnt lgkmcnt(2)\n"
    "ds_read_b128 v[20:23], v4 offset:34688\n"
    "v_mfma_f32_16x16x32_bf16 v[64:67], v[24:27], v[148:151], v[64:67]\n"
    "s_mov_b32 m0, s27\n"
    "v_mfma_f32_16x16x32_bf16 v[72:75], v[24:27], v[180:183], v[72:75]\n"
    "global_load_lds_dwordx4 v60, s[18:19]\n"
    "v_mfma_f32_16x16x32_bf16 v[80:83], v[24:27], v[212:215], v[80:83]\n"
    "v_mfma_f32_16x16x32_bf16 v[88:91], v[24:27], v[244:247], v[88:91]\n"
    "s_waitcnt lgkmcnt(2)\n"
    "ds_read_b128 v[24:27], v4 offset:34240\n"
    "v_mfma_f32_16x16x32_bf16 v[68:71], v[28:31], v[148:151], v[68:71]\n"
    "v_mfma_f32_16x16x32_bf16 v[76:79], v[28:31], v[180:183], v[76:79]\n"
    "global_load_lds_dwordx4 v61, s[18:19] offset:1056\n"
    "v_mfma_f32_16x16x32_bf16 v[84:87], v[28:31], v[212:215], v[84:87]\n"
    "v_mfma_f32_16x16x32_bf16 v[92:95], v[28:31], v[244:247], v[92:95]\n"
    "s_waitcnt lgkmcnt(2)\n"
    "ds_read_b128 v[28:31], v4 offset:34752\n"
    "v_mfma_f32_16x16x32_bf16 v[64:67], v[16:19], v[152:155], v[64:67]\n"
    "v_mfma_f32_16x16x32_bf16 v[72:75], v[16:19], v[184:187], v[72:75]\n"
    "global_load_lds_dwordx4 v62, s[18:19] offset:2112\n"
    "v_mfma_f32_16x16x32_bf16 v[80:83], v[16:19], v[216:219], v[80:83]\n"
    "v_mfma_f32_16x16x32_bf16 v[88:91], v[16:19], v[248:251], v[88:91]\n"
    "s_waitcnt lgkmcnt(2)\n"
    "ds_read_b64_tr_b16 v[16:17], v5\n"
    "ds_read_b64_tr_b16 v[18:19], v5 offset:512\n"
    "v_mfma_f32_16x16x32_bf16 v[68:71], v[20:23], v[152:155], v[68:71]\n"
    "v_mfma_f32_16x16x32_bf16 v[76:79], v[20:23], v[184:187], v[76:79]\n"
    "global_load_lds_dwordx4 v63, s[18:19] offset:3168\n"
    "v_mfma_f32_16x16x32_bf16 v[84:87], v[20:23], v[216:219], v[84:87]\n"
    "s_add_u32 s18, s18, 0x8000\n"
    "s_addc_u32 s19, s19, 0\n"
    "v_mfma_f32_16x16x32_bf16 v[92:95], v[20:23], v[248:251], v[92:95]\n"
    "s_waitcnt lgkmcnt(3)\n"
    "ds_read_b64_tr_b16 v[20:21], v5 offset:32\n"
    "ds_read_b64_tr_b16 v[22:23], v5 offset:544\n"
    "v_mfma_f32_16x16x32_bf16 v[64:67], v[24:27], v[156:159], v[64:67]\n"
    "global_load_dword v55, v56, s[34:35]\n"
    "v_mfma_f32_16x16x32_bf16 v[72:75], v[24:27], v[188:191], v[72:75]\n"
    "s_cmp_gt_u32 s36, 0x1000\n"
    "s_cselect_b32 s30, 0x10000, 0\n"
    "v_mfma_f32_16x16x32_bf16 v[80:83], v[24:27], v[220:223], v[80:83]\n"
    "s_add_u32 s34, s34, s30\n"
    "s_addc_u32 s35, s35, 0\n"
    "v_mfma_f32_16x16x32_bf16 v[88:91], v[24:27], v[252:255], v[88:91]\n"
    "s_waitcnt lgkmcnt(4)\n"
    "ds_read_b64_tr_b16 v[24:25], v5 offset:64\n"
    "ds_read_b64_tr_b16 v[26:27], v5 offset:576\n"
    "v_mfma_f32_16x16x32_bf16 v[68:71], v[28:31], v[156:159], v[68:71]\n"
    "v_mfma_f32_16x16x32_bf16 v[76:79], v[28:31], v[188:191], v[76:79]\n"
    "v_mfma_f32_16x16x32_bf16 v[84:87], v[28:31], v[220:223], v[84:87]\n"
    "v_mfma_f32_16x16x32_bf16 v[92:95], v[28:31], v[252:255], v[92:95]\n"
    "s_waitcnt lgkmcnt(4)\n"
    "ds_read_b64_tr_b16 v[28:29], v5 offset:96\n"
    "ds_read_b64_tr_b16 v[30:31], v5 offset:608\n"
    "v_mfma_f32_16x16x32_bf16 a[0:3], v[16:19], v[96:99], a[0:3]\n"
    "v_mfma_f32_16x16x32_bf16 a[4:7], v[16:19], v[104:107], a[4:7]\n"
    "v_max3_f32 v7, v64, v65, v66\n"
    "v_max3_f32 v7, v7, v67, v72\n"
    "v_mfma_f32_16x16x32_bf16 a[8:11], v[16:19], v[112:115], a[8:11]\n"
    "v_max3_f32 v7, v7, v73, v74\n"
    "v_max3_f32 v7, v7, v75, v80\n"
    "v_mfma_f32_16x16x32_bf16 a[12:15], v[16:19], v[120:123], a[12:15]\n"
    "s_waitcnt lgkmcnt(4)\n"
    "ds_read_b64_tr_b16 v[16:17], v5 offset:128\n"
    "ds_read_b64_tr_b16 v[18:19], v5 offset:640\n"
    "v_mfma_f32_16x16x32_bf16 a[16:19], v[20:23], v[96:99], a[16:19]\n"
    "v_max3_f32 v7, v7, v81, v82\n"
    "v_max3_f32 v7, v7, v83, v88\n"
    "v_mfma_f32_16x16x32_bf16 a[20:23], v[20:23], v[104:107], a[20:23]\n"
    "v_max3_f32 v7, v7, v89, v90\n"
    "v_max3_f32 v7, v7, v91, v68\n"
    "v_mfma_f32_16x16x32_bf16 a[24:27], v[20:23], v[112:115], a[24:27]\n"
    "v_max3_f32 v7, v7, v69, v70\n"
    "v_max3_f32 v7, v7, v71, v76\n"
    "v_mfma_f32_16x16x32_bf16 a[28:31], v[20:23], v[120:123], a[28:31]\n"
    "s_waitcnt lgkmcnt(4)\n"
    "ds_read_b64_tr_b16 v[20:21], v5 offset:160\n"
    "ds_read_b64_tr_b16 v[22:23], v5 offset:672\n"
    "v_mfma_f32_16x16x32_bf16 a[32:35], v[24:27], v[96:99], a[32:35]\n"
    "v_max3_f32 v7, v7, v77, v78\n"
    "v_max3_f32 v7, v7, v79, v84\n"
    "v_mfma_f32_16x16x32_bf16 a[36:39], v[24:27], v[104:107], a[36:39]\n"
    "v_max3_f32 v7, v7, v85, v86\n"
    "v_max3_f32 v7, v7, v87, v92\n"
    "v_mfma_f32_16x16x32_bf16 a[40:43], v[24:27], v[112:115], a[40:43]\n"
    "v_max3_f32 v7, v7, v93, v94\n"
    "v_max_f32 v7, v7, v95\n"
    "v_mfma_f32_16x16x32_bf16 a[44:47], v[24:27], v[120:123], a[44:47]\n"
    "s_waitcnt lgkmcnt(4)\n"
    "ds_read_b64_tr_b16 v[24:25], v5 offset:192\n"
    "ds_read_b64_tr_b16 v[26:27], v5 offset:704\n"
    "v_mfma_f32_16x16x32_bf16 a[48:51], v[28:31], v[96:99], a[48:51]\n"
    "v_cmp_lt_f32 vcc, s38, v7\n"
    "v_mfma_f32_16x16x32_bf16 a[52:55], v[28:31], v[104:107], a[52:55]\n"
    "s_cbranch_vccnz L_rareA_p2\n"
    "L_rareA_ret_p2:\n"
    "v_exp_f32 v64, v64\n"
    "v_mfma_f32_16x16x32_bf16 a[56:59], v[28:31], v[112:115], a[56:59]\n"
    "v_exp_f32 v65, v65\n"
    "v_mfma_f32_16x16x32_bf16 a[60:63], v[28:31], v[120:123], a[60:63]\n"
    "s_waitcnt lgkmcnt(4)\n"
    "ds_read_b64_tr_b16 v[28:29], v5 offset:224\n"
    "ds_read_b64_tr_b16 v[30:31], v5 offset:736\n"
    "v_mfma_f32_16x16x32_bf16 a[64:67], v[16:19], v[96:99], a[64:67]\n"
    "v_exp_f32 v66, v66\n"
    "v_mfma_f32_16x16x32_bf16 a[68:71], v[16:19], v[104:107], a[68:71]\n"
    "v_exp_f32 v67, v67\n"
    "v_mfma_f32_16x16x32_bf16 a[72:75], v[16:19], v[112:115], a[72:75]\n"
    "v_exp_f32 v68, v68\n"
    "v_mfma_f32_16x16x32_bf16 a[76:79], v[16:19], v[120:123], a[76:79]\n"
    "s_waitcnt lgkmcnt(4)\n"
    "ds_read_b64_tr_b16 v[16:17], v6 offset:256\n"
    "ds_read_b64_tr_b16 v[18:19], v6 offset:768\n"
    "v_mfma_f32_16x16x32_bf16 a[80:83], v[20:23], v[96:99], a[80:83]\n"
    "v_exp_f32 v69, v69\n"
    "v_mfma_f32_16x16x32_bf16 a[84:87], v[20:23], v[104:107], a[84:87]\n"
    "v_exp_f32 v70, v70\n"
    "v_mfma_f32_16x16x32_bf16 a[88:91], v[20:23], v[112:115], a[88:91]\n"
    "v_exp_f32 v71, v71\n"
    "v_mfma_f32_16x16x32_bf16 a[92:95], v[20:23], v[120:123], a[92:95]\n"
    "s_waitcnt lgkmcnt(4)\n"
    "ds_read_b64_tr_b16 v[20:21], v6 offset:288\n"
    "ds_read_b64_tr_b16 v[22:23], v6 offset:800\n"
    "v_mfma_f32_16x16x32_bf16 a[96:99], v[24:27], v[96:99], a[96:99]\n"
    "v_exp_f32 v72, v72\n"
    "v_mfma_f32_16x16x32_bf16 a[100:103], v[24:27], v[104:107], a[100:103]\n"
    "v_exp_f32 v73, v73\n"
    "v_mfma_f32_16x16x32_bf16 a[104:107], v[24:27], v[112:115], a[104:107]\n"
    "v_exp_f32 v74, v74\n"
    "v_mfma_f32_16x16x32_bf16 a[108:111], v[24:27], v[120:123], a[108:111]\n"
    "s_waitcnt lgkmcnt(4)\n"
    "ds_read_b64_tr_b16 v[24:25], v6 offset:320\n"
    "ds_read_b64_tr_b16 v[26:27], v6 offset:832\n"
    "v_mfma_f32_16x16x32_bf16 a[112:115], v[28:31], v[96:99], a[112:115]\n"
    "v_exp_f32 v75, v75\n"
    "v_mfma_f32_16x16x32_bf16 a[116:119], v[28:31], v[104:107], a[116:119]\n"
    "v_exp_f32 v76, v76\n"
    "v_mfma_f32_16x16x32_bf16 a[120:123], v[28:31], v[112:115], a[120:123]\n"
    "v_exp_f32 v77, v77\n"
    "v_mfma_f32_16x16x32_bf16 a[124:127], v[28:31], v[120:123], a[124:127]\n"
    "s_waitcnt lgkmcnt(4)\n"
    "ds_read_b64_tr_b16 v[28:29], v6 offset:352\n"
    "ds_read_b64_tr_b16 v[30:31], v6 offset:864\n"
    "v_mfma_f32_16x16x32_bf16 a[128:131], v[16:19], v[96:99], a[128:131]\n"
    "v_exp_f32 v78, v78\n"
    "v_mfma_f32_16x16x32_bf16 a[132:135], v[16:19], v[104:107], a[132:135]\n"
    "v_exp_f32 v79, v79\n"
    "v_mfma_f32_16x16x32_bf16 a[136:139], v[16:19], v[112:115], a[136:139]\n"
    "v_exp_f32 v80, v80\n"
    "v_mfma_f32_16x16x32_bf16 a[140:143], v[16:19], v[120:123], a[140:143]\n"
    "s_waitcnt lgkmcnt(4)\n"
    "ds_read_b64_tr_b16 v[16:17], v6 offset:384\n"
    "ds_read_b64_tr_b16 v[18:19], v6 offset:896\n"
    "v_mfma_f32_16x16x32_bf16 a[144:147], v[20:23], v[96:99], a[144:147]\n"
    "v_exp_f32 v81, v81\n"
    "v_mfma_f32_16x16x32_bf16 a[148:151], v[20:23], v[104:107], a[148:151]\n"
    "v_exp_f32 v82, v82\n"
    "v_mfma_f32_16x16x32_bf16 a[152:155], v[20:23], v[112:115], a[152:155]\n"
    "v_exp_f32 v83, v83\n"
    "v_mfma_f32_16x16x32_bf16 a[156:159], v[20:23], v[120:123], a[156:159]\n"
    "s_waitcnt lgkmcnt(4)\n"
    "ds_read_b64_tr_b16 v[20:21], v6 offset:416\n"
    "ds_read_b64_tr_b16 v[22:23], v6 offset:928\n"
    "v_mfma_f32_16x16x32_bf16 a[160:163], v[24:27], v[96:99], a[160:163]\n"
    "v_exp_f32 v84, v84\n"
    "v_mfma_f32_16x16x32_bf16 a[164:167], v[24:27], v[104:107], a[164:167]\n"
    "v_exp_f32 v85, v85\n"
    "v_mfma_f32_16x16x32_bf16 a[168:171], v[24:27], v[112:115], a[168:171]\n"
    "v_exp_f32 v86, v86\n"
    "v_mfma_f32_16x16x32_bf16 a[172:175], v[24:27], v[120:123], a[172:175]\n"
    "s_waitcnt lgkmcnt(4)\n"
    "ds_read_b64_tr_b16 v[24:25], v6 offset:448\n"
    "ds_read_b64_tr_b16 v[26:27], v6 offset:960\n"
    "v_mfma_f32_16x16x32_bf16 a[176:179], v[28:31], v[96:99], a[176:179]\n"
    "v_exp_f32 v87, v87\n"
    "v_mfma_f32_16x16x32_bf16 a[180:183], v[28:31], v[104:107], a[180:183]\n"
    "v_exp_f32 v88, v88\n"
    "v_mfma_f32_16x16x32_bf16 a[184:187], v[28:31], v[112:115], a[184:187]\n"
    "v_exp_f32 v89, v89\n"
    "v_mfma_f32_16x16x32_bf16 a[188:191], v[28:31], v[120:123], a[188:191]\n"
    "s_waitcnt lgkmcnt(4)\n"
    "ds_read_b64_tr_b16 v[28:29], v6 offset:480\n"
    "ds_read_b64_tr_b16 v[30:31], v6 offset:992\n"
    "v_mfma_f32_16x16x32_bf16 a[192:195], v[16:19], v[96:99], a[192:195]\n"
    "v_exp_f32 v90, v90\n"
    "v_mfma_f32_16x16x32_bf16 a[196:199], v[16:19], v[104:107], a[196:199]\n"
    "v_exp_f32 v91, v91\n"
    "v_mfma_f32_16x16x32_bf16 a[200:203], v[16:19], v[112:115], a[200:203]\n"
    "v_exp_f32 v92, v92\n"
    "v_mfma_f32_16x16x32_bf16 a[204:207], v[16:19], v[120:123], a[204:207]\n"
    "s_waitcnt lgkmcnt(4)\n"
    "v_exp_f32 v93, v93\n"
    "v_mfma_f32_16x16x32_bf16 a[208:211], v[20:23], v[96:99], a[208:211]\n"
    "v_exp_f32 v94, v94\n"
    "v_mfma_f32_16x16x32_bf16 a[212:215], v[20:23], v[104:107], a[212:215]\n"
    "v_exp_f32 v95, v95\n"
    "v_mfma_f32_16x16x32_bf16 a[216:219], v[20:23], v[112:115], a[216:219]\n"
    "v_add_f32 v8, v8, v64\n"
    "v_add_f32 v8, v8, v65\n"
    "v_mfma_f32_16x16x32_bf16 a[220:223], v[20:23], v[120:123], a[220:223]\n"
    "s_waitcnt vmcnt(1) lgkmcnt(0)\n"
    "s_barrier\n"
    "ds_read_b128 v[16:19], v4\n"
    "v_mfma_f32_16x16x32_bf16 a[224:227], v[24:27], v[96:99], a[224:227]\n"
    "s_mov_b32 m0, s22\n"
    "ds_read_b128 v[20:23], v4 offset:512\n"
    "v_cvt_pk_bf16_f32 v64, v64, v65\n"
    "v_mfma_f32_16x16x32_bf16 a[228:231], v[24:27], v[104:107], a[228:231]\n"
    "global_load_lds_dwordx4 v0, s[16:17]\n"
    "v_add_f32 v8, v8, v66\n"
    "v_mfma_f32_16x16x32_bf16 a[232:235], v[24:27], v[112:115], a[232:235]\n"
    "v_add_f32 v8, v8, v67\n"
    "v_cvt_pk_bf16_f32 v65, v66, v67\n"
    "v_mfma_f32_16x16x32_bf16 a[236:239], v[24:27], v[120:123], a[236:239]\n"
    "ds_read_b128 v[24:27], v4 offset:64\n"
    "v_add_f32 v8, v8, v68\n"
    "v_mfma_f32_16x16x32_bf16 a[240:243], v[28:31], v[96:99], a[240:243]\n"
    "v_add_f32 v8, v8, v69\n"
    "v_cvt_pk_bf16_f32 v66, v68, v69\n"
    "v_mfma_f32_16x16x32_bf16 a[244:247], v[28:31], v[104:107], a[244:247]\n"
    "global_load_lds_dwordx4 v1, s[16:17] offset:1056\n"
    "v_add_f32 v8, v8, v70\n"
    "v_mfma_f32_16x16x32_bf16 a[248:251], v[28:31], v[112:115], a[248:251]\n"
    "v_add_f32 v8, v8, v71\n"
    "v_cvt_pk_bf16_f32 v67, v70, v71\n"
    "v_mfma_f32_16x16x32_bf16 a[252:255], v[28:31], v[120:123], a[252:255]\n"
    "s_waitcnt lgkmcnt(2)\n"
    "ds_read_b128 v[28:31], v4 offset:576\n"
    "v_add_f32 v9, v9, v72\n"
    "v_mfma_f32_16x16x32_bf16 v[96:99], v[16:19], v[128:131], v[32:35]\n"
    "v_add_f32 v9, v9, v73\n"
    "v_cvt_pk_bf16_f32 v72, v72, v73\n"
    "v_mfma_f32_16x16x32_bf16 v[104:107], v[16:19], v[160:163], v[36:39]\n"
    "global_load_lds_dwordx4 v2, s[16:17] offset:2112\n"
    "v_add_f32 v9, v9, v74\n"
    "v_mfma_f32_16x16x32_bf16 v[112:115], v[16:19], v[192:195], v[40:43]\n"
    "s_cmp_lg_u32 s37, 0\n"
    "s_cbranch_scc1 L_rb_p2\n"
    "L_rb_ret_p2:\n"
    "v_add_f32 v9, v9, v75\n"
    "v_mfma_f32_16x16x32_bf16 v[120:123], v[16:19], v[224:227], v[44:47]\n"
    "s_waitcnt lgkmcnt(2)\n"
    "ds_read_b128 v[16:19], v4 offset:128\n"
    "v_cvt_pk_bf16_f32 v73, v74, v75\n"
    "v_mfma_f32_16x16x32_bf16 v[100:103], v[20:23], v[128:131], v[32:35]\n"
    "v_add_f32 v9, v9, v76\n"
    "v_add_f32 v9, v9, v77\n"
    "v_mfma_f32_16x16x32_bf16 v[108:111], v[20:23], v[160:163], v[36:39]\n"
    "global_load_lds_dwordx4 v3, s[16:17] offset:3168\n"
    "v_cvt_pk_bf16_f32 v74, v76, v77\n"
    "v_mfma_f32_16x16x32_bf16 v[116:119], v[20:23], v[192:195], v[40:43]\n"
    "v_add_f32 v9, v9, v78\n"
    "v_add_f32 v9, v9, v79\n"
    "v_mfma_f32_16x16x32_bf16 v[124:127], v[20:23], v[224:227], v[44:47]\n"
    "s_waitcnt lgkmcnt(2)\n"
    "ds_read_b128 v[20:23], v4 offset:640\n"
    "v_cvt_pk_bf16_f32 v75, v78, v79\n"
    "v_mfma_f32_16x16x32_bf16 v[96:99], v[24:27], v[132:135], v[96:99]\n"
    "s_mov_b32 m0, s23\n"
    "v_add_f32 v10, v10, v80\n"
    "v_add_f32 v10, v10, v81\n"
    "v_mfma_f32_16x16x32_bf16 v[104:107], v[24:27], v[164:167], v[104:107]\n"
    "global_load_lds_dwordx4 v60, s[16:17]\n"
    "v_cvt_pk_bf16_f32 v80, v80, v81\n"
    "v_mfma_f32_16x16x32_bf16 v[112:115], v[24:27], v[196:199], v[112:115]\n"
    "v_add_f32 v10, v10, v82\n"
    "v_add_f32 v10, v10, v83\n"
    "v_mfma_f32_16x16x32_bf16 v[120:123], v[24:27], v[228:231], v[120:123]\n"
    "s_waitcnt lgkmcnt(2)\n"
    "ds_read_b128 v[24:27], v4 offset:192\n"
    "v_cvt_pk_bf16_f32 v81, v82, v83\n"
    "v_mfma_f32_16x16x32_bf16 v[100:103], v[28:31], v[132:135], v[100:103]\n"
    "v_add_f32 v10, v10, v84\n"
    "v_add_f32 v10, v10, v85\n"
    "v_mfma_f32_16x16x32_bf16 v[108:111], v[28:31], v[164:167], v[108:111]\n"
    "global_load_lds_dwordx4 v61, s[16:17] offset:1056\n"
    "v_cvt_pk_bf16_f32 v82, v84, v85\n"
    "v_mfma_f32_16x16x32_bf16 v[116:119], v[28:31], v[196:199], v[116:119]\n"
    "v_add_f32 v10, v10, v86\n"
    "v_add_f32 v10, v10, v87\n"
    "v_mfma_f32_16x16x32_bf16 v[124:127], v[28:31], v[228:231], v[124:127]\n"
    "s_waitcnt lgkmcnt(2)\n"
    "ds_read_b128 v[28:31], v4 offset:704\n"
    "v_cvt_pk_bf16_f32 v83, v86, v87\n"
    "v_mfma_f32_16x16x32_bf16 v[96:99], v[16:19], v[136:139], v[96:99]\n"
    "v_add_f32 v11, v11, v88\n"
    "v_add_f32 v11, v11, v89\n"
    "v_mfma_f32_16x16x32_bf16 v[104:107], v[16:19], v[168:171], v[104:107]\n"
    "global_load_lds_dwordx4 v62, s[16:17] offset:2112\n"
    "v_cvt_pk_bf16_f32 v88, v88, v89\n"
    "v_mfma_f32_16x16x32_bf16 v[112:115], v[16:19], v[200:203], v[112:115]\n"
    "v_add_f32 v11, v11, v90\n"
    "v_add_f32 v11, v11, v91\n"
    "v_mfma_f32_16x16x32_bf16 v[120:123], v[16:19], v[232:235], v[120:123]\n"
    "s_waitcnt lgkmcnt(2)\n"
    "ds_read_b128 v[16:19], v4 offset:256\n"
    "v_cvt_pk_bf16_f32 v89, v90, v91\n"
    "v_mfma_f32_16x16x32_bf16 v[100:103], v[20:23], v[136:139], v[100:103]\n"
    "v_add_f32 v11, v11, v92\n"
    "v_add_f32 v11, v11, v93\n"
    "v_mfma_f32_16x16x32_bf16 v[108:111], v[20:23], v[168:171], v[108:111]\n"
    "global_load_lds_dwordx4 v63, s[16:17] offset:3168\n"
    "v_cvt_pk_bf16_f32 v90, v92, v93\n"
    "v_mfma_f32_16x16x32_bf16 v[116:119], v[20:23], v[200:203], v[116:119]\n"
    "s_add_u32 s16, s16, 0x8000\n"
    "s_addc_u32 s17, s17, 0\n"
    "v_add_f32 v11, v11, v94\n"
    "v_mfma_f32_16x16x32_bf16 v[124:127], v[20:23], v[232:235], v[124:127]\n"
    "s_waitcnt lgkmcnt(2)\n"
    "ds_read_b128 v[20:23], v4 offset:768\n"
    "v_add_f32 v11, v11, v95\n"
    "v_mfma_f32_16x16x32_bf16 v[96:99], v[24:27], v[140:143], v[96:99]\n"
    "s_mov_b32 m0, s24\n"
    "v_cvt_pk_bf16_f32 v91, v94, v95\n"
    "v_mfma_f32_16x16x32_bf16 v[104:107], v[24:27], v[172:175], v[104:107]\n"
    "global_load_lds_dwordx4 v0, s[18:19]\n"
    "v_mfma_f32_16x16x32_bf16 v[112:115], v[24:27], v[204:207], v[112:115]\n"
    "v_mfma_f32_16x16x32_bf16 v[120:123], v[24:27], v[236:239], v[120:123]\n"
    "s_waitcnt lgkmcnt(2)\n"
    "ds_read_b128 v[24:27], v4 offset:320\n"
    "v_mfma_f32_16x16x32_bf16 v[100:103], v[28:31], v[140:143], v[100:103]\n"
    "v_mfma_f32_16x16x32_bf16 v[108:111], v[28:31], v[172:175], v[108:111]\n"
    "global_load_lds_dwordx4 v1, s[18:19] offset:1056\n"
    "v_mfma_f32_16x16x32_bf16 v[116:119], v[28:31], v[204:207], v[116:119]\n"
    "v_mfma_f32_16x16x32_bf16 v[124:127], v[28:31], v[236:239], v[124:127]\n"
    "s_waitcnt lgkmcnt(2)\n"
    "ds_read_b128 v[28:31], v4 offset:832\n"
    "v_mfma_f32_16x16x32_bf16 v[96:99], v[16:19], v[144:147], v[96:99]\n"
    "v_mfma_f32_16x16x32_bf16 v[104:107], v[16:19], v[176:179], v[104:107]\n"
    "global_load_lds_dwordx4 v2, s[18:19] offset:2112\n"
    "v_mfma_f32_16x16x32_bf16 v[112:115], v[16:19], v[208:211], v[112:115]\n"
    "v_mfma_f32_16x16x32_bf16 v[120:123], v[16:19], v[240:243], v[120:123]\n"
    "s_waitcnt lgkmcnt(2)\n"
    "ds_read_b128 v[16:19], v4 offset:384\n"
    "v_mfma_f32_16x16x32_bf16 v[100:103], v[20:23], v[144:147], v[100:103]\n"
    "v_mfma_f32_16x16x32_bf16 v[108:111], v[20:23], v[176:179], v[108:111]\n"
    "global_load_lds_dwordx4 v3, s[18:19] offset:3168\n"
    "v_mfma_f32_16x16x32_bf16 v[116:119], v[20:23], v[208:211], v[116:119]\n"
    "v_mfma_f32_16x16x32_bf16 v[124:127], v[20:23], v[240:243], v[124:127]\n"
    "s_waitcnt lgkmcnt(2)\n"
    "ds_read_b128 v[20:23], v4 offset:896\n"
    "v_mfma_f32_16x16x32_bf16 v[96:99], v[24:27], v[148:151], v[96:99]\n"
    "s_mov_b32 m0, s25\n"
    "v_mfma_f32_16x16x32_bf16 v[104:107], v[24:27], v[180:183], v[104:107]\n"
    "global_load_lds_dwordx4 v60, s[18:19]\n"
    "v_mfma_f32_16x16x32_bf16 v[112:115], v[24:27], v[212:215], v[112:115]\n"
    "v_mfma_f32_16x16x32_bf16 v[120:123], v[24:27], v[244:247], v[120:123]\n"
    "s_waitcnt lgkmcnt(2)\n"
    "ds_read_b128 v[24:27], v4 offset:448\n"
    "v_mfma_f32_16x16x32_bf16 v[100:103], v[28:31], v[148:151], v[100:103]\n"
    "v_mfma_f32_16x16x32_bf16 v[108:111], v[28:31], v[180:183], v[108:111]\n"
    "global_load_lds_dwordx4 v61, s[18:19] offset:1056\n"
    "v_mfma_f32_16x16x32_bf16 v[116:119], v[28:31], v[212:215], v[116:119]\n"
    "v_mfma_f32_16x16x32_bf16 v[124:127], v[28:31], v[244:247], v[124:127]\n"
    "s_waitcnt lgkmcnt(2)\n"
    "ds_read_b128 v[28:31], v4 offset:960\n"
    "v_mfma_f32_16x16x32_bf16 v[96:99], v[16:19], v[152:155], v[96:99]\n"
    "v_mfma_f32_16x16x32_bf16 v[104:107], v[16:19], v[184:187], v[104:107]\n"
    "global_load_lds_dwordx4 v62, s[18:19] offset:2112\n"
    "v_mfma_f32_16x16x32_bf16 v[112:115], v[16:19], v[216:219], v[112:115]\n"
    "v_mfma_f32_16x16x32_bf16 v[120:123], v[16:19], v[248:251], v[120:123]\n"
    "s_waitcnt lgkmcnt(2)\n"
    "ds_read_b64_tr_b16 v[16:17], v5 offset:33792\n"
    "ds_read_b64_tr_b16 v[18:19], v5 offset:34304\n"
    "v_mfma_f32_16x16x32_bf16 v[100:103], v[20:23], v[152:155], v[100:103]\n"
    "v_mfma_f32_16x16x32_bf16 v[108:111], v[20:23], v[184:187], v[108:111]\n"
    "global_load_lds_dwordx4 v63, s[18:19] offset:3168\n"
    "v_mfma_f32_16x16x32_bf16 v[116:119], v[20:23], v[216:219], v[116:119]\n"
    "s_add_u32 s18, s18, 0x8000\n"
    "s_addc_u32 s19, s19, 0\n"
    "v_mfma_f32_16x16x32_bf16 v[124:127], v[20:23], v[248:251], v[124:127]\n"
    "s_waitcnt lgkmcnt(3)\n"
    "ds_read_b64_tr_b16 v[20:21], v5 offset:33824\n"
    "ds_read_b64_tr_b16 v[22:23], v5 offset:34336\n"
    "v_mfma_f32_16x16x32_bf16 v[96:99], v[24:27], v[156:159], v[96:99]\n"
    "global_load_dword v57, v56, s[76:77]\n"
    "v_mfma_f32_16x16x32_bf16 v[104:107], v[24:27], v[188:191], v[104:107]\n"
    "v_mfma_f32_16x16x32_bf16 v[112:115], v[24:27], v[220:223], v[112:115]\n"
    "s_add_u32 s76, s76, s30\n"
    "s_addc_u32 s77, s77, 0\n"
    "v_mfma_f32_16x16x32_bf16 v[120:123], v[24:27], v[252:255], v[120:123]\n"
    "s_waitcnt lgkmcnt(4)\n"
    "ds_read_b64_tr_b16 v[24:25], v5 offset:33856\n"
    "ds_read_b64_tr_b16 v[26:27], v5 offset:34368\n"
    "v_mfma_f32_16x16x32_bf16 v[100:103], v[28:31], v[156:159], v[100:103]\n"
    "v_mfma_f32_16x16x32_bf16 v[108:111], v[28:31], v[188:191], v[108:111]\n"
    "v_mfma_f32_16x16x32_bf16 v[116:119], v[28:31], v[220:223], v[116:119]\n"
    "v_mfma_f32_16x16x32_bf16 v[124:127], v[28:31], v[252:255], v[124:127]\n"
    "s_waitcnt lgkmcnt(4)\n"
    "ds_read_b64_tr_b16 v[28:29], v5 offset:33888\n"
    "ds_read_b64_tr_b16 v[30:31], v5 offset:34400\n"
    "s_sub_u32 s36, s36, 0x400\n"
    "s_cmp_lg_u32 s36, 0\n"
    "s_cbranch_scc1 L_loop\n"
    "v_mfma_f32_16x16x32_bf16 a[0:3], v[16:19], v[64:67], a[0:3]\n"
    "v_mfma_f32_16x16x32_bf16 a[4:7], v[16:19], v[72:75], a[4:7]\n"
    "v_max3_f32 v7, v96, v97, v98\n"
    "v_max3_f32 v7, v7, v99, v104\n"
    "v_mfma_f32_16x16x32_bf16 a[8:11], v[16:19], v[80:83], a[8:11]\n"
    "s_cmp_lg_u32 s11, 0\n"
    "s_cbranch_scc0 L_mpub\n"
    "L_mpub_ret:\n"
    "v_max3_f32 v7, v7, v105, v106\n"
    "v_max3_f32 v7, v7, v107, v112\n"
    "v_mfma_f32_16x16x32_bf16 a[12:15], v[16:19], v[88:91], a[12:15]\n"
    "s_waitcnt lgkmcnt(4)\n"
    "ds_read_b64_tr_b16 v[16:17], v5 offset:33920\n"
    "ds_read_b64_tr_b16 v[18:19], v5 offset:34432\n"
    "v_mfma_f32_16x16x32_bf16 a[16:19], v[20:23], v[64:67], a[16:19]\n"
    "v_max3_f32 v7, v7, v113, v114\n"
    "v_max3_f32 v7, v7, v115, v120\n"
    "v_mfma_f32_16x16x32_bf16 a[20:23], v[20:23], v[72:75], a[20:23]\n"
    "v_max3_f32 v7, v7, v121, v122\n"
    "v_max3_f32 v7, v7, v123, v100\n"
    "v_mfma_f32_16x16x32_bf16 a[24:27], v[20:23], v[80:83], a[24:27]\n"
    "v_max3_f32 v7, v7, v101, v102\n"
    "v_max3_f32 v7, v7, v103, v108\n"
    "v_mfma_f32_16x16x32_bf16 a[28:31], v[20:23], v[88:91], a[28:31]\n"
    "s_waitcnt lgkmcnt(4)\n"
    "ds_read_b64_tr_b16 v[20:21], v5 offset:33952\n"
    "ds_read_b64_tr_b16 v[22:23], v5 offset:34464\n"
    "v_mfma_f32_16x16x32_bf16 a[32:35], v[24:27], v[64:67], a[32:35]\n"
    "v_max3_f32 v7, v7, v109, v110\n"
    "v_max3_f32 v7, v7, v111, v116\n"
    "v_mfma_f32_16x16x32_bf16 a[36:39], v[24:27], v[72:75], a[36:39]\n"
    "v_max3_f32 v7, v7, v117, v118\n"
    "v_max3_f32 v7, v7, v119, v124\n"
    "v_mfma_f32_16x16x32_bf16 a[40:43], v[24:27], v[80:83], a[40:43]\n"
    "v_max3_f32 v7, v7, v125, v126\n"
    "v_max_f32 v7, v7, v127\n"
    "v_mfma_f32_16x16x32_bf16 a[44:47], v[24:27], v[88:91], a[44:47]\n"
    "s_waitcnt lgkmcnt(4)\n"
    "ds_read_b64_tr_b16 v[24:25], v5 offset:33984\n"
    "ds_read_b64_tr_b16 v[26:27], v5 offset:34496\n"
    "v_mfma_f32_16x16x32_bf16 a[48:51], v[28:31], v[64:67], a[48:51]\n"
    "v_cmp_lt_f32 vcc, s38, v7\n"
    "v_mbcnt_lo_u32_b32 v240, -1, 0\n"
    "v_mfma_f32_16x16x32_bf16 a[52:55], v[28:31], v[72:75], a[52:55]\n"
    "s_cbranch_vccnz L_rareA_p31\n"
    "L_rareA_ret_p31:\n"
    "v_mbcnt_hi_u32_b32 v240, -1, v240\n"
    "v_mul_u32_u24 v248, 0x124a, v240\n"
    "v_mfma_f32_16x16x32_bf16 a[56:59], v[28:31], v[80:83], a[56:59]\n"
    "v_lshlrev_b32 v247, 4, v240\n"
    "v_add_u32 v249, 0xc00, v247\n"
    "v_mfma_f32_16x16x32_bf16 a[60:63], v[28:31], v[88:91], a[60:63]\n"
    "s_waitcnt lgkmcnt(4)\n"
    "ds_read_b64_tr_b16 v[28:29], v5 offset:34016\n"
    "ds_read_b64_tr_b16 v[30:31], v5 offset:34528\n"
    "v_mfma_f32_16x16x32_bf16 a[64:67], v[16:19], v[64:67], a[64:67]\n"
    "v_add_u32 v250, 0x1c00, v247\n"
    "v_lshrrev_b32 v0, 16, v248\n"
    "v_mfma_f32_16x16x32_bf16 a[68:71], v[16:19], v[72:75], a[68:71]\n"
    "v_mad_u32_u24 v0, v0, s74, v249\n"
    "v_add_u32 v1, 0x49280, v248\n"
    "v_mfma_f32_16x16x32_bf16 a[72:75], v[16:19], v[80:83], a[72:75]\n"
    "v_lshrrev_b32 v1, 16, v1\n"
    "v_mad_u32_u24 v1, v1, s74, v249\n"
    "v_mfma_f32_16x16x32_bf16 a[76:79], v[16:19], v[88:91], a[76:79]\n"
    "s_waitcnt lgkmcnt(4)\n"
    "ds_read_b64_tr_b16 v[16:17], v6 offset:34048\n"
    "ds_read_b64_tr_b16 v[18:19], v6 offset:34560\n"
    "v_mfma_f32_16x16x32_bf16 a[80:83], v[20:23], v[64:67], a[80:83]\n"
    "v_add_u32 v2, 0x92500, v248\n"
    "v_lshrrev_b32 v2, 16, v2\n"
    "v_mfma_f32_16x16x32_bf16 a[84:87], v[20:23], v[72:75], a[84:87]\n"
    "v_mad_u32_u24 v2, v2, s74, v249\n"
    "v_add_u32 v3, 0xdb780, v248\n"
    "v_mfma_f32_16x16x32_bf16 a[88:91], v[20:23], v[80:83], a[88:91]\n"
    "v_lshrrev_b32 v3, 16, v3\n"
    "v_mad_u32_u24 v3, v3, s74, v249\n"
    "v_mfma_f32_16x16x32_bf16 a[92:95], v[20:23], v[88:91], a[92:95]\n"
    "s_waitcnt lgkmcnt(4)\n"
    "ds_read_b64_tr_b16 v[20:21], v6 offset:34080\n"
    "ds_read_b64_tr_b16 v[22:23], v6 offset:34592\n"
    "v_mfma_f32_16x16x32_bf16 a[96:99], v[24:27], v[64:67], a[96:99]\n"
    "v_add_u32 v60, 0x124a00, v248\n"
    "v_lshrrev_b32 v60, 16, v60\n"
    "v_mfma_f32_16x16x32_bf16 a[100:103], v[24:27], v[72:75], a[100:103]\n"
    "v_mad_u32_u24 v60, v60, s74, v250\n"
    "v_add_u32 v61, 0x16dc80, v248\n"
    "v_mfma_f32_16x16x32_bf16 a[104:107], v[24:27], v[80:83], a[104:107]\n"
    "v_lshrrev_b32 v61, 16, v61\n"
    "v_mad_u32_u24 v61, v61, s74, v250\n"
    "v_mfma_f32_16x16x32_bf16 a[108:111], v[24:27], v[88:91], a[108:111]\n"
    "s_waitcnt lgkmcnt(4)\n"
    "ds_read_b64_tr_b16 v[24:25], v6 offset:34112\n"
    "ds_read_b64_tr_b16 v[26:27], v6 offset:34624\n"
    "v_mfma_f32_16x16x32_bf16 a[112:115], v[28:31], v[64:67], a[112:115]\n"
    "v_add_u32 v62, 0x1b6f00, v248\n"
    "v_lshrrev_b32 v62, 16, v62\n"
    "v_mfma_f32_16x16x32_bf16 a[116:119], v[28:31], v[72:75], a[116:119]\n"
    "v_mad_u32_u24 v62, v62, s74, v250\n"
    "s_add_u32 m0, s75, 0x21000\n"
    "v_mfma_f32_16x16x32_bf16 a[120:123], v[28:31], v[80:83], a[120:123]\n"
    "global_load_lds_dwordx4 v0, s[70:71]\n"
    "global_load_lds_dwordx4 v1, s[70:71] offset:1024\n"
    "v_mfma_f32_16x16x32_bf16 a[124:127], v[28:31], v[88:91], a[124:127]\n"
    "s_waitcnt lgkmcnt(4)\n"
    "ds_read_b64_tr_b16 v[28:29], v6 offset:34144\n"
    "ds_read_b64_tr_b16 v[30:31], v6 offset:34656\n"
    "v_mfma_f32_16x16x32_bf16 a[128:131], v[16:19], v[64:67], a[128:131]\n"
    "global_load_lds_dwordx4 v2, s[70:71] offset:2048\n"
    "global_load_lds_dwordx4 v3, s[70:71] offset:3072\n"
    "v_mfma_f32_16x16x32_bf16 a[132:135], v[16:19], v[72:75], a[132:135]\n"
    "s_add_u32 m0, s75, 0x22000\n"
    "s_nop 0\n"
    "global_load_lds_dwordx4 v60, s[70:71]\n"
    "global_load_lds_dwordx4 v61, s[70:71] offset:1024\n"
    "v_mfma_f32_16x16x32_bf16 a[136:139], v[16:19], v[80:83], a[136:139]\n"
    "global_load_lds_dwordx4 v62, s[70:71] offset:2048\n"
    "s_add_u32 s70, s70, 0x1c000\n"
    "v_mfma_f32_16x16x32_bf16 a[140:143], v[16:19], v[88:91], a[140:143]\n"
    "s_waitcnt lgkmcnt(4)\n"
    "ds_read_b64_tr_b16 v[16:17], v6 offset:34176\n"
    "ds_read_b64_tr_b16 v[18:19], v6 offset:34688\n"
    "v_mfma_f32_16x16x32_bf16 a[144:147], v[20:23], v[64:67], a[144:147]\n"
    "s_addc_u32 s71, s71, 0\n"
    "v_exp_f32 v96, v96\n"
    "v_mfma_f32_16x16x32_bf16 a[148:151], v[20:23], v[72:75], a[148:151]\n"
    "v_exp_f32 v97, v97\n"
    "v_mfma_f32_16x16x32_bf16 a[152:155], v[20:23], v[80:83], a[152:155]\n"
    "v_exp_f32 v98, v98\n"
    "v_mfma_f32_16x16x32_bf16 a[156:159], v[20:23], v[88:91], a[156:159]\n"
    "s_waitcnt lgkmcnt(4)\n"
    "ds_read_b64_tr_b16 v[20:21], v6 offset:34208\n"
    "ds_read_b64_tr_b16 v[22:23], v6 offset:34720\n"
    "v_mfma_f32_16x16x32_bf16 a[160:163], v[24:27], v[64:67], a[160:163]\n"
    "v_exp_f32 v99, v99\n"
    "v_mfma_f32_16x16x32_bf16 a[164:167], v[24:27], v[72:75], a[164:167]\n"
    "v_exp_f32 v100, v100\n"
    "v_mfma_f32_16x16x32_bf16 a[168:171], v[24:27], v[80:83], a[168:171]\n"
    "v_exp_f32 v101, v101\n"
    "v_mfma_f32_16x16x32_bf16 a[172:175], v[24:27], v[88:91], a[172:175]\n"
    "s_waitcnt lgkmcnt(4)\n"
    "ds_read_b64_tr_b16 v[24:25], v6 offset:34240\n"
    "ds_read_b64_tr_b16 v[26:27], v6 offset:34752\n"
    "v_mfma_f32_16x16x32_bf16 a[176:179], v[28:31], v[64:67], a[176:179]\n"
    "v_exp_f32 v102, v102\n"
    "v_mfma_f32_16x16x32_bf16 a[180:183], v[28:31], v[72:75], a[180:183]\n"
    "v_exp_f32 v103, v103\n"
    "v_mfma_f32_16x16x32_bf16 a[184:187], v[28:31], v[80:83], a[184:187]\n"
    "v_exp_f32 v104, v104\n"
    "v_mfma_f32_16x16x32_bf16 a[188:191], v[28:31], v[88:91], a[188:191]\n"
    "s_waitcnt lgkmcnt(4)\n"
    "ds_read_b64_tr_b16 v[28:29], v6 offset:34272\n"
    "ds_read_b64_tr_b16 v[30:31], v6 offset:34784\n"
    "v_mfma_f32_16x16x32_bf16 a[192:195], v[16:19], v[64:67], a[192:195]\n"
    "v_exp_f32 v105, v105\n"
    "v_mfma_f32_16x16x32_bf16 a[196:199], v[16:19], v[72:75], a[196:199]\n"
    "v_exp_f32 v106, v106\n"
    "v_mfma_f32_16x16x32_bf16 a[200:203], v[16:19], v[80:83], a[200:203]\n"
    "v_exp_f32 v107, v107\n"
    "v_mfma_f32_16x16x32_bf16 a[204:207], v[16:19], v[88:91], a[204:207]\n"
    "s_waitcnt lgkmcnt(4)\n"
    "v_exp_f32 v108, v108\n"
    "v_mfma_f32_16x16x32_bf16 a[208:211], v[20:23], v[64:67], a[208:211]\n"
    "v_exp_f32 v109, v109\n"
    "v_mfma_f32_16x16x32_bf16 a[212:215], v[20:23], v[72:75], a[212:215]\n"
    "v_exp_f32 v110, v110\n"
    "v_mfma_f32_16x16x32_bf16 a[216:219], v[20:23], v[80:83], a[216:219]\n"
    "v_exp_f32 v111, v111\n"
    "v_mfma_f32_16x16x32_bf16 a[220:223], v[20:23], v[88:91], a[220:223]\n"
    "s_waitcnt vmcnt(7) lgkmcnt(0)\n"
    "s_barrier\n"
    "v_lshrrev_b32 v248, 2, v240\n"
    "v_mfma_f32_16x16x32_bf16 a[224:227], v[24:27], v[64:67], a[224:227]\n"
    "s_add_u32 m0, s75, 0x1a000\n"
    "v_exp_f32 v112, v112\n"
    "v_mfma_f32_16x16x32_bf16 a[228:231], v[24:27], v[72:75], a[228:231]\n"
    "global_load_lds_dwordx4 v0, s[70:71]\n"
    "v_mul_u32_u24 v248, 0xe0, v248\n"
    "v_mfma_f32_16x16x32_bf16 a[232:235], v[24:27], v[80:83], a[232:235]\n"
    "global_load_lds_dwordx4 v1, s[70:71] offset:1024\n"
    "v_and_b32 v249, 3, v240\n"
    "v_mfma_f32_16x16x32_bf16 a[236:239], v[24:27], v[88:91], a[236:239]\n"
    "global_load_lds_dwordx4 v2, s[70:71] offset:2048\n"
    "v_lshl_add_u32 v248, v249, 3, v248\n"
    "v_mfma_f32_16x16x32_bf16 a[240:243], v[28:31], v[64:67], a[240:243]\n"
    "global_load_lds_dwordx4 v3, s[70:71] offset:3072\n"
    "v_add_u32 v248, s75, v248\n"
    "v_mfma_f32_16x16x32_bf16 a[244:247], v[28:31], v[72:75], a[244:247]\n"
    "s_add_u32 m0, s75, 0x1b000\n"
    "v_exp_f32 v113, v113\n"
    "v_mfma_f32_16x16x32_bf16 a[248:251], v[28:31], v[80:83], a[248:251]\n"
    "global_load_lds_dwordx4 v60, s[70:71]\n"
    "v_add_u32 v4, 0x21000, v248\n"
    "v_mfma_f32_16x16x32_bf16 a[252:255], v[28:31], v[88:91], a[252:255]\n"
    "global_load_lds_dwordx4 v61, s[70:71] offset:1024\n"
    "v_add_u32 v56, 0x1a000, v248\n"
    "s_cmp_lg_u32 s37, 0\n"
    "s_cbranch_scc1 L_rb_p31\n"
    "L_rb_ret_p31:\n"
    "global_load_lds_dwordx4 v62, s[70:71] offset:2048\n"
    "s_add_u32 s70, s70, 0x1c000\n"
    "s_addc_u32 s71, s71, 0\n"
    "ds_read_b64_tr_b16 v[16:17], v6 offset:256\n"
    "ds_read_b64_tr_b16 v[18:19], v6 offset:768\n"
    "ds_read_b64_tr_b16 v[20:21], v6 offset:288\n"
    "ds_read_b64_tr_b16 v[22:23], v6 offset:800\n"
    "ds_read_b64_tr_b16 v[24:25], v5\n"
    "ds_read_b64_tr_b16 v[26:27], v5 offset:512\n"
    "s_waitcnt lgkmcnt(4)\n"
    "ds_read_b64_tr_b16 v[28:29], v5 offset:32\n"
    "ds_read_b64_tr_b16 v[30:31], v5 offset:544\n"
    "v_add_u32 v57, 0x13000, v248\n"
    "v_add_u32 v58, 0xc000, v248\n"
    "s_lshl_b32 s44, s11, 13\n"
    "s_xor_b32 s45, s44, 0x2000\n"
    "v_add_u32 v244, s44, v247\n"
    "v_add_u32 v243, s45, v247\n"
    "s_lshl_b32 s44, s11, 12\n"
    "s_add_u32 s44, s44, 0x8000\n"
    "v_add_u32 v245, s44, v247\n"
    "v_add_u32 v246, 0x8000, v247\n"
    "v_and_b32 v248, 15, v240\n"
    "v_lshlrev_b32 v248, 3, v248\n"
    "s_lshl_b32 s44, s11, 9\n"
    "s_add_u32 s44, s44, 0x18c00\n"
    "s_xor_b32 s45, s44, 0x200\n"
    "v_add_u32 v241, s44, v248\n"
    "v_add_u32 v242, s45, v248\n"
    "v_exp_f32 v114, v114\n"
    "v_exp_f32 v115, v115\n"
    "v_exp_f32 v116, v116\n"
    "v_exp_f32 v117, v117\n"
    "v_exp_f32 v118, v118\n"
    "v_exp_f32 v119, v119\n"
    "v_exp_f32 v120, v120\n"
    "v_exp_f32 v121, v121\n"
    "v_exp_f32 v122, v122\n"
    "v_exp_f32 v123, v123\n"
    "v_exp_f32 v124, v124\n"
    "v_exp_f32 v125, v125\n"
    "v_exp_f32 v126, v126\n"
    "v_exp_f32 v127, v127\n"
    "v_add_f32 v8, v8, v96\n"
    "v_add_f32 v8, v8, v97\n"
    "v_cvt_pk_bf16_f32 v96, v96, v97\n"
    "v_add_f32 v8, v8, v98\n"
    "v_add_f32 v8, v8, v99\n"
    "v_cvt_pk_bf16_f32 v97, v98, v99\n"
    "v_add_f32 v8, v8, v100\n"
    "v_add_f32 v8, v8, v101\n"
    "v_cvt_pk_bf16_f32 v98, v100, v101\n"
    "v_add_f32 v8, v8, v102\n"
    "v_add_f32 v8, v8, v103\n"
    "v_cvt_pk_bf16_f32 v99, v102, v103\n"
    "v_add_f32 v9, v9, v104\n"
    "v_add_f32 v9, v9, v105\n"
    "v_cvt_pk_bf16_f32 v104, v104, v105\n"
    "v_add_f32 v9, v9, v106\n"
    "v_add_f32 v9, v9, v107\n"
    "v_cvt_pk_bf16_f32 v105, v106, v107\n"
    "v_add_f32 v9, v9, v108\n"
    "v_add_f32 v9, v9, v109\n"
    "v_cvt_pk_bf16_f32 v106, v108, v109\n"
    "v_add_f32 v9, v9, v110\n"
    "v_add_f32 v9, v9, v111\n"
    "v_cvt_pk_bf16_f32 v107, v110, v111\n"
    "v_add_f32 v10, v10, v112\n"
    "v_add_f32 v10, v10, v113\n"
    "v_cvt_pk_bf16_f32 v112, v112, v113\n"
    "v_add_f32 v10, v10, v114\n"
    "v_add_f32 v10, v10, v115\n"
    "v_cvt_pk_bf16_f32 v113, v114, v115\n"
    "v_add_f32 v10, v10, v116\n"
    "v_add_f32 v10, v10, v117\n"
    "v_cvt_pk_bf16_f32 v114, v116, v117\n"
    "v_add_f32 v10, v10, v118\n"
    "v_add_f32 v10, v10, v119\n"
    "v_cvt_pk_bf16_f32 v115, v118, v119\n"
    "v_add_f32 v11, v11, v120\n"
    "v_add_f32 v11, v11, v121\n"
    "v_cvt_pk_bf16_f32 v120, v120, v121\n"
    "v_add_f32 v11, v11, v122\n"
    "v_add_f32 v11, v11, v123\n"
    "v_cvt_pk_bf16_f32 v121, v122, v123\n"
    "v_add_f32 v11, v11, v124\n"
    "v_add_f32 v11, v11, v125\n"
    "v_cvt_pk_bf16_f32 v122, v124, v125\n"
    "v_add_f32 v11, v11, v126\n"
    "v_add_f32 v11, v11, v127\n"
    "v_cvt_pk_bf16_f32 v123, v126, v127\n"
    "v_mov_b32 v248, v8\n"
    "v_mov_b32 v249, v9\n"
    "v_mov_b32 v250, v10\n"
    "v_mov_b32 v251, v11\n"
    "v_permlane16_swap_b32 v8, v248\n"
    "v_permlane16_swap_b32 v9, v249\n"
    "v_permlane16_swap_b32 v10, v250\n"
    "v_permlane16_swap_b32 v11, v251\n"
    "v_add_f32 v8, v8, v248\n"
    "v_add_f32 v9, v9, v249\n"
    "v_add_f32 v10, v10, v250\n"
    "v_add_f32 v11, v11, v251\n"
    "v_mov_b32 v248, v8\n"
    "v_mov_b32 v249, v9\n"
    "v_mov_b32 v250, v10\n"
    "v_mov_b32 v251, v11\n"
    "v_permlane32_swap_b32 v8, v248\n"
    "v_permlane32_swap_b32 v9, v249\n"
    "v_permlane32_swap_b32 v10, v250\n"
    "v_permlane32_swap_b32 v11, v251\n"
    "v_add_f32 v8, v8, v248\n"
    "v_add_f32 v9, v9, v249\n"
    "v_add_f32 v10, v10, v250\n"
    "v_add_f32 v11, v11, v251\n"
    "v_sub_f32 v48, 0, v32\n"
    "v_mov_b32 v49, v8\n"
    "ds_write_b64 v241, v[48:49]\n"
    "v_sub_f32 v50, 0, v36\n"
    "v_mov_b32 v51, v9\n"
    "ds_write_b64 v241, v[50:51] offset:128\n"
    "v_sub_f32 v52, 0, v40\n"
    "v_mov_b32 v53, v10\n"
    "ds_write_b64 v241, v[52:53] offset:256\n"
    "v_sub_f32 v54, 0, v44\n"
    "v_mov_b32 v55, v11\n"
    "ds_write_b64 v241, v[54:55] offset:384\n"
    "s_waitcnt lgkmcnt(0)\n"
    "s_barrier\n"
    "v_mfma_f32_16x16x32_bf16 a[128:131], v[16:19], v[96:99], a[128:131]\n"
    "s_lshl_b32 s44, s12, 8\n"
    "s_add_u32 s44, s44, 0x3001000\n"
    "ds_read_b64 v[48:49], v242\n"
    "ds_read_b64 v[50:51], v242 offset:128\n"
    "ds_read_b64 v[52:53], v242 offset:256\n"
    "v_mfma_f32_16x16x32_bf16 a[132:135], v[16:19], v[104:107], a[132:135]\n"
    "s_add_u32 s28, s68, s44\n"
    "s_addc_u32 s29, s69, 0\n"
    "ds_read_b64 v[54:55], v242 offset:384\n"
    "v_sub_f32 v248, 0, v32\n"
    "s_waitcnt lgkmcnt(3)\n"
    "v_max_f32 v249, v248, v48\n"
    "v_mfma_f32_16x16x32_bf16 a[136:139], v[16:19], v[112:115], a[136:139]\n"
    "s_add_u32 s30, s28, 0x3000\n"
    "s_addc_u32 s31, s29, 0\n"
    "ds_write_b128 v243, a[128:131]\n"
    "v_mfma_f32_16x16x32_bf16 a[140:143], v[16:19], v[120:123], a[140:143]\n"
    "ds_read_b64_tr_b16 v[16:17], v6 offset:320\n"
    "ds_read_b64_tr_b16 v[18:19], v6 offset:832\n"
    "ds_write_b128 v243, a[132:135] offset:1024\n"
    "v_mfma_f32_16x16x32_bf16 a[144:147], v[20:23], v[96:99], a[144:147]\n"
    "s_lshl_b32 s45, s12, 2\n"
    "s_lshr_b32 s46, s13, 1\n"
    "s_add_u32 s45, s45, s46\n"
    "ds_write_b128 v243, a[136:139] offset:2048\n"
    "v_mfma_f32_16x16x32_bf16 a[148:151], v[20:23], v[104:107], a[148:151]\n"
    "s_mul_i32 s45, s45, 14\n"
    "s_and_b32 s46, s13, 1\n"
    "s_mul_i32 s46, s46, 7\n"
    "ds_write_b128 v243, a[140:143] offset:3072\n"
    "v_mfma_f32_16x16x32_bf16 a[152:155], v[20:23], v[112:115], a[152:155]\n"
    "s_add_u32 s45, s45, s46\n"
    "s_lshl_b32 s45, s45, 10\n"
    "ds_write_b128 v243, a[144:147] offset:4096\n"
    "v_mfma_f32_16x16x32_bf16 a[156:159], v[20:23], v[120:123], a[156:159]\n"
    "ds_read_b64_tr_b16 v[20:21], v6 offset:352\n"
    "ds_read_b64_tr_b16 v[22:23], v6 offset:864\n"
    "ds_write_b128 v243, a[148:151] offset:5120\n"
    "v_mfma_f32_16x16x32_bf16 a[0:3], v[24:27], v[96:99], a[0:3]\n"
    "s_add_u32 s32, s68, s45\n"
    "s_addc_u32 s33, s69, 0\n"
    "ds_write_b128 v243, a[152:155] offset:6144\n"
    "v_mfma_f32_16x16x32_bf16 a[4:7], v[24:27], v[104:107], a[4:7]\n"
    "s_add_u32 s34, s32, 0x1000000\n"
    "s_addc_u32 s35, s33, 0\n"
    "ds_write_b128 v243, a[156:159] offset:7168\n"
    "v_mfma_f32_16x16x32_bf16 a[8:11], v[24:27], v[112:115], a[8:11]\n"
    "s_mov_b32 s76, 0x3f80\n"
    "s_mov_b32 s77, 0x3f800000\n"
    "v_sub_f32 v250, v248, v249\n"
    "v_sub_f32 v251, v48, v249\n"
    "v_mfma_f32_16x16x32_bf16 a[12:15], v[24:27], v[120:123], a[12:15]\n"
    "ds_read_b64_tr_b16 v[24:25], v5 offset:64\n"
    "ds_read_b64_tr_b16 v[26:27], v5 offset:576\n"
    "v_exp_f32 v250, v250\n"
    "v_mfma_f32_16x16x32_bf16 a[16:19], v[28:31], v[96:99], a[16:19]\n"
    "s_mul_i32 s44, s11, 0x70\n"
    "s_add_u32 s45, s44, 0x6f\n"
    "v_exp_f32 v251, v251\n"
    "v_mul_f32 v252, v250, v8\n"
    "v_mfma_f32_16x16x32_bf16 a[20:23], v[28:31], v[104:107], a[20:23]\n"
    "s_lshr_b32 s47, s13, 1\n"
    "s_mul_i32 s47, s47, 0xe0\n"
    "s_mul_i32 s36, s12, 0xe000\n"
    "v_fma_f32 v252, v251, v49, v252\n"
    "v_mfma_f32_16x16x32_bf16 a[24:27], v[28:31], v[112:115], a[24:27]\n"
    "v_rcp_f32 v252, v252\n"
    "s_nop 0\n"
    "v_mul_f32 v12, v250, v252\n"
    "v_mul_f32 v8, v251, v252\n"
    "v_mfma_f32_16x16x32_bf16 a[28:31], v[28:31], v[120:123], a[28:31]\n"
    "s_waitcnt lgkmcnt(11)\n"
    "s_add_u32 s38, s48, 0x3800\n"
    "s_addc_u32 s39, s49, 0\n"
    "ds_read_b64_tr_b16 v[28:29], v5 offset:96\n"
    "ds_read_b64_tr_b16 v[30:31], v5 offset:608\n"
    "v_mfma_f32_16x16x32_bf16 a[160:163], v[16:19], v[96:99], a[160:163]\n"
    "v_sub_f32 v248, 0, v36\n"
    "v_max_f32 v249, v248, v50\n"
    "v_sub_f32 v250, v248, v249\n"
    "v_sub_f32 v251, v50, v249\n"
    "v_mfma_f32_16x16x32_bf16 a[164:167], v[16:19], v[104:107], a[164:167]\n"
    "v_exp_f32 v250, v250\n"
    "v_exp_f32 v251, v251\n"
    "v_mfma_f32_16x16x32_bf16 a[168:171], v[16:19], v[112:115], a[168:171]\n"
    "v_mul_f32 v252, v250, v9\n"
    "v_fma_f32 v252, v251, v51, v252\n"
    "v_rcp_f32 v252, v252\n"
    "v_mfma_f32_16x16x32_bf16 a[172:175], v[16:19], v[120:123], a[172:175]\n"
    "s_waitcnt lgkmcnt(7)\n"
    "ds_read_b64_tr_b16 v[16:17], v6 offset:384\n"
    "ds_read_b64_tr_b16 v[18:19], v6 offset:896\n"
    "v_mul_f32 v13, v250, v252\n"
    "v_mul_f32 v9, v251, v252\n"
    "v_mfma_f32_16x16x32_bf16 a[176:179], v[20:23], v[96:99], a[176:179]\n"
    "v_sub_f32 v248, 0, v40\n"
    "v_max_f32 v249, v248, v52\n"
    "v_sub_f32 v250, v248, v249\n"
    "v_sub_f32 v251, v52, v249\n"
    "v_mfma_f32_16x16x32_bf16 a[180:183], v[20:23], v[104:107], a[180:183]\n"
    "v_exp_f32 v250, v250\n"
    "v_exp_f32 v251, v251\n"
    "v_mfma_f32_16x16x32_bf16 a[184:187], v[20:23], v[112:115], a[184:187]\n"
    "v_mul_f32 v252, v250, v10\n"
    "v_fma_f32 v252, v251, v53, v252\n"
    "v_rcp_f32 v252, v252\n"
    "v_mfma_f32_16x16x32_bf16 a[188:191], v[20:23], v[120:123], a[188:191]\n"
    "s_waitcnt lgkmcnt(4)\n"
    "ds_read_b64_tr_b16 v[20:21], v6 offset:416\n"
    "ds_read_b64_tr_b16 v[22:23], v6 offset:928\n"
    "v_mul_f32 v14, v250, v252\n"
    "v_mul_f32 v10, v251, v252\n"
    "v_mfma_f32_16x16x32_bf16 a[32:35], v[24:27], v[96:99], a[32:35]\n"
    "v_sub_f32 v248, 0, v44\n"
    "v_max_f32 v249, v248, v54\n"
    "v_sub_f32 v250, v248, v249\n"
    "v_sub_f32 v251, v54, v249\n"
    "v_mfma_f32_16x16x32_bf16 a[36:39], v[24:27], v[104:107], a[36:39]\n"
    "v_exp_f32 v250, v250\n"
    "v_exp_f32 v251, v251\n"
    "v_mfma_f32_16x16x32_bf16 a[40:43], v[24:27], v[112:115], a[40:43]\n"
    "v_mul_f32 v252, v250, v11\n"
    "v_fma_f32 v252, v251, v55, v252\n"
    "v_rcp_f32 v252, v252\n"
    "v_mfma_f32_16x16x32_bf16 a[44:47], v[24:27], v[120:123], a[44:47]\n"
    "s_waitcnt lgkmcnt(4)\n"
    "ds_read_b64_tr_b16 v[24:25], v5 offset:128\n"
    "ds_read_b64_tr_b16 v[26:27], v5 offset:640\n"
    "v_mul_f32 v15, v250, v252\n"
    "v_mul_f32 v11, v251, v252\n"
    "v_mfma_f32_16x16x32_bf16 a[48:51], v[28:31], v[96:99], a[48:51]\n"
    "v_add_u32 v45, s44, v240\n"
    "v_add_u32 v46, 64, v45\n"
    "v_min_u32 v46, s45, v46\n"
    "v_mfma_f32_16x16x32_bf16 a[52:55], v[28:31], v[104:107], a[52:55]\n"
    "v_lshlrev_b32 v45, 4, v45\n"
    "v_lshlrev_b32 v46, 4, v46\n"
    "v_and_b32 v7, 7, v240\n"
    "v_mfma_f32_16x16x32_bf16 a[56:59], v[28:31], v[112:115], a[56:59]\n"
    "v_lshlrev_b32 v7, 3, v7\n"
    "v_mov_b32 v59, s13\n"
    "v_lshlrev_b32 v59, 3, v59\n"
    "v_mfma_f32_16x16x32_bf16 a[60:63], v[28:31], v[120:123], a[60:63]\n"
    "s_waitcnt lgkmcnt(4)\n"
    "ds_read_b64_tr_b16 v[28:29], v5 offset:160\n"
    "ds_read_b64_tr_b16 v[30:31], v5 offset:672\n"
    "s_waitcnt lgkmcnt(0)\n"
    "s_barrier\n"
    "ds_read_b128 v[64:67], v244\n"
    "ds_read_b128 v[68:71], v244 offset:1024\n"
    "ds_read_b128 v[72:75], v244 offset:2048\n"
    "ds_read_b128 v[76:79], v244 offset:3072\n"
    "ds_read_b128 v[80:83], v244 offset:4096\n"
    "ds_read_b128 v[84:87], v244 offset:5120\n"
    "ds_read_b128 v[88:91], v244 offset:6144\n"
    "ds_read_b128 v[92:95], v244 offset:7168\n"
    "v_mfma_f32_16x16x32_bf16 a[192:195], v[16:19], v[96:99], a[192:195]\n"
    "v_accvgpr_read_b32 v48, a0\n"
    "v_accvgpr_read_b32 v49, a1\n"
    "v_accvgpr_read_b32 v50, a2\n"
    "v_accvgpr_read_b32 v51, a3\n"
    "v_mul_f32 v48, v48, v12\n"
    "v_mul_f32 v49, v49, v12\n"
    "v_mul_f32 v50, v50, v12\n"
    "v_mul_f32 v51, v51, v12\n"
    "v_mfma_f32_16x16x32_bf16 a[196:199], v[16:19], v[104:107], a[196:199]\n"
    "s_waitcnt lgkmcnt(7)\n"
    "v_fma_f32 v64, v64, v8, v48\n"
    "v_fma_f32 v65, v65, v8, v49\n"
    "v_fma_f32 v66, v66, v8, v50\n"
    "v_fma_f32 v67, v67, v8, v51\n"
    "v_accvgpr_read_b32 v52, a16\n"
    "v_accvgpr_read_b32 v53, a17\n"
    "v_accvgpr_read_b32 v54, a18\n"
    "v_accvgpr_read_b32 v55, a19\n"
    "v_mfma_f32_16x16x32_bf16 a[200:203], v[16:19], v[112:115], a[200:203]\n"
    "v_mul_f32 v52, v52, v12\n"
    "v_mul_f32 v53, v53, v12\n"
    "v_mul_f32 v54, v54, v12\n"
    "v_mul_f32 v55, v55, v12\n"
    "s_waitcnt lgkmcnt(3)\n"
    "v_fma_f32 v80, v80, v8, v52\n"
    "v_fma_f32 v81, v81, v8, v53\n"
    "v_fma_f32 v82, v82, v8, v54\n"
    "v_fma_f32 v83, v83, v8, v55\n"
    "v_mfma_f32_16x16x32_bf16 a[204:207], v[16:19], v[120:123], a[204:207]\n"
    "ds_read_b64_tr_b16 v[16:17], v6 offset:448\n"
    "ds_read_b64_tr_b16 v[18:19], v6 offset:960\n"
    "v_cvt_pk_bf16_f32 v64, v64, v65\n"
    "v_cvt_pk_bf16_f32 v65, v66, v67\n"
    "v_cvt_pk_bf16_f32 v66, v80, v81\n"
    "v_cvt_pk_bf16_f32 v67, v82, v83\n"
    "ds_write_b128 v245, v[64:67]\n"
    "v_mfma_f32_16x16x32_bf16 a[208:211], v[20:23], v[96:99], a[208:211]\n"
    "v_accvgpr_read_b32 v48, a4\n"
    "v_accvgpr_read_b32 v49, a5\n"
    "v_accvgpr_read_b32 v50, a6\n"
    "v_accvgpr_read_b32 v51, a7\n"
    "v_mul_f32 v48, v48, v13\n"
    "v_mul_f32 v49, v49, v13\n"
    "v_mul_f32 v50, v50, v13\n"
    "v_mul_f32 v51, v51, v13\n"
    "v_mfma_f32_16x16x32_bf16 a[212:215], v[20:23], v[104:107], a[212:215]\n"
    "v_fma_f32 v68, v68, v9, v48\n"
    "v_fma_f32 v69, v69, v9, v49\n"
    "v_fma_f32 v70, v70, v9, v50\n"
    "v_fma_f32 v71, v71, v9, v51\n"
    "v_accvgpr_read_b32 v52, a20\n"
    "v_accvgpr_read_b32 v53, a21\n"
    "v_accvgpr_read_b32 v54, a22\n"
    "v_accvgpr_read_b32 v55, a23\n"
    "v_mfma_f32_16x16x32_bf16 a[216:219], v[20:23], v[112:115], a[216:219]\n"
    "v_mul_f32 v52, v52, v13\n"
    "v_mul_f32 v53, v53, v13\n"
    "v_mul_f32 v54, v54, v13\n"
    "v_mul_f32 v55, v55, v13\n"
    "s_waitcnt lgkmcnt(5)\n"
    "v_fma_f32 v84, v84, v9, v52\n"
    "v_fma_f32 v85, v85, v9, v53\n"
    "v_fma_f32 v86, v86, v9, v54\n"
    "v_fma_f32 v87, v87, v9, v55\n"
    "v_mfma_f32_16x16x32_bf16 a[220:223], v[20:23], v[120:123], a[220:223]\n"
    "ds_read_b64_tr_b16 v[20:21], v6 offset:480\n"
    "ds_read_b64_tr_b16 v[22:23], v6 offset:992\n"
    "v_cvt_pk_bf16_f32 v68, v68, v69\n"
    "v_cvt_pk_bf16_f32 v69, v70, v71\n"
    "v_cvt_pk_bf16_f32 v70, v84, v85\n"
    "v_cvt_pk_bf16_f32 v71, v86, v87\n"
    "ds_write_b128 v245, v[68:71] offset:1024\n"
    "v_mfma_f32_16x16x32_bf16 a[64:67], v[24:27], v[96:99], a[64:67]\n"
    "v_accvgpr_read_b32 v48, a8\n"
    "v_accvgpr_read_b32 v49, a9\n"
    "v_accvgpr_read_b32 v50, a10\n"
    "v_accvgpr_read_b32 v51, a11\n"
    "v_mul_f32 v48, v48, v14\n"
    "v_mul_f32 v49, v49, v14\n"
    "v_mul_f32 v50, v50, v14\n"
    "v_mul_f32 v51, v51, v14\n"
    "v_mfma_f32_16x16x32_bf16 a[68:71], v[24:27], v[104:107], a[68:71]\n"
    "v_fma_f32 v72, v72, v10, v48\n"
    "v_fma_f32 v73, v73, v10, v49\n"
    "v_fma_f32 v74, v74, v10, v50\n"
    "v_fma_f32 v75, v75, v10, v51\n"
    "v_accvgpr_read_b32 v52, a24\n"
    "v_accvgpr_read_b32 v53, a25\n"
    "v_accvgpr_read_b32 v54, a26\n"
    "v_accvgpr_read_b32 v55, a27\n"
    "v_mfma_f32_16x16x32_bf16 a[72:75], v[24:27], v[112:115], a[72:75]\n"
    "v_mul_f32 v52, v52, v14\n"
    "v_mul_f32 v53, v53, v14\n"
    "v_mul_f32 v54, v54, v14\n"
    "v_mul_f32 v55, v55, v14\n"
    "s_waitcnt lgkmcnt(7)\n"
    "v_fma_f32 v88, v88, v10, v52\n"
    "v_fma_f32 v89, v89, v10, v53\n"
    "v_fma_f32 v90, v90, v10, v54\n"
    "v_fma_f32 v91, v91, v10, v55\n"
    "v_mfma_f32_16x16x32_bf16 a[76:79], v[24:27], v[120:123], a[76:79]\n"
    "ds_read_b64_tr_b16 v[24:25], v5 offset:192\n"
    "ds_read_b64_tr_b16 v[26:27], v5 offset:704\n"
    "v_cvt_pk_bf16_f32 v72, v72, v73\n"
    "v_cvt_pk_bf16_f32 v73, v74, v75\n"
    "v_cvt_pk_bf16_f32 v74, v88, v89\n"
    "v_cvt_pk_bf16_f32 v75, v90, v91\n"
    "ds_write_b128 v245, v[72:75] offset:2048\n"
    "v_mfma_f32_16x16x32_bf16 a[80:83], v[28:31], v[96:99], a[80:83]\n"
    "v_mov_b32 v254, s100\n"
    "v_accvgpr_read_b32 v48, a12\n"
    "v_accvgpr_read_b32 v49, a13\n"
    "v_accvgpr_read_b32 v50, a14\n"
    "v_accvgpr_read_b32 v51, a15\n"
    "v_mul_f32 v48, v48, v15\n"
    "v_mul_f32 v49, v49, v15\n"
    "v_mfma_f32_16x16x32_bf16 a[84:87], v[28:31], v[104:107], a[84:87]\n"
    "v_mov_b32 v255, s101\n"
    "v_mul_f32 v50, v50, v15\n"
    "v_mul_f32 v51, v51, v15\n"
    "v_fma_f32 v76, v76, v11, v48\n"
    "v_fma_f32 v77, v77, v11, v49\n"
    "v_fma_f32 v78, v78, v11, v50\n"
    "v_fma_f32 v79, v79, v11, v51\n"
    "v_mfma_f32_16x16x32_bf16 a[88:91], v[28:31], v[112:115], a[88:91]\n"
    "v_accvgpr_read_b32 v52, a28\n"
    "v_accvgpr_read_b32 v53, a29\n"
    "v_accvgpr_read_b32 v54, a30\n"
    "v_accvgpr_read_b32 v55, a31\n"
    "v_mul_f32 v52, v52, v15\n"
    "v_mul_f32 v53, v53, v15\n"
    "v_mul_f32 v54, v54, v15\n"
    "v_mfma_f32_16x16x32_bf16 a[92:95], v[28:31], v[120:123], a[92:95]\n"
    "s_waitcnt lgkmcnt(7)\n"
    "ds_read_b64_tr_b16 v[28:29], v5 offset:224\n"
    "ds_read_b64_tr_b16 v[30:31], v5 offset:736\n"
    "v_mul_f32 v55, v55, v15\n"
    "v_fma_f32 v92, v92, v11, v52\n"
    "v_fma_f32 v93, v93, v11, v53\n"
    "v_fma_f32 v94, v94, v11, v54\n"
    "v_fma_f32 v95, v95, v11, v55\n"
    "v_cvt_pk_bf16_f32 v76, v76, v77\n"
    "v_cvt_pk_bf16_f32 v77, v78, v79\n"
    "v_cvt_pk_bf16_f32 v78, v92, v93\n"
    "v_cvt_pk_bf16_f32 v79, v94, v95\n"
    "ds_write_b128 v245, v[76:79] offset:3072\n"
    "s_waitcnt lgkmcnt(0)\n"
    "s_barrier\n"
    "ds_read_b128 a[0:3], v246\n"
    "ds_read_b128 a[4:7], v246 offset:1024\n"
    "ds_read_b128 a[8:11], v246 offset:2048\n"
    "ds_read_b128 a[12:15], v246 offset:3072\n"
    "ds_read_b128 a[16:19], v246 offset:4096\n"
    "ds_read_b128 a[20:23], v246 offset:5120\n"
    "ds_read_b128 a[24:27], v246 offset:6144\n"
    "ds_read_b128 a[28:31], v246 offset:7168\n"
    "ds_read_b128 a[128:131], v246 offset:8192\n"
    "ds_read_b128 a[132:135], v246 offset:9216\n"
    "ds_read_b128 a[136:139], v246 offset:10240\n"
    "ds_read_b128 a[140:143], v246 offset:11264\n"
    "ds_read_b128 a[144:147], v246 offset:12288\n"
    "ds_read_b128 a[148:151], v246 offset:13312\n"
    "ds_read_b128 a[152:155], v246 offset:14336\n"
    "ds_read_b128 a[156:159], v246 offset:15360\n"
    "v_mfma_f32_16x16x32_bf16 a[224:227], v[16:19], v[96:99], a[224:227]\n"
    "s_add_u32 m0, s75, 0x13000\n"
    "ds_write_b128 v243, a[160:163]\n"
    "v_mfma_f32_16x16x32_bf16 a[228:231], v[16:19], v[104:107], a[228:231]\n"
    "global_load_lds_dwordx4 v0, s[70:71]\n"
    "v_mfma_f32_16x16x32_bf16 a[232:235], v[16:19], v[112:115], a[232:235]\n"
    "global_load_lds_dwordx4 v1, s[70:71] offset:1024\n"
    "ds_write_b128 v243, a[164:167] offset:1024\n"
    "v_mfma_f32_16x16x32_bf16 a[236:239], v[16:19], v[120:123], a[236:239]\n"
    "s_waitcnt vmcnt(9)\n"
    "ds_read_b64_tr_b16 v[16:17], v4\n"
    "ds_read_b64_tr_b16 v[18:19], v4 offset:3584\n"
    "global_load_lds_dwordx4 v2, s[70:71] offset:2048\n"
    "v_mfma_f32_16x16x32_bf16 a[240:243], v[20:23], v[96:99], a[240:243]\n"
    "global_load_lds_dwordx4 v3, s[70:71] offset:3072\n"
    "ds_write_b128 v243, a[168:171] offset:2048\n"
    "v_mfma_f32_16x16x32_bf16 a[244:247], v[20:23], v[104:107], a[244:247]\n"
    "s_add_u32 m0, s75, 0x14000\n"
    "v_mfma_f32_16x16x32_bf16 a[248:251], v[20:23], v[112:115], a[248:251]\n"
    "global_load_lds_dwordx4 v60, s[70:71]\n"
    "ds_write_b128 v243, a[172:175] offset:3072\n"
    "v_mfma_f32_16x16x32_bf16 a[252:255], v[20:23], v[120:123], a[252:255]\n"
    "ds_read_b64_tr_b16 v[20:21], v4 offset:32\n"
    "ds_read_b64_tr_b16 v[22:23], v4 offset:3616\n"
    "global_load_lds_dwordx4 v61, s[70:71] offset:1024\n"
    "v_mfma_f32_16x16x32_bf16 a[96:99], v[24:27], v[96:99], a[96:99]\n"
    "global_load_lds_dwordx4 v62, s[70:71] offset:2048\n"
    "ds_write_b128 v243, a[176:179] offset:4096\n"
    "v_mfma_f32_16x16x32_bf16 a[100:103], v[24:27], v[104:107], a[100:103]\n"
    "s_add_u32 s70, s70, 0x1c000\n"
    "s_addc_u32 s71, s71, 0\n"
    "v_mfma_f32_16x16x32_bf16 a[104:107], v[24:27], v[112:115], a[104:107]\n"
    "s_add_u32 m0, s75, 0xc000\n"
    "ds_write_b128 v243, a[180:183] offset:5120\n"
    "v_mfma_f32_16x16x32_bf16 a[108:111], v[24:27], v[120:123], a[108:111]\n"
    "ds_read_b64_tr_b16 v[24:25], v4 offset:64\n"
    "ds_read_b64_tr_b16 v[26:27], v4 offset:3648\n"
    "global_load_lds_dwordx4 v0, s[70:71]\n"
    "v_mfma_f32_16x16x32_bf16 a[112:115], v[28:31], v[96:99], a[112:115]\n"
    "global_load_lds_dwordx4 v1, s[70:71] offset:1024\n"
    "ds_write_b128 v243, a[184:187] offset:6144\n"
    "v_mfma_f32_16x16x32_bf16 a[116:119], v[28:31], v[104:107], a[116:119]\n"
    "global_load_lds_dwordx4 v2, s[70:71] offset:2048\n"
    "v_mfma_f32_16x16x32_bf16 a[120:123], v[28:31], v[112:115], a[120:123]\n"
    "global_load_lds_dwordx4 v3, s[70:71] offset:3072\n"
    "ds_write_b128 v243, a[188:191] offset:7168\n"
    "v_mfma_f32_16x16x32_bf16 a[124:127], v[28:31], v[120:123], a[124:127]\n"
    "s_waitcnt lgkmcnt(10)\n"
    "s_add_u32 m0, s75, 0xd000\n"
    "ds_read_b64_tr_b16 v[28:29], v4 offset:96\n"
    "ds_read_b64_tr_b16 v[30:31], v4 offset:3680\n"
    "v_mfma_f32_16x16x32_bf16 v[128:131], v[16:19], a[0:3], 0\n"
    "global_load_lds_dwordx4 v60, s[70:71]\n"
    "v_mfma_f32_16x16x32_bf16 v[132:135], v[16:19], a[4:7], 0\n"
    "global_load_lds_dwordx4 v61, s[70:71] offset:1024\n"
    "v_mfma_f32_16x16x32_bf16 v[136:139], v[16:19], a[8:11], 0\n"
    "global_load_lds_dwordx4 v62, s[70:71] offset:2048\n"
    "v_mfma_f32_16x16x32_bf16 v[140:143], v[16:19], a[12:15], 0\n"
    "s_waitcnt lgkmcnt(8)\n"
    "s_sub_u32 s70, s70, 0x4d000\n"
    "s_subb_u32 s71, s71, 0\n"
    "ds_read_b64_tr_b16 v[16:17], v4 offset:128\n"
    "ds_read_b64_tr_b16 v[18:19], v4 offset:3712\n"
    "v_mfma_f32_16x16x32_bf16 v[144:147], v[20:23], a[0:3], 0\n"
    "v_mfma_f32_16x16x32_bf16 v[148:151], v[20:23], a[4:7], 0\n"
    "v_mfma_f32_16x16x32_bf16 v[152:155], v[20:23], a[8:11], 0\n"
    "v_mfma_f32_16x16x32_bf16 v[156:159], v[20:23], a[12:15], 0\n"
    "s_waitcnt lgkmcnt(6)\n"
    "ds_read_b64_tr_b16 v[20:21], v4 offset:160\n"
    "ds_read_b64_tr_b16 v[22:23], v4 offset:3744\n"
    "v_mfma_f32_16x16x32_bf16 v[160:163], v[24:27], a[0:3], 0\n"
    "v_mfma_f32_16x16x32_bf16 v[164:167], v[24:27], a[4:7], 0\n"
    "v_mfma_f32_16x16x32_bf16 v[168:171], v[24:27], a[8:11], 0\n"
    "v_mfma_f32_16x16x32_bf16 v[172:175], v[24:27], a[12:15], 0\n"
    "s_waitcnt lgkmcnt(4)\n"
    "ds_read_b64_tr_b16 v[24:25], v4 offset:192\n"
    "ds_read_b64_tr_b16 v[26:27], v4 offset:3776\n"
    "s_waitcnt lgkmcnt(0)\n"
    "s_barrier\n"
    "ds_read_b128 v[64:67], v244\n"
    "ds_read_b128 v[68:71], v244 offset:1024\n"
    "ds_read_b128 v[72:75], v244 offset:2048\n"
    "ds_read_b128 v[76:79], v244 offset:3072\n"
    "ds_read_b128 v[80:83], v244 offset:4096\n"
    "ds_read_b128 v[84:87], v244 offset:5120\n"
    "ds_read_b128 v[88:91], v244 offset:6144\n"
    "ds_read_b128 v[92:95], v244 offset:7168\n"
    "v_mfma_f32_16x16x32_bf16 v[176:179], v[28:31], a[0:3], 0\n"
    "v_accvgpr_read_b32 v48, a32\n"
    "v_accvgpr_read_b32 v49, a33\n"
    "v_accvgpr_read_b32 v50, a34\n"
    "v_mfma_f32_16x16x32_bf16 v[180:183], v[28:31], a[4:7], 0\n"
    "v_accvgpr_read_b32 v51, a35\n"
    "v_mul_f32 v48, v48, v12\n"
    "v_mfma_f32_16x16x32_bf16 v[184:187], v[28:31], a[8:11], 0\n"
    "v_mul_f32 v49, v49, v12\n"
    "v_mul_f32 v50, v50, v12\n"
    "v_mfma_f32_16x16x32_bf16 v[188:191], v[28:31], a[12:15], 0\n"
    "s_waitcnt vmcnt(14)\n"
    "ds_read_b64_tr_b16 v[28:29], v56\n"
    "ds_read_b64_tr_b16 v[30:31], v56 offset:3584\n"
    "v_mul_f32 v51, v51, v12\n"
    "v_mfma_f32_16x16x32_bf16 v[192:195], v[16:19], a[0:3], 0\n"
    "s_waitcnt lgkmcnt(9)\n"
    "v_fma_f32 v64, v64, v8, v48\n"
    "v_fma_f32 v65, v65, v8, v49\n"
    "v_mfma_f32_16x16x32_bf16 v[196:199], v[16:19], a[4:7], 0\n"
    "v_fma_f32 v66, v66, v8, v50\n"
    "v_fma_f32 v67, v67, v8, v51\n"
    "v_mfma_f32_16x16x32_bf16 v[200:203], v[16:19], a[8:11], 0\n"
    "global_load_dwordx2 v[252:253], v7, s[28:29] sc1\n"
    "v_accvgpr_read_b32 v52, a48\n"
    "v_accvgpr_read_b32 v53, a49\n"
    "v_mfma_f32_16x16x32_bf16 v[204:207], v[16:19], a[12:15], 0\n"
    "ds_read_b64_tr_b16 v[16:17], v56 offset:32\n"
    "ds_read_b64_tr_b16 v[18:19], v56 offset:3616\n"
    "v_mfma_f32_16x16x32_bf16 v[208:211], v[20:23], a[0:3], 0\n"
    "v_accvgpr_read_b32 v54, a50\n"
    "v_accvgpr_read_b32 v55, a51\n"
    "v_mfma_f32_16x16x32_bf16 v[212:215], v[20:23], a[4:7], 0\n"
    "v_mul_f32 v52, v52, v12\n"
    "v_mul_f32 v53, v53, v12\n"
    "v_mul_f32 v54, v54, v12\n"
    "v_mfma_f32_16x16x32_bf16 v[216:219], v[20:23], a[8:11], 0\n"
    "v_mul_f32 v55, v55, v12\n"
    "s_waitcnt lgkmcnt(7)\n"
    "v_fma_f32 v80, v80, v8, v52\n"
    "v_mfma_f32_16x16x32_bf16 v[220:223], v[20:23], a[12:15], 0\n"
    "ds_read_b64_tr_b16 v[20:21], v56 offset:64\n"
    "ds_read_b64_tr_b16 v[22:23], v56 offset:3648\n"
    "v_mfma_f32_16x16x32_bf16 v[224:227], v[24:27], a[0:3], 0\n"
    "s_add_u32 m0, s75, 0x21000\n"
    "v_fma_f32 v81, v81, v8, v53\n"
    "v_fma_f32 v82, v82, v8, v54\n"
    "v_fma_f32 v83, v83, v8, v55\n"
    "v_mfma_f32_16x16x32_bf16 v[228:231], v[24:27], a[4:7], 0\n"
    "global_load_lds_dwordx4 v0, s[70:71]\n"
    "v_cvt_pk_bf16_f32 v64, v64, v65\n"
    "v_mfma_f32_16x16x32_bf16 v[232:235], v[24:27], a[8:11], 0\n"
    "v_cvt_pk_bf16_f32 v65, v66, v67\n"
    "v_cvt_pk_bf16_f32 v66, v80, v81\n"
    "v_mfma_f32_16x16x32_bf16 v[236:239], v[24:27], a[12:15], 0\n"
    "s_waitcnt lgkmcnt(4)\n"
    "ds_read_b64_tr_b16 v[24:25], v56 offset:96\n"
    "ds_read_b64_tr_b16 v[26:27], v56 offset:3680\n"
    "v_cvt_pk_bf16_f32 v67, v82, v83\n"
    "v_mfma_f32_16x16x32_bf16 v[128:131], v[28:31], a[16:19], v[128:131]\n"
    "ds_write_b128 v245, v[64:67]\n"
    "v_mfma_f32_16x16x32_bf16 v[132:135], v[28:31], a[20:23], v[132:135]\n"
    "global_load_lds_dwordx4 v1, s[70:71] offset:1024\n"
    "v_accvgpr_read_b32 v48, a36\n"
    "v_mfma_f32_16x16x32_bf16 v[136:139], v[28:31], a[24:27], v[136:139]\n"
    "v_accvgpr_read_b32 v49, a37\n"
    "v_accvgpr_read_b32 v50, a38\n"
    "v_accvgpr_read_b32 v51, a39\n"
    "v_mfma_f32_16x16x32_bf16 v[140:143], v[28:31], a[28:31], v[140:143]\n"
    "s_waitcnt lgkmcnt(5)\n"
    "ds_read_b64_tr_b16 v[28:29], v56 offset:128\n"
    "ds_read_b64_tr_b16 v[30:31], v56 offset:3712\n"
    "v_mfma_f32_16x16x32_bf16 v[144:147], v[16:19], a[16:19], v[144:147]\n"
    "v_mul_f32 v48, v48, v13\n"
    "v_mul_f32 v49, v49, v13\n"
    "v_mfma_f32_16x16x32_bf16 v[148:151], v[16:19], a[20:23], v[148:151]\n"
    "global_load_lds_dwordx4 v2, s[70:71] offset:2048\n"
    "v_mul_f32 v50, v50, v13\n"
    "v_mul_f32 v51, v51, v13\n"
    "v_mfma_f32_16x16x32_bf16 v[152:155], v[16:19], a[24:27], v[152:155]\n"
    "v_fma_f32 v68, v68, v9, v48\n"
    "v_fma_f32 v69, v69, v9, v49\n"
    "v_mfma_f32_16x16x32_bf16 v[156:159], v[16:19], a[28:31], v[156:159]\n"
    "s_waitcnt lgkmcnt(5)\n"
    "ds_read_b64_tr_b16 v[16:17], v56 offset:160\n"
    "ds_read_b64_tr_b16 v[18:19], v56 offset:3744\n"
    "v_mfma_f32_16x16x32_bf16 v[160:163], v[20:23], a[16:19], v[160:163]\n"
    "v_fma_f32 v70, v70, v9, v50\n"
    "v_fma_f32 v71, v71, v9, v51\n"
    "v_accvgpr_read_b32 v52, a52\n"
    "v_mfma_f32_16x16x32_bf16 v[164:167], v[20:23], a[20:23], v[164:167]\n"
    "global_load_lds_dwordx4 v3, s[70:71] offset:3072\n"
    "v_accvgpr_read_b32 v53, a53\n"
    "v_mfma_f32_16x16x32_bf16 v[168:171], v[20:23], a[24:27], v[168:171]\n"
    "v_accvgpr_read_b32 v54, a54\n"
    "v_accvgpr_read_b32 v55, a55\n"
    "v_mfma_f32_16x16x32_bf16 v[172:175], v[20:23], a[28:31], v[172:175]\n"
    "s_waitcnt lgkmcnt(5)\n"
    "ds_read_b64_tr_b16 v[20:21], v56 offset:192\n"
    "ds_read_b64_tr_b16 v[22:23], v56 offset:3776\n"
    "v_mul_f32 v52, v52, v13\n"
    "v_mfma_f32_16x16x32_bf16 v[176:179], v[24:27], a[16:19], v[176:179]\n"
    "s_add_u32 m0, s75, 0x22000\n"
    "v_mul_f32 v53, v53, v13\n"
    "v_mul_f32 v54, v54, v13\n"
    "v_mfma_f32_16x16x32_bf16 v[180:183], v[24:27], a[20:23], v[180:183]\n"
    "global_load_lds_dwordx4 v60, s[70:71]\n"
    "v_mul_f32 v55, v55, v13\n"
    "v_mfma_f32_16x16x32_bf16 v[184:187], v[24:27], a[24:27], v[184:187]\n"
    "v_fma_f32 v84, v84, v9, v52\n"
    "v_fma_f32 v85, v85, v9, v53\n"
    "v_fma_f32 v86, v86, v9, v54\n"
    "v_mfma_f32_16x16x32_bf16 v[188:191], v[24:27], a[28:31], v[188:191]\n"
    "s_waitcnt lgkmcnt(4)\n"
    "s_waitcnt vmcnt(13)\n"
    "ds_read_b64_tr_b16 v[24:25], v57\n"
    "ds_read_b64_tr_b16 v[26:27], v57 offset:3584\n"
    "v_mfma_f32_16x16x32_bf16 v[192:195], v[28:31], a[16:19], v[192:195]\n"
    "v_fma_f32 v87, v87, v9, v55\n"
    "v_cvt_pk_bf16_f32 v68, v68, v69\n"
    "v_mfma_f32_16x16x32_bf16 v[196:199], v[28:31], a[20:23], v[196:199]\n"
    "global_load_lds_dwordx4 v61, s[70:71] offset:1024\n"
    "v_cvt_pk_bf16_f32 v69, v70, v71\n"
    "v_cvt_pk_bf16_f32 v70, v84, v85\n"
    "v_mfma_f32_16x16x32_bf16 v[200:203], v[28:31], a[24:27], v[200:203]\n"
    "v_cvt_pk_bf16_f32 v71, v86, v87\n"
    "v_mfma_f32_16x16x32_bf16 v[204:207], v[28:31], a[28:31], v[204:207]\n"
    "s_waitcnt lgkmcnt(4)\n"
    "ds_read_b64_tr_b16 v[28:29], v57 offset:32\n"
    "ds_read_b64_tr_b16 v[30:31], v57 offset:3616\n"
    "v_mfma_f32_16x16x32_bf16 v[208:211], v[16:19], a[16:19], v[208:211]\n"
    "ds_write_b128 v245, v[68:71] offset:1024\n"
    "v_mfma_f32_16x16x32_bf16 v[212:215], v[16:19], a[20:23], v[212:215]\n"
    "global_load_lds_dwordx4 v62, s[70:71] offset:2048\n"
    "v_accvgpr_read_b32 v48, a40\n"
    "v_mfma_f32_16x16x32_bf16 v[216:219], v[16:19], a[24:27], v[216:219]\n"
    "s_add_u32 s70, s70, 0x1c000\n"
    "s_addc_u32 s71, s71, 0\n"
    "v_accvgpr_read_b32 v49, a41\n"
    "v_mfma_f32_16x16x32_bf16 v[220:223], v[16:19], a[28:31], v[220:223]\n"
    "s_waitcnt lgkmcnt(5)\n"
    "ds_read_b64_tr_b16 v[16:17], v57 offset:64\n"
    "ds_read_b64_tr_b16 v[18:19], v57 offset:3648\n"
    "v_accvgpr_read_b32 v50, a42\n"
    "v_mfma_f32_16x16x32_bf16 v[224:227], v[20:23], a[16:19], v[224:227]\n"
    "s_add_u32 m0, s75, 0x1a000\n"
    "v_accvgpr_read_b32 v51, a43\n"
    "v_mul_f32 v48, v48, v14\n"
    "v_mfma_f32_16x16x32_bf16 v[228:231], v[20:23], a[20:23], v[228:231]\n"
    "global_load_lds_dwordx4 v0, s[70:71]\n"
    "v_mul_f32 v49, v49, v14\n"
    "v_mfma_f32_16x16x32_bf16 v[232:235], v[20:23], a[24:27], v[232:235]\n"
    "v_mul_f32 v50, v50, v14\n"
    "v_mul_f32 v51, v51, v14\n"
    "v_fma_f32 v72, v72, v10, v48\n"
    "v_mfma_f32_16x16x32_bf16 v[236:239], v[20:23], a[28:31], v[236:239]\n"
    "s_waitcnt lgkmcnt(5)\n"
    "ds_read_b64_tr_b16 v[20:21], v57 offset:96\n"
    "ds_read_b64_tr_b16 v[22:23], v57 offset:3680\n"
    "v_mfma_f32_16x16x32_bf16 v[128:131], v[24:27], a[128:131], v[128:131]\n"
    "v_fma_f32 v73, v73, v10, v49\n"
    "v_fma_f32 v74, v74, v10, v50\n"
    "v_mfma_f32_16x16x32_bf16 v[132:135], v[24:27], a[132:135], v[132:135]\n"
    "global_load_lds_dwordx4 v1, s[70:71] offset:1024\n"
    "v_fma_f32 v75, v75, v10, v51\n"
    "v_accvgpr_read_b32 v52, a56\n"
    "v_mfma_f32_16x16x32_bf16 v[136:139], v[24:27], a[136:139], v[136:139]\n"
    "v_accvgpr_read_b32 v53, a57\n"
    "v_accvgpr_read_b32 v54, a58\n"
    "v_mfma_f32_16x16x32_bf16 v[140:143], v[24:27], a[140:143], v[140:143]\n"
    "s_waitcnt lgkmcnt(5)\n"
    "ds_read_b64_tr_b16 v[24:25], v57 offset:128\n"
    "ds_read_b64_tr_b16 v[26:27], v57 offset:3712\n"
    "v_mfma_f32_16x16x32_bf16 v[144:147], v[28:31], a[128:131], v[144:147]\n"
    "v_accvgpr_read_b32 v55, a59\n"
    "v_mul_f32 v52, v52, v14\n"
    "v_mul_f32 v53, v53, v14\n"
    "v_mfma_f32_16x16x32_bf16 v[148:151], v[28:31], a[132:135], v[148:151]\n"
    "global_load_lds_dwordx4 v2, s[70:71] offset:2048\n"
    "v_mul_f32 v54, v54, v14\n"
    "v_mfma_f32_16x16x32_bf16 v[152:155], v[28:31], a[136:139], v[152:155]\n"
    "v_mul_f32 v55, v55, v14\n"
    "v_fma_f32 v88, v88, v10, v52\n"
    "v_mfma_f32_16x16x32_bf16 v[156:159], v[28:31], a[140:143], v[156:159]\n"
    "s_waitcnt lgkmcnt(4)\n"
    "ds_read_b64_tr_b16 v[28:29], v57 offset:160\n"
    "ds_read_b64_tr_b16 v[30:31], v57 offset:3744\n"
    "v_fma_f32 v89, v89, v10, v53\n"
    "v_mfma_f32_16x16x32_bf16 v[160:163], v[16:19], a[128:131], v[160:163]\n"
    "v_fma_f32 v90, v90, v10, v54\n"
    "v_fma_f32 v91, v91, v10, v55\n"
    "v_mfma_f32_16x16x32_bf16 v[164:167], v[16:19], a[132:135], v[164:167]\n"
    "global_load_lds_dwordx4 v3, s[70:71] offset:3072\n"
    "v_cvt_pk_bf16_f32 v72, v72, v73\n"
    "v_mfma_f32_16x16x32_bf16 v[168:171], v[16:19], a[136:139], v[168:171]\n"
    "v_cvt_pk_bf16_f32 v73, v74, v75\n"
    "v_cvt_pk_bf16_f32 v74, v88, v89\n"
    "v_cvt_pk_bf16_f32 v75, v90, v91\n"
    "v_mfma_f32_16x16x32_bf16 v[172:175], v[16:19], a[140:143], v[172:175]\n"
    "s_waitcnt lgkmcnt(4)\n"
    "ds_read_b64_tr_b16 v[16:17], v57 offset:192\n"
    "ds_read_b64_tr_b16 v[18:19], v57 offset:3776\n"
    "v_mfma_f32_16x16x32_bf16 v[176:179], v[20:23], a[128:131], v[176:179]\n"
    "s_add_u32 m0, s75, 0x1b000\n"
    "ds_write_b128 v245, v[72:75] offset:2048\n"
    "v_mfma_f32_16x16x32_bf16 v[180:183], v[20:23], a[132:135], v[180:183]\n"
    "global_load_lds_dwordx4 v60, s[70:71]\n"
    "v_accvgpr_read_b32 v48, a44\n"
    "v_accvgpr_read_b32 v49, a45\n"
    "v_mfma_f32_16x16x32_bf16 v[184:187], v[20:23], a[136:139], v[184:187]\n"
    "s_waitcnt vmcnt(12)\n"
    "v_cmp_eq_u32 vcc, s100, v252\n"
    "v_cmp_eq_u32_e64 s[44:45], s101, v253\n"
    "v_mfma_f32_16x16x32_bf16 v[188:191], v[20:23], a[140:143], v[188:191]\n"
    "s_waitcnt lgkmcnt(5)\n"
    "ds_read_b64_tr_b16 v[20:21], v58\n"
    "ds_read_b64_tr_b16 v[22:23], v58 offset:3584\n"
    "v_mfma_f32_16x16x32_bf16 v[192:195], v[24:27], a[128:131], v[192:195]\n"
    "s_and_b64 vcc, vcc, s[44:45]\n"
    "s_cmp_eq_u64 vcc, -1\n"
    "s_cbranch_scc0 L_mp_slow\n"
    "L_mp_ok:\n"
    "v_accvgpr_read_b32 v50, a46\n"
    "v_mfma_f32_16x16x32_bf16 v[196:199], v[24:27], a[132:135], v[196:199]\n"
    "global_load_lds_dwordx4 v61, s[70:71] offset:1024\n"
    "v_accvgpr_read_b32 v51, a47\n"
    "v_mfma_f32_16x16x32_bf16 v[200:203], v[24:27], a[136:139], v[200:203]\n"
    "v_mul_f32 v48, v48, v15\n"
    "v_mul_f32 v49, v49, v15\n"
    "v_mfma_f32_16x16x32_bf16 v[204:207], v[24:27], a[140:143], v[204:207]\n"
    "s_waitcnt lgkmcnt(5)\n"
    "ds_read_b64_tr_b16 v[24:25], v58 offset:32\n"
    "ds_read_b64_tr_b16 v[26:27], v58 offset:3616\n"
    "v_mul_f32 v50, v50, v15\n"
    "v_mfma_f32_16x16x32_bf16 v[208:211], v[28:31], a[128:131], v[208:211]\n"
    "v_mul_f32 v51, v51, v15\n"
    "v_fma_f32 v76, v76, v11, v48\n"
    "v_mfma_f32_16x16x32_bf16 v[212:215], v[28:31], a[132:135], v[212:215]\n"
    "global_load_lds_dwordx4 v62, s[70:71] offset:2048\n"
    "v_fma_f32 v77, v77, v11, v49\n"
    "v_mfma_f32_16x16x32_bf16 v[216:219], v[28:31], a[136:139], v[216:219]\n"
    "s_add_u32 s70, s70, 0x1c000\n"
    "s_addc_u32 s71, s71, 0\n"
    "global_load_dwordx4 a[0:3], v45, s[32:33] sc1\n"
    "v_fma_f32 v78, v78, v11, v50\n"
    "v_mfma_f32_16x16x32_bf16 v[220:223], v[28:31], a[140:143], v[220:223]\n"
    "s_waitcnt lgkmcnt(5)\n"
    "ds_read_b64_tr_b16 v[28:29], v58 offset:64\n"
    "ds_read_b64_tr_b16 v[30:31], v58 offset:3648\n"
    "v_mfma_f32_16x16x32_bf16 v[224:227], v[16:19], a[128:131], v[224:227]\n"
    "s_add_u32 m0, s75, 0x13000\n"
    "v_fma_f32 v79, v79, v11, v51\n"
    "v_accvgpr_read_b32 v52, a60\n"
    "v_mfma_f32_16x16x32_bf16 v[228:231], v[16:19], a[132:135], v[228:231]\n"
    "global_load_lds_dwordx4 v0, s[70:71]\n"
    "v_accvgpr_read_b32 v53, a61\n"
    "v_accvgpr_read_b32 v54, a62\n"
    "v_mfma_f32_16x16x32_bf16 v[232:235], v[16:19], a[136:139], v[232:235]\n"
    "v_accvgpr_read_b32 v55, a63\n"
    "v_mul_f32 v52, v52, v15\n"
    "v_mfma_f32_16x16x32_bf16 v[236:239], v[16:19], a[140:143], v[236:239]\n"
    "s_waitcnt lgkmcnt(4)\n"
    "ds_read_b64_tr_b16 v[16:17], v58 offset:96\n"
    "ds_read_b64_tr_b16 v[18:19], v58 offset:3680\n"
    "v_mfma_f32_16x16x32_bf16 v[128:131], v[20:23], a[144:147], v[128:131]\n"
    "v_mul_f32 v53, v53, v15\n"
    "v_mul_f32 v54, v54, v15\n"
    "v_mul_f32 v55, v55, v15\n"
    "v_mfma_f32_16x16x32_bf16 v[132:135], v[20:23], a[148:151], v[132:135]\n"
    "global_load_lds_dwordx4 v1, s[70:71] offset:1024\n"
    "v_fma_f32 v92, v92, v11, v52\n"
    "v_mfma_f32_16x16x32_bf16 v[136:139], v[20:23], a[152:155], v[136:139]\n"
    "global_load_dwordx4 a[4:7], v46, s[32:33] sc1\n"
    "v_fma_f32 v93, v93, v11, v53\n"
    "v_mfma_f32_16x16x32_bf16 v[140:143], v[20:23], a[156:159], v[140:143]\n"
    "s_waitcnt lgkmcnt(4)\n"
    "s_add_u32 s32, s32, 0x1c0000\n"
    "s_addc_u32 s33, s33, 0\n"
    "ds_read_b64_tr_b16 v[20:21], v58 offset:128\n"
    "ds_read_b64_tr_b16 v[22:23], v58 offset:3712\n"
    "v_mfma_f32_16x16x32_bf16 v[144:147], v[24:27], a[144:147], v[144:147]\n"
    "v_fma_f32 v94, v94, v11, v54\n"
    "v_fma_f32 v95, v95, v11, v55\n"
    "v_mfma_f32_16x16x32_bf16 v[148:151], v[24:27], a[148:151], v[148:151]\n"
    "global_load_lds_dwordx4 v2, s[70:71] offset:2048\n"
    "v_cvt_pk_bf16_f32 v76, v76, v77\n"
    "v_mfma_f32_16x16x32_bf16 v[152:155], v[24:27], a[152:155], v[152:155]\n"
    "v_cvt_pk_bf16_f32 v77, v78, v79\n"
    "v_cvt_pk_bf16_f32 v78, v92, v93\n"
    "v_cvt_pk_bf16_f32 v79, v94, v95\n"
    "v_mfma_f32_16x16x32_bf16 v[156:159], v[24:27], a[156:159], v[156:159]\n"
    "s_waitcnt lgkmcnt(4)\n"
    "ds_read_b64_tr_b16 v[24:25], v58 offset:160\n"
    "ds_read_b64_tr_b16 v[26:27], v58 offset:3744\n"
    "v_mfma_f32_16x16x32_bf16 v[160:163], v[28:31], a[144:147], v[160:163]\n"
    "ds_write_b128 v245, v[76:79] offset:3072\n"
    "v_mfma_f32_16x16x32_bf16 v[164:167], v[28:31], a[148:151], v[164:167]\n"
    "global_load_lds_dwordx4 v3, s[70:71] offset:3072\n"
    "v_mfma_f32_16x16x32_bf16 v[168:171], v[28:31], a[152:155], v[168:171]\n"
    "global_load_dwordx4 a[8:11], v45, s[32:33] sc1\n"
    "v_mfma_f32_16x16x32_bf16 v[172:175], v[28:31], a[156:159], v[172:175]\n"
    "s_waitcnt lgkmcnt(5)\n"
    "ds_read_b64_tr_b16 v[28:29], v58 offset:192\n"
    "ds_read_b64_tr_b16 v[30:31], v58 offset:3776\n"
    "s_waitcnt lgkmcnt(0)\n"
    "s_barrier\n"
    "ds_read_b128 a[32:35], v246\n"
    "ds_read_b128 a[36:39], v246 offset:1024\n"
    "ds_read_b128 a[40:43], v246 offset:2048\n"
    "ds_read_b128 a[44:47], v246 offset:3072\n"
    "ds_read_b128 a[48:51], v246 offset:4096\n"
    "ds_read_b128 a[52:55], v246 offset:5120\n"
    "ds_read_b128 a[56:59], v246 offset:6144\n"
    "ds_read_b128 a[60:63], v246 offset:7168\n"
    "ds_read_b128 a[160:163], v246 offset:8192\n"
    "ds_read_b128 a[164:167], v246 offset:9216\n"
    "ds_read_b128 a[168:171], v246 offset:10240\n"
    "ds_read_b128 a[172:175], v246 offset:11264\n"
    "ds_read_b128 a[176:179], v246 offset:12288\n"
    "ds_read_b128 a[180:183], v246 offset:13312\n"
    "ds_read_b128 a[184:187], v246 offset:14336\n"
    "ds_read_b128 a[188:191], v246 offset:15360\n"
    "v_mfma_f32_16x16x32_bf16 v[176:179], v[16:19], a[144:147], v[176:179]\n"
    "s_add_u32 m0, s75, 0x14000\n"
    "ds_write_b128 v243, a[192:195]\n"
    "v_mfma_f32_16x16x32_bf16 v[180:183], v[16:19], a[148:151], v[180:183]\n"
    "global_load_lds_dwordx4 v60, s[70:71]\n"
    "v_lshrrev_b32 v32, 10, v45\n"
    "v_add_u32 v32, s46, v32\n"
    "v_mfma_f32_16x16x32_bf16 v[184:187], v[16:19], a[152:155], v[184:187]\n"
    "ds_write_b128 v243, a[196:199] offset:1024\n"
    "v_mfma_f32_16x16x32_bf16 v[188:191], v[16:19], a[156:159], v[188:191]\n"
    "s_waitcnt vmcnt(15)\n"
    "ds_read_b64_tr_b16 v[16:17], v4\n"
    "ds_read_b64_tr_b16 v[18:19], v4 offset:3584\n"
    "v_mfma_f32_16x16x32_bf16 v[192:195], v[20:23], a[144:147], v[192:195]\n"
    "ds_write_b128 v243, a[200:203] offset:2048\n"
    "v_mfma_f32_16x16x32_bf16 v[196:199], v[20:23], a[148:151], v[196:199]\n"
    "global_load_lds_dwordx4 v61, s[70:71] offset:1024\n"
    "v_and_b32 v33, 1, v32\n"
    "v_lshlrev_b32 v33, 5, v33\n"
    "v_mfma_f32_16x16x32_bf16 v[200:203], v[20:23], a[152:155], v[200:203]\n"
    "global_load_dwordx4 a[12:15], v46, s[32:33] sc1\n"
    "ds_write_b128 v243, a[204:207] offset:3072\n"
    "v_mfma_f32_16x16x32_bf16 v[204:207], v[20:23], a[156:159], v[204:207]\n"
    "s_add_u32 s32, s32, 0x1c0000\n"
    "s_addc_u32 s33, s33, 0\n"
    "ds_read_b64_tr_b16 v[20:21], v4 offset:32\n"
    "ds_read_b64_tr_b16 v[22:23], v4 offset:3616\n"
    "v_mfma_f32_16x16x32_bf16 v[208:211], v[24:27], a[144:147], v[208:211]\n"
    "ds_write_b128 v243, a[208:211] offset:4096\n"
    "v_mfma_f32_16x16x32_bf16 v[212:215], v[24:27], a[148:151], v[212:215]\n"
    "global_load_lds_dwordx4 v62, s[70:71] offset:2048\n"
    "v_bfe_u32 v34, v45, 4, 4\n"
    "v_add_u32 v33, v33, v34\n"
    "v_mfma_f32_16x16x32_bf16 v[216:219], v[24:27], a[152:155], v[216:219]\n"
    "s_add_u32 s70, s70, 0x1c000\n"
    "s_addc_u32 s71, s71, 0\n"
    "global_load_dwordx4 a[16:19], v45, s[32:33] sc1\n"
    "ds_write_b128 v243, a[212:215] offset:5120\n"
    "v_mfma_f32_16x16x32_bf16 v[220:223], v[24:27], a[156:159], v[220:223]\n"
    "ds_read_b64_tr_b16 v[24:25], v4 offset:64\n"
    "ds_read_b64_tr_b16 v[26:27], v4 offset:3648\n"
    "v_mul_u32_u24 v33, 0x380, v33\n"
    "v_mfma_f32_16x16x32_bf16 v[224:227], v[28:31], a[144:147], v[224:227]\n"
    "s_add_u32 m0, s75, 0xc000\n"
    "ds_write_b128 v243, a[216:219] offset:6144\n"
    "v_mfma_f32_16x16x32_bf16 v[228:231], v[28:31], a[148:151], v[228:231]\n"
    "global_load_lds_dwordx4 v0, s[70:71]\n"
    "v_lshrrev_b32 v32, 1, v32\n"
    "v_mfma_f32_16x16x32_bf16 v[232:235], v[28:31], a[152:155], v[232:235]\n"
    "ds_write_b128 v243, a[220:223] offset:7168\n"
    "v_mfma_f32_16x16x32_bf16 v[236:239], v[28:31], a[156:159], v[236:239]\n"
    "s_waitcnt lgkmcnt(10)\n"
    "ds_read_b64_tr_b16 v[28:29], v4 offset:96\n"
    "ds_read_b64_tr_b16 v[30:31], v4 offset:3680\n"
    "v_lshlrev_b32 v32, 5, v32\n"
    "v_mfma_f32_16x16x32_bf16 v[128:131], v[16:19], a[32:35], v[128:131]\n"
    "v_bfe_u32 v34, v45, 8, 2\n"
    "v_lshl_add_u32 v32, v34, 3, v32\n"
    "v_add_u32 v5, s47, v32\n"
    "v_mfma_f32_16x16x32_bf16 v[132:135], v[16:19], a[36:39], v[132:135]\n"
    "global_load_lds_dwordx4 v1, s[70:71] offset:1024\n"
    "v_add_u32 v33, v33, v5\n"
    "v_mfma_f32_16x16x32_bf16 v[136:139], v[16:19], a[40:43], v[136:139]\n"
    "global_load_dwordx4 a[20:23], v46, s[32:33] sc1\n"
    "v_add_u32 v47, s36, v33\n"
    "v_lshrrev_b32 v32, 10, v46\n"
    "v_mfma_f32_16x16x32_bf16 v[140:143], v[16:19], a[44:47], v[140:143]\n"
    "s_waitcnt lgkmcnt(8)\n"
    "s_add_u32 s32, s32, 0x1c0000\n"
    "s_addc_u32 s33, s33, 0\n"
    "ds_read_b64_tr_b16 v[16:17], v4 offset:128\n"
    "ds_read_b64_tr_b16 v[18:19], v4 offset:3712\n"
    "v_mfma_f32_16x16x32_bf16 v[144:147], v[20:23], a[32:35], v[144:147]\n"
    "v_add_u32 v32, s46, v32\n"
    "v_and_b32 v33, 1, v32\n"
    "v_mfma_f32_16x16x32_bf16 v[148:151], v[20:23], a[36:39], v[148:151]\n"
    "global_load_lds_dwordx4 v2, s[70:71] offset:2048\n"
    "v_lshlrev_b32 v33, 5, v33\n"
    "v_bfe_u32 v34, v46, 4, 4\n"
    "v_mfma_f32_16x16x32_bf16 v[152:155], v[20:23], a[40:43], v[152:155]\n"
    "v_add_u32 v33, v33, v34\n"
    "v_mul_u32_u24 v33, 0x380, v33\n"
    "v_lshrrev_b32 v32, 1, v32\n"
    "v_mfma_f32_16x16x32_bf16 v[156:159], v[20:23], a[44:47], v[156:159]\n"
    "s_waitcnt lgkmcnt(6)\n"
    "ds_read_b64_tr_b16 v[20:21], v4 offset:160\n"
    "ds_read_b64_tr_b16 v[22:23], v4 offset:3744\n"
    "v_lshlrev_b32 v32, 5, v32\n"
    "v_mfma_f32_16x16x32_bf16 v[160:163], v[24:27], a[32:35], v[160:163]\n"
    "v_bfe_u32 v34, v46, 8, 2\n"
    "v_lshl_add_u32 v32, v34, 3, v32\n"
    "v_mfma_f32_16x16x32_bf16 v[164:167], v[24:27], a[36:39], v[164:167]\n"
    "global_load_lds_dwordx4 v3, s[70:71] offset:3072\n"
    "v_add_u32 v6, s47, v32\n"
    "v_add_u32 v33, v33, v6\n"
    "v_mfma_f32_16x16x32_bf16 v[168:171], v[24:27], a[40:43], v[168:171]\n"
    "global_load_dwordx4 a[24:27], v45, s[32:33] sc1\n"
    "v_add_u32 v44, s36, v33\n"
    "v_mfma_f32_16x16x32_bf16 v[172:175], v[24:27], a[44:47], v[172:175]\n"
    "s_waitcnt lgkmcnt(4)\n"
    "ds_read_b64_tr_b16 v[24:25], v4 offset:192\n"
    "ds_read_b64_tr_b16 v[26:27], v4 offset:3776\n"
    "s_waitcnt lgkmcnt(0)\n"
    "s_barrier\n"
    "ds_read_b128 v[64:67], v244\n"
    "ds_read_b128 v[68:71], v244 offset:1024\n"
    "ds_read_b128 v[72:75], v244 offset:2048\n"
    "ds_read_b128 v[76:79], v244 offset:3072\n"
    "ds_read_b128 v[80:83], v244 offset:4096\n"
    "ds_read_b128 v[84:87], v244 offset:5120\n"
    "ds_read_b128 v[88:91], v244 offset:6144\n"
    "ds_read_b128 v[92:95], v244 offset:7168\n"
    "v_mfma_f32_16x16x32_bf16 v[176:179], v[28:31], a[32:35], v[176:179]\n"
    "s_add_u32 m0, s75, 0xd000\n"
    "v_accvgpr_read_b32 v48, a64\n"
    "v_accvgpr_read_b32 v49, a65\n"
    "v_accvgpr_read_b32 v50, a66\n"
    "v_mfma_f32_16x16x32_bf16 v[180:183], v[28:31], a[36:39], v[180:183]\n"
    "global_load_lds_dwordx4 v60, s[70:71]\n"
    "v_accvgpr_read_b32 v51, a67\n"
    "v_mfma_f32_16x16x32_bf16 v[184:187], v[28:31], a[40:43], v[184:187]\n"
    "v_mul_f32 v48, v48, v12\n"
    "v_mul_f32 v49, v49, v12\n"
    "v_mul_f32 v50, v50, v12\n"
    "v_mfma_f32_16x16x32_bf16 v[188:191], v[28:31], a[44:47], v[188:191]\n"
    "s_waitcnt vmcnt(19)\n"
    "ds_read_b64_tr_b16 v[28:29], v56\n"
    "ds_read_b64_tr_b16 v[30:31], v56 offset:3584\n"
    "v_mfma_f32_16x16x32_bf16 v[192:195], v[16:19], a[32:35], v[192:195]\n"
    "v_mul_f32 v51, v51, v12\n"
    "s_waitcnt lgkmcnt(9)\n"
    "v_fma_f32 v64, v64, v8, v48\n"
    "v_fma_f32 v65, v65, v8, v49\n"
    "v_mfma_f32_16x16x32_bf16 v[196:199], v[16:19], a[36:39], v[196:199]\n"
    "global_load_lds_dwordx4 v61, s[70:71] offset:1024\n"
    "v_fma_f32 v66, v66, v8, v50\n"
    "v_mfma_f32_16x16x32_bf16 v[200:203], v[16:19], a[40:43], v[200:203]\n"
    "global_load_dwordx4 a[28:31], v46, s[32:33] sc1\n"
    "v_fma_f32 v67, v67, v8, v51\n"
    "v_accvgpr_read_b32 v52, a80\n"
    "v_mfma_f32_16x16x32_bf16 v[204:207], v[16:19], a[44:47], v[204:207]\n"
    "s_add_u32 s32, s32, 0x1c0000\n"
    "s_addc_u32 s33, s33, 0\n"
    "ds_read_b64_tr_b16 v[16:17], v56 offset:32\n"
    "ds_read_b64_tr_b16 v[18:19], v56 offset:3616\n"
    "v_mfma_f32_16x16x32_bf16 v[208:211], v[20:23], a[32:35], v[208:211]\n"
    "v_accvgpr_read_b32 v53, a81\n"
    "v_accvgpr_read_b32 v54, a82\n"
    "v_accvgpr_read_b32 v55, a83\n"
    "v_mfma_f32_16x16x32_bf16 v[212:215], v[20:23], a[36:39], v[212:215]\n"
    "global_load_lds_dwordx4 v62, s[70:71] offset:2048\n"
    "v_mul_f32 v52, v52, v12\n"
    "v_mfma_f32_16x16x32_bf16 v[216:219], v[20:23], a[40:43], v[216:219]\n"
    "s_sub_u32 s70, s70, 0x4d000\n"
    "s_subb_u32 s71, s71, 0\n"
    "global_load_dwordx4 a[128:131], v45, s[32:33] sc1\n"
    "v_mul_f32 v53, v53, v12\n"
    "v_mfma_f32_16x16x32_bf16 v[220:223], v[20:23], a[44:47], v[220:223]\n"
    "ds_read_b64_tr_b16 v[20:21], v56 offset:64\n"
    "ds_read_b64_tr_b16 v[22:23], v56 offset:3648\n"
    "v_mfma_f32_16x16x32_bf16 v[224:227], v[24:27], a[32:35], v[224:227]\n"
    "s_add_u32 m0, s75, 0x21000\n"
    "v_mul_f32 v54, v54, v12\n"
    "v_mul_f32 v55, v55, v12\n"
    "s_waitcnt lgkmcnt(9)\n"
    "v_fma_f32 v80, v80, v8, v52\n"
    "v_mfma_f32_16x16x32_bf16 v[228:231], v[24:27], a[36:39], v[228:231]\n"
    "global_load_lds_dwordx4 v0, s[70:71]\n"
    "v_fma_f32 v81, v81, v8, v53\n"
    "v_mfma_f32_16x16x32_bf16 v[232:235], v[24:27], a[40:43], v[232:235]\n"
    "v_fma_f32 v82, v82, v8, v54\n"
    "v_fma_f32 v83, v83, v8, v55\n"
    "v_cvt_pk_bf16_f32 v64, v64, v65\n"
    "v_mfma_f32_16x16x32_bf16 v[236:239], v[24:27], a[44:47], v[236:239]\n"
    "s_waitcnt lgkmcnt(4)\n"
    "ds_read_b64_tr_b16 v[24:25], v56 offset:96\n"
    "ds_read_b64_tr_b16 v[26:27], v56 offset:3680\n"
    "v_mfma_f32_16x16x32_bf16 v[128:131], v[28:31], a[48:51], v[128:131]\n"
    "v_cvt_pk_bf16_f32 v65, v66, v67\n"
    "v_cvt_pk_bf16_f32 v66, v80, v81\n"
    "v_cvt_pk_bf16_f32 v67, v82, v83\n"
    "v_mfma_f32_16x16x32_bf16 v[132:135], v[28:31], a[52:55], v[132:135]\n"
    "global_load_lds_dwordx4 v1, s[70:71] offset:1024\n"
    "v_mfma_f32_16x16x32_bf16 v[136:139], v[28:31], a[56:59], v[136:139]\n"
    "global_load_dwordx4 a[132:135], v46, s[32:33] sc1\n"
    "ds_write_b128 v245, v[64:67]\n"
    "v_mfma_f32_16x16x32_bf16 v[140:143], v[28:31], a[60:63], v[140:143]\n"
    "s_waitcnt lgkmcnt(5)\n"
    "s_add_u32 s32, s32, 0x1c0000\n"
    "s_addc_u32 s33, s33, 0\n"
    "ds_read_b64_tr_b16 v[28:29], v56 offset:128\n"
    "ds_read_b64_tr_b16 v[30:31], v56 offset:3712\n"
    "v_mfma_f32_16x16x32_bf16 v[144:147], v[16:19], a[48:51], v[144:147]\n"
    "v_accvgpr_read_b32 v48, a68\n"
    "v_accvgpr_read_b32 v49, a69\n"
    "v_mfma_f32_16x16x32_bf16 v[148:151], v[16:19], a[52:55], v[148:151]\n"
    "global_load_lds_dwordx4 v2, s[70:71] offset:2048\n"
    "v_accvgpr_read_b32 v50, a70\n"
    "v_accvgpr_read_b32 v51, a71\n"
    "v_mfma_f32_16x16x32_bf16 v[152:155], v[16:19], a[56:59], v[152:155]\n"
    "v_mul_f32 v48, v48, v13\n"
    "v_mul_f32 v49, v49, v13\n"
    "v_mfma_f32_16x16x32_bf16 v[156:159], v[16:19], a[60:63], v[156:159]\n"
    "s_waitcnt lgkmcnt(5)\n"
    "ds_read_b64_tr_b16 v[16:17], v56 offset:160\n"
    "ds_read_b64_tr_b16 v[18:19], v56 offset:3744\n"
    "v_mul_f32 v50, v50, v13\n"
    "v_mfma_f32_16x16x32_bf16 v[160:163], v[20:23], a[48:51], v[160:163]\n"
    "v_mul_f32 v51, v51, v13\n"
    "v_fma_f32 v68, v68, v9, v48\n"
    "v_mfma_f32_16x16x32_bf16 v[164:167], v[20:23], a[52:55], v[164:167]\n"
    "global_load_lds_dwordx4 v3, s[70:71] offset:3072\n"
    "v_fma_f32 v69, v69, v9, v49\n"
    "v_fma_f32 v70, v70, v9, v50\n"
    "v_mfma_f32_16x16x32_bf16 v[168:171], v[20:23], a[56:59], v[168:171]\n"
    "global_load_dwordx4 a[136:139], v45, s[32:33] sc1\n"
    "v_fma_f32 v71, v71, v9, v51\n"
    "v_mfma_f32_16x16x32_bf16 v[172:175], v[20:23], a[60:63], v[172:175]\n"
    "s_waitcnt lgkmcnt(5)\n"
    "ds_read_b64_tr_b16 v[20:21], v56 offset:192\n"
    "ds_read_b64_tr_b16 v[22:23], v56 offset:3776\n"
    "v_accvgpr_read_b32 v52, a84\n"
    "v_mfma_f32_16x16x32_bf16 v[176:179], v[24:27], a[48:51], v[176:179]\n"
    "s_add_u32 m0, s75, 0x22000\n"
    "v_accvgpr_read_b32 v53, a85\n"
    "v_accvgpr_read_b32 v54, a86\n"
    "v_mfma_f32_16x16x32_bf16 v[180:183], v[24:27], a[52:55], v[180:183]\n"
    "global_load_lds_dwordx4 v60, s[70:71]\n"
    "v_accvgpr_read_b32 v55, a87\n"
    "v_mul_f32 v52, v52, v13\n"
    "v_mfma_f32_16x16x32_bf16 v[184:187], v[24:27], a[56:59], v[184:187]\n"
    "v_mul_f32 v53, v53, v13\n"
    "v_mul_f32 v54, v54, v13\n"
    "v_mfma_f32_16x16x32_bf16 v[188:191], v[24:27], a[60:63], v[188:191]\n"
    "s_waitcnt lgkmcnt(4)\n"
    "s_waitcnt vmcnt(19)\n"
    "ds_read_b64_tr_b16 v[24:25], v57\n"
    "ds_read_b64_tr_b16 v[26:27], v57 offset:3584\n"
    "v_mul_f32 v55, v55, v13\n"
    "v_mfma_f32_16x16x32_bf16 v[192:195], v[28:31], a[48:51], v[192:195]\n"
    "v_fma_f32 v84, v84, v9, v52\n"
    "v_fma_f32 v85, v85, v9, v53\n"
    "v_mfma_f32_16x16x32_bf16 v[196:199], v[28:31], a[52:55], v[196:199]\n"
    "global_load_lds_dwordx4 v61, s[70:71] offset:1024\n"
    "v_fma_f32 v86, v86, v9, v54\n"
    "v_fma_f32 v87, v87, v9, v55\n"
    "v_mfma_f32_16x16x32_bf16 v[200:203], v[28:31], a[56:59], v[200:203]\n"
    "global_load_dwordx4 a[140:143], v46, s[32:33] sc1\n"
    "v_cvt_pk_bf16_f32 v68, v68, v69\n"
    "v_mfma_f32_16x16x32_bf16 v[204:207], v[28:31], a[60:63], v[204:207]\n"
    "s_waitcnt lgkmcnt(4)\n"
    "s_add_u32 s32, s32, 0x1c0000\n"
    "s_addc_u32 s33, s33, 0\n"
    "ds_read_b64_tr_b16 v[28:29], v57 offset:32\n"
    "ds_read_b64_tr_b16 v[30:31], v57 offset:3616\n"
    "v_mfma_f32_16x16x32_bf16 v[208:211], v[16:19], a[48:51], v[208:211]\n"
    "v_cvt_pk_bf16_f32 v69, v70, v71\n"
    "v_cvt_pk_bf16_f32 v70, v84, v85\n"
    "v_mfma_f32_16x16x32_bf16 v[212:215], v[16:19], a[52:55], v[212:215]\n"
    "global_load_lds_dwordx4 v62, s[70:71] offset:2048\n"
    "v_cvt_pk_bf16_f32 v71, v86, v87\n"
    "v_mfma_f32_16x16x32_bf16 v[216:219], v[16:19], a[56:59], v[216:219]\n"
    "s_add_u32 s70, s70, 0x1c000\n"
    "s_addc_u32 s71, s71, 0\n"
    "global_load_dwordx4 a[144:147], v45, s[32:33] sc1\n"
    "v_mfma_f32_16x16x32_bf16 v[220:223], v[16:19], a[60:63], v[220:223]\n"
    "s_waitcnt lgkmcnt(4)\n"
    "ds_read_b64_tr_b16 v[16:17], v57 offset:64\n"
    "ds_read_b64_tr_b16 v[18:19], v57 offset:3648\n"
    "v_mfma_f32_16x16x32_bf16 v[224:227], v[20:23], a[48:51], v[224:227]\n"
    "s_add_u32 m0, s75, 0x1a000\n"
    "ds_write_b128 v245, v[68:71] offset:1024\n"
    "v_mfma_f32_16x16x32_bf16 v[228:231], v[20:23], a[52:55], v[228:231]\n"
    "global_load_lds_dwordx4 v0, s[70:71]\n"
    "v_accvgpr_read_b32 v48, a72\n"
    "v_mfma_f32_16x16x32_bf16 v[232:235], v[20:23], a[56:59], v[232:235]\n"
    "v_accvgpr_read_b32 v49, a73\n"
    "v_accvgpr_read_b32 v50, a74\n"
    "v_accvgpr_read_b32 v51, a75\n"
    "v_mfma_f32_16x16x32_bf16 v[236:239], v[20:23], a[60:63], v[236:239]\n"
    "s_waitcnt lgkmcnt(5)\n"
    "ds_read_b64_tr_b16 v[20:21], v57 offset:96\n"
    "ds_read_b64_tr_b16 v[22:23], v57 offset:3680\n"
    "v_mfma_f32_16x16x32_bf16 v[128:131], v[24:27], a[160:163], v[128:131]\n"
    "v_mul_f32 v48, v48, v14\n"
    "v_mul_f32 v49, v49, v14\n"
    "v_mul_f32 v50, v50, v14\n"
    "v_mfma_f32_16x16x32_bf16 v[132:135], v[24:27], a[164:167], v[132:135]\n"
    "global_load_lds_dwordx4 v1, s[70:71] offset:1024\n"
    "v_mul_f32 v51, v51, v14\n"
    "v_mfma_f32_16x16x32_bf16 v[136:139], v[24:27], a[168:171], v[136:139]\n"
    "global_load_dwordx4 a[148:151], v46, s[32:33] sc1\n"
    "v_fma_f32 v72, v72, v10, v48\n"
    "v_fma_f32 v73, v73, v10, v49\n"
    "v_mfma_f32_16x16x32_bf16 v[140:143], v[24:27], a[172:175], v[140:143]\n"
    "s_waitcnt lgkmcnt(5)\n"
    "s_add_u32 s32, s32, 0x1c0000\n"
    "s_addc_u32 s33, s33, 0\n"
    "ds_read_b64_tr_b16 v[24:25], v57 offset:128\n"
    "ds_read_b64_tr_b16 v[26:27], v57 offset:3712\n"
    "v_mfma_f32_16x16x32_bf16 v[144:147], v[28:31], a[160:163], v[144:147]\n"
    "v_fma_f32 v74, v74, v10, v50\n"
    "v_fma_f32 v75, v75, v10, v51\n"
    "v_accvgpr_read_b32 v52, a88\n"
    "v_mfma_f32_16x16x32_bf16 v[148:151], v[28:31], a[164:167], v[148:151]\n"
    "global_load_lds_dwordx4 v2, s[70:71] offset:2048\n"
    "v_accvgpr_read_b32 v53, a89\n"
    "v_mfma_f32_16x16x32_bf16 v[152:155], v[28:31], a[168:171], v[152:155]\n"
    "v_accvgpr_read_b32 v54, a90\n"
    "v_accvgpr_read_b32 v55, a91\n"
    "v_mul_f32 v52, v52, v14\n"
    "v_mfma_f32_16x16x32_bf16 v[156:159], v[28:31], a[172:175], v[156:159]\n"
    "s_waitcnt lgkmcnt(5)\n"
    "ds_read_b64_tr_b16 v[28:29], v57 offset:160\n"
    "ds_read_b64_tr_b16 v[30:31], v57 offset:3744\n"
    "v_mfma_f32_16x16x32_bf16 v[160:163], v[16:19], a[160:163], v[160:163]\n"
    "v_mul_f32 v53, v53, v14\n"
    "v_mul_f32 v54, v54, v14\n"
    "v_mul_f32 v55, v55, v14\n"
    "v_mfma_f32_16x16x32_bf16 v[164:167], v[16:19], a[164:167], v[164:167]\n"
    "global_load_lds_dwordx4 v3, s[70:71] offset:3072\n"
    "v_fma_f32 v88, v88, v10, v52\n"
    "v_mfma_f32_16x16x32_bf16 v[168:171], v[16:19], a[168:171], v[168:171]\n"
    "global_load_dwordx4 a[152:155], v45, s[32:33] sc1\n"
    "v_fma_f32 v89, v89, v10, v53\n"
    "v_fma_f32 v90, v90, v10, v54\n"
    "v_mfma_f32_16x16x32_bf16 v[172:175], v[16:19], a[172:175], v[172:175]\n"
    "s_waitcnt lgkmcnt(4)\n"
    "ds_read_b64_tr_b16 v[16:17], v57 offset:192\n"
    "ds_read_b64_tr_b16 v[18:19], v57 offset:3776\n"
    "v_mfma_f32_16x16x32_bf16 v[176:179], v[20:23], a[160:163], v[176:179]\n"
    "s_add_u32 m0, s75, 0x1b000\n"
    "v_fma_f32 v91, v91, v10, v55\n"
    "v_cvt_pk_bf16_f32 v72, v72, v73\n"
    "v_cvt_pk_bf16_f32 v73, v74, v75\n"
    "v_mfma_f32_16x16x32_bf16 v[180:183], v[20:23], a[164:167], v[180:183]\n"
    "global_load_lds_dwordx4 v60, s[70:71]\n"
    "v_cvt_pk_bf16_f32 v74, v88, v89\n"
    "v_mfma_f32_16x16x32_bf16 v[184:187], v[20:23], a[168:171], v[184:187]\n"
    "v_cvt_pk_bf16_f32 v75, v90, v91\n"
    "ds_write_b128 v245, v[72:75] offset:2048\n"
    "v_mfma_f32_16x16x32_bf16 v[188:191], v[20:23], a[172:175], v[188:191]\n"
    "s_waitcnt lgkmcnt(5)\n"
    "s_waitcnt vmcnt(19)\n"
    "ds_read_b64_tr_b16 v[20:21], v58\n"
    "ds_read_b64_tr_b16 v[22:23], v58 offset:3584\n"
    "v_mfma_f32_16x16x32_bf16 v[192:195], v[24:27], a[160:163], v[192:195]\n"
    "v_accvgpr_read_b32 v48, a76\n"
    "v_accvgpr_read_b32 v49, a77\n"
    "v_accvgpr_read_b32 v50, a78\n"
    "v_mfma_f32_16x16x32_bf16 v[196:199], v[24:27], a[164:167], v[196:199]\n"
    "global_load_lds_dwordx4 v61, s[70:71] offset:1024\n"
    "v_accvgpr_read_b32 v51, a79\n"
    "v_mfma_f32_16x16x32_bf16 v[200:203], v[24:27], a[168:171], v[200:203]\n"
    "global_load_dwordx4 a[156:159], v46, s[32:33] sc1\n"
    "v_mul_f32 v48, v48, v15\n"
    "v_mfma_f32_16x16x32_bf16 v[204:207], v[24:27], a[172:175], v[204:207]\n"
    "s_waitcnt lgkmcnt(5)\n"
    "ds_read_b64_tr_b16 v[24:25], v58 offset:32\n"
    "ds_read_b64_tr_b16 v[26:27], v58 offset:3616\n"
    "v_mul_f32 v49, v49, v15\n"
    "v_mfma_f32_16x16x32_bf16 v[208:211], v[28:31], a[160:163], v[208:211]\n"
    "v_mul_f32 v50, v50, v15\n"
    "v_mul_f32 v51, v51, v15\n"
    "v_mfma_f32_16x16x32_bf16 v[212:215], v[28:31], a[164:167], v[212:215]\n"
    "global_load_lds_dwordx4 v62, s[70:71] offset:2048\n"
    "v_fma_f32 v76, v76, v11, v48\n"
    "v_fma_f32 v77, v77, v11, v49\n"
    "v_mfma_f32_16x16x32_bf16 v[216:219], v[28:31], a[168:171], v[216:219]\n"
    "s_add_u32 s70, s70, 0x1c000\n"
    "s_addc_u32 s71, s71, 0\n"
    "v_fma_f32 v78, v78, v11, v50\n"
    "v_mfma_f32_16x16x32_bf16 v[220:223], v[28:31], a[172:175], v[220:223]\n"
    "s_waitcnt lgkmcnt(5)\n"
    "ds_read_b64_tr_b16 v[28:29], v58 offset:64\n"
    "ds_read_b64_tr_b16 v[30:31], v58 offset:3648\n"
    "v_fma_f32 v79, v79, v11, v51\n"
    "v_mfma_f32_16x16x32_bf16 v[224:227], v[16:19], a[160:163], v[224:227]\n"
    "s_add_u32 m0, s75, 0x13000\n"
    "v_accvgpr_read_b32 v52, a92\n"
    "v_accvgpr_read_b32 v53, a93\n"
    "v_mfma_f32_16x16x32_bf16 v[228:231], v[16:19], a[164:167], v[228:231]\n"
    "global_load_lds_dwordx4 v0, s[70:71]\n"
    "v_accvgpr_read_b32 v54, a94\n"
    "v_accvgpr_read_b32 v55, a95\n"
    "v_mfma_f32_16x16x32_bf16 v[232:235], v[16:19], a[168:171], v[232:235]\n"
    "v_mul_f32 v52, v52, v15\n"
    "v_mul_f32 v53, v53, v15\n"
    "v_mfma_f32_16x16x32_bf16 v[236:239], v[16:19], a[172:175], v[236:239]\n"
    "s_waitcnt lgkmcnt(4)\n"
    "ds_read_b64_tr_b16 v[16:17], v58 offset:96\n"
    "ds_read_b64_tr_b16 v[18:19], v58 offset:3680\n"
    "v_mul_f32 v54, v54, v15\n"
    "v_mfma_f32_16x16x32_bf16 v[128:131], v[20:23], a[176:179], v[128:131]\n"
    "v_mul_f32 v55, v55, v15\n"
    "v_fma_f32 v92, v92, v11, v52\n"
    "v_mfma_f32_16x16x32_bf16 v[132:135], v[20:23], a[180:183], v[132:135]\n"
    "global_load_lds_dwordx4 v1, s[70:71] offset:1024\n"
    "v_fma_f32 v93, v93, v11, v53\n"
    "v_fma_f32 v94, v94, v11, v54\n"
    "v_mfma_f32_16x16x32_bf16 v[136:139], v[20:23], a[184:187], v[136:139]\n"
    "global_load_dwordx2 v[32:33], v47, s[48:49]\n"
    "v_fma_f32 v95, v95, v11, v55\n"
    "v_mfma_f32_16x16x32_bf16 v[140:143], v[20:23], a[188:191], v[140:143]\n"
    "s_waitcnt lgkmcnt(4)\n"
    "ds_read_b64_tr_b16 v[20:21], v58 offset:128\n"
    "ds_read_b64_tr_b16 v[22:23], v58 offset:3712\n"
    "v_cvt_pk_bf16_f32 v76, v76, v77\n"
    "v_mfma_f32_16x16x32_bf16 v[144:147], v[24:27], a[176:179], v[144:147]\n"
    "v_cvt_pk_bf16_f32 v77, v78, v79\n"
    "v_cvt_pk_bf16_f32 v78, v92, v93\n"
    "v_mfma_f32_16x16x32_bf16 v[148:151], v[24:27], a[180:183], v[148:151]\n"
    "global_load_lds_dwordx4 v2, s[70:71] offset:2048\n"
    "v_cvt_pk_bf16_f32 v79, v94, v95\n"
    "v_mfma_f32_16x16x32_bf16 v[152:155], v[24:27], a[184:187], v[152:155]\n"
    "global_load_dwordx2 v[34:35], v47, s[38:39]\n"
    "v_mfma_f32_16x16x32_bf16 v[156:159], v[24:27], a[188:191], v[156:159]\n"
    "s_waitcnt lgkmcnt(4)\n"
    "ds_read_b64_tr_b16 v[24:25], v58 offset:160\n"
    "ds_read_b64_tr_b16 v[26:27], v58 offset:3744\n"
    "v_mfma_f32_16x16x32_bf16 v[160:163], v[28:31], a[176:179], v[160:163]\n"
    "ds_write_b128 v245, v[76:79] offset:3072\n"
    "v_mfma_f32_16x16x32_bf16 v[164:167], v[28:31], a[180:183], v[164:167]\n"
    "global_load_lds_dwordx4 v3, s[70:71] offset:3072\n"
    "v_mfma_f32_16x16x32_bf16 v[168:171], v[28:31], a[184:187], v[168:171]\n"
    "global_load_dwordx2 v[40:41], v5, s[64:65]\n"
    "v_mfma_f32_16x16x32_bf16 v[172:175], v[28:31], a[188:191], v[172:175]\n"
    "s_waitcnt lgkmcnt(5)\n"
    "ds_read_b64_tr_b16 v[28:29], v58 offset:192\n"
    "ds_read_b64_tr_b16 v[30:31], v58 offset:3776\n"
    "s_waitcnt lgkmcnt(0)\n"
    "s_barrier\n"
    "ds_read_b128 a[64:67], v246\n"
    "ds_read_b128 a[68:71], v246 offset:1024\n"
    "ds_read_b128 a[72:75], v246 offset:2048\n"
    "ds_read_b128 a[76:79], v246 offset:3072\n"
    "ds_read_b128 a[80:83], v246 offset:4096\n"
    "ds_read_b128 a[84:87], v246 offset:5120\n"
    "ds_read_b128 a[88:91], v246 offset:6144\n"
    "ds_read_b128 a[92:95], v246 offset:7168\n"
    "ds_read_b128 a[192:195], v246 offset:8192\n"
    "ds_read_b128 a[196:199], v246 offset:9216\n"
    "ds_read_b128 a[200:203], v246 offset:10240\n"
    "ds_read_b128 a[204:207], v246 offset:11264\n"
    "ds_read_b128 a[208:211], v246 offset:12288\n"
    "ds_read_b128 a[212:215], v246 offset:13312\n"
    "ds_read_b128 a[216:219], v246 offset:14336\n"
    "ds_read_b128 a[220:223], v246 offset:15360\n"
    "v_mfma_f32_16x16x32_bf16 v[176:179], v[16:19], a[176:179], v[176:179]\n"
    "s_add_u32 m0, s75, 0x14000\n"
    "ds_write_b128 v243, a[224:227]\n"
    "v_mfma_f32_16x16x32_bf16 v[180:183], v[16:19], a[180:183], v[180:183]\n"
    "global_load_lds_dwordx4 v60, s[70:71]\n"
    "v_mfma_f32_16x16x32_bf16 v[184:187], v[16:19], a[184:187], v[184:187]\n"
    "ds_write_b128 v243, a[228:231] offset:1024\n"
    "v_mfma_f32_16x16x32_bf16 v[188:191], v[16:19], a[188:191], v[188:191]\n"
    "s_waitcnt vmcnt(19)\n"
    "ds_read_b64_tr_b16 v[16:17], v4\n"
    "ds_read_b64_tr_b16 v[18:19], v4 offset:3584\n"
    "v_mfma_f32_16x16x32_bf16 v[192:195], v[20:23], a[176:179], v[192:195]\n"
    "ds_write_b128 v243, a[232:235] offset:2048\n"
    "v_mfma_f32_16x16x32_bf16 v[196:199], v[20:23], a[180:183], v[196:199]\n"
    "global_load_lds_dwordx4 v61, s[70:71] offset:1024\n"
    "v_mfma_f32_16x16x32_bf16 v[200:203], v[20:23], a[184:187], v[200:203]\n"
    "global_load_dwordx2 v[36:37], v44, s[48:49]\n"
    "ds_write_b128 v243, a[236:239] offset:3072\n"
    "v_mfma_f32_16x16x32_bf16 v[204:207], v[20:23], a[188:191], v[204:207]\n"
    "ds_read_b64_tr_b16 v[20:21], v4 offset:32\n"
    "ds_read_b64_tr_b16 v[22:23], v4 offset:3616\n"
    "v_mfma_f32_16x16x32_bf16 v[208:211], v[24:27], a[176:179], v[208:211]\n"
    "ds_write_b128 v243, a[240:243] offset:4096\n"
    "v_mfma_f32_16x16x32_bf16 v[212:215], v[24:27], a[180:183], v[212:215]\n"
    "global_load_lds_dwordx4 v62, s[70:71] offset:2048\n"
    "v_mfma_f32_16x16x32_bf16 v[216:219], v[24:27], a[184:187], v[216:219]\n"
    "s_add_u32 s70, s70, 0x1c000\n"
    "s_addc_u32 s71, s71, 0\n"
    "global_load_dwordx2 v[38:39], v44, s[38:39]\n"
    "ds_write_b128 v243, a[244:247] offset:5120\n"
    "v_mfma_f32_16x16x32_bf16 v[220:223], v[24:27], a[188:191], v[220:223]\n"
    "ds_read_b64_tr_b16 v[24:25], v4 offset:64\n"
    "ds_read_b64_tr_b16 v[26:27], v4 offset:3648\n"
    "v_mfma_f32_16x16x32_bf16 v[224:227], v[28:31], a[176:179], v[224:227]\n"
    "s_add_u32 m0, s75, 0xc000\n"
    "ds_write_b128 v243, a[248:251] offset:6144\n"
    "v_mfma_f32_16x16x32_bf16 v[228:231], v[28:31], a[180:183], v[228:231]\n"
    "global_load_lds_dwordx4 v0, s[70:71]\n"
    "v_mfma_f32_16x16x32_bf16 v[232:235], v[28:31], a[184:187], v[232:235]\n"
    "global_load_dwordx2 v[42:43], v6, s[64:65]\n"
    "ds_write_b128 v243, a[252:255] offset:7168\n"
    "v_mfma_f32_16x16x32_bf16 v[236:239], v[28:31], a[188:191], v[236:239]\n"
    "s_waitcnt lgkmcnt(10)\n"
    "ds_read_b64_tr_b16 v[28:29], v4 offset:96\n"
    "ds_read_b64_tr_b16 v[30:31], v4 offset:3680\n"
    "v_mfma_f32_16x16x32_bf16 v[128:131], v[16:19], a[64:67], v[128:131]\n"
    "v_mfma_f32_16x16x32_bf16 v[132:135], v[16:19], a[68:71], v[132:135]\n"
    "global_load_lds_dwordx4 v1, s[70:71] offset:1024\n"
    "v_mfma_f32_16x16x32_bf16 v[136:139], v[16:19], a[72:75], v[136:139]\n"
    "v_mfma_f32_16x16x32_bf16 v[140:143], v[16:19], a[76:79], v[140:143]\n"
    "s_waitcnt lgkmcnt(8)\n"
    "ds_read_b64_tr_b16 v[16:17], v4 offset:128\n"
    "ds_read_b64_tr_b16 v[18:19], v4 offset:3712\n"
    "v_mfma_f32_16x16x32_bf16 v[144:147], v[20:23], a[64:67], v[144:147]\n"
    "v_mfma_f32_16x16x32_bf16 v[148:151], v[20:23], a[68:71], v[148:151]\n"
    "global_load_lds_dwordx4 v2, s[70:71] offset:2048\n"
    "v_mfma_f32_16x16x32_bf16 v[152:155], v[20:23], a[72:75], v[152:155]\n"
    "v_mfma_f32_16x16x32_bf16 v[156:159], v[20:23], a[76:79], v[156:159]\n"
    "s_waitcnt lgkmcnt(6)\n"
    "ds_read_b64_tr_b16 v[20:21], v4 offset:160\n"
    "ds_read_b64_tr_b16 v[22:23], v4 offset:3744\n"
    "v_mfma_f32_16x16x32_bf16 v[160:163], v[24:27], a[64:67], v[160:163]\n"
    "v_mfma_f32_16x16x32_bf16 v[164:167], v[24:27], a[68:71], v[164:167]\n"
    "global_load_lds_dwordx4 v3, s[70:71] offset:3072\n"
    "v_mfma_f32_16x16x32_bf16 v[168:171], v[24:27], a[72:75], v[168:171]\n"
    "v_mfma_f32_16x16x32_bf16 v[172:175], v[24:27], a[76:79], v[172:175]\n"
    "s_waitcnt lgkmcnt(4)\n"
    "ds_read_b64_tr_b16 v[24:25], v4 offset:192\n"
    "ds_read_b64_tr_b16 v[26:27], v4 offset:3776\n"
    "s_waitcnt lgkmcnt(0)\n"
    "s_barrier\n"
    "ds_read_b128 v[64:67], v244\n"
    "ds_read_b128 v[68:71], v244 offset:1024\n"
    "ds_read_b128 v[72:75], v244 offset:2048\n"
    "ds_read_b128 v[76:79], v244 offset:3072\n"
    "ds_read_b128 v[80:83], v244 offset:4096\n"
    "ds_read_b128 v[84:87], v244 offset:5120\n"
    "ds_read_b128 v[88:91], v244 offset:6144\n"
    "ds_read_b128 v[92:95], v244 offset:7168\n"
    "v_mfma_f32_16x16x32_bf16 v[176:179], v[28:31], a[64:67], v[176:179]\n"
    "s_add_u32 m0, s75, 0xd000\n"
    "v_accvgpr_read_b32 v48, a96\n"
    "v_accvgpr_read_b32 v49, a97\n"
    "v_accvgpr_read_b32 v50, a98\n"
    "v_mfma_f32_16x16x32_bf16 v[180:183], v[28:31], a[68:71], v[180:183]\n"
    "global_load_lds_dwordx4 v60, s[70:71]\n"
    "v_accvgpr_read_b32 v51, a99\n"
    "v_mfma_f32_16x16x32_bf16 v[184:187], v[28:31], a[72:75], v[184:187]\n"
    "v_mul_f32 v48, v48, v12\n"
    "v_mul_f32 v49, v49, v12\n"
    "v_mfma_f32_16x16x32_bf16 v[188:191], v[28:31], a[76:79], v[188:191]\n"
    "s_waitcnt vmcnt(18)\n"
    "ds_read_b64_tr_b16 v[28:29], v56\n"
    "ds_read_b64_tr_b16 v[30:31], v56 offset:3584\n"
    "v_mul_f32 v50, v50, v12\n"
    "v_mfma_f32_16x16x32_bf16 v[192:195], v[16:19], a[64:67], v[192:195]\n"
    "v_mul_f32 v51, v51, v12\n"
    "s_waitcnt lgkmcnt(9)\n"
    "v_fma_f32 v64, v64, v8, v48\n"
    "v_mfma_f32_16x16x32_bf16 v[196:199], v[16:19], a[68:71], v[196:199]\n"
    "global_load_lds_dwordx4 v61, s[70:71] offset:1024\n"
    "v_fma_f32 v65, v65, v8, v49\n"
    "v_mfma_f32_16x16x32_bf16 v[200:203], v[16:19], a[72:75], v[200:203]\n"
    "v_fma_f32 v66, v66, v8, v50\n"
    "v_fma_f32 v67, v67, v8, v51\n"
    "v_mfma_f32_16x16x32_bf16 v[204:207], v[16:19], a[76:79], v[204:207]\n"
    "ds_read_b64_tr_b16 v[16:17], v56 offset:32\n"
    "ds_read_b64_tr_b16 v[18:19], v56 offset:3616\n"
    "v_accvgpr_read_b32 v52, a112\n"
    "v_mfma_f32_16x16x32_bf16 v[208:211], v[20:23], a[64:67], v[208:211]\n"
    "v_accvgpr_read_b32 v53, a113\n"
    "v_accvgpr_read_b32 v54, a114\n"
    "v_mfma_f32_16x16x32_bf16 v[212:215], v[20:23], a[68:71], v[212:215]\n"
    "global_load_lds_dwordx4 v62, s[70:71] offset:2048\n"
    "v_accvgpr_read_b32 v55, a115\n"
    "v_mfma_f32_16x16x32_bf16 v[216:219], v[20:23], a[72:75], v[216:219]\n"
    "s_sub_u32 s70, s70, 0x4d000\n"
    "s_subb_u32 s71, s71, 0\n"
    "v_mul_f32 v52, v52, v12\n"
    "v_mul_f32 v53, v53, v12\n"
    "v_mfma_f32_16x16x32_bf16 v[220:223], v[20:23], a[76:79], v[220:223]\n"
    "ds_read_b64_tr_b16 v[20:21], v56 offset:64\n"
    "ds_read_b64_tr_b16 v[22:23], v56 offset:3648\n"
    "v_mfma_f32_16x16x32_bf16 v[224:227], v[24:27], a[64:67], v[224:227]\n"
    "s_add_u32 m0, s75, 0x21000\n"
    "v_mul_f32 v54, v54, v12\n"
    "v_mul_f32 v55, v55, v12\n"
    "v_mfma_f32_16x16x32_bf16 v[228:231], v[24:27], a[68:71], v[228:231]\n"
    "global_load_lds_dwordx4 v0, s[70:71]\n"
    "s_waitcnt lgkmcnt(9)\n"
    "v_fma_f32 v80, v80, v8, v52\n"
    "v_mfma_f32_16x16x32_bf16 v[232:235], v[24:27], a[72:75], v[232:235]\n"
    "v_fma_f32 v81, v81, v8, v53\n"
    "v_fma_f32 v82, v82, v8, v54\n"
    "v_fma_f32 v83, v83, v8, v55\n"
    "v_mfma_f32_16x16x32_bf16 v[236:239], v[24:27], a[76:79], v[236:239]\n"
    "s_waitcnt lgkmcnt(4)\n"
    "ds_read_b64_tr_b16 v[24:25], v56 offset:96\n"
    "ds_read_b64_tr_b16 v[26:27], v56 offset:3680\n"
    "v_mfma_f32_16x16x32_bf16 v[128:131], v[28:31], a[80:83], v[128:131]\n"
    "v_cvt_pk_bf16_f32 v64, v64, v65\n"
    "v_cvt_pk_bf16_f32 v65, v66, v67\n"
    "v_mfma_f32_16x16x32_bf16 v[132:135], v[28:31], a[84:87], v[132:135]\n"
    "global_load_lds_dwordx4 v1, s[70:71] offset:1024\n"
    "v_cvt_pk_bf16_f32 v66, v80, v81\n"
    "v_cvt_pk_bf16_f32 v67, v82, v83\n"
    "v_mfma_f32_16x16x32_bf16 v[136:139], v[28:31], a[88:91], v[136:139]\n"
    "ds_write_b128 v245, v[64:67]\n"
    "v_mfma_f32_16x16x32_bf16 v[140:143], v[28:31], a[92:95], v[140:143]\n"
    "s_waitcnt lgkmcnt(5)\n"
    "ds_read_b64_tr_b16 v[28:29], v56 offset:128\n"
    "ds_read_b64_tr_b16 v[30:31], v56 offset:3712\n"
    "v_mfma_f32_16x16x32_bf16 v[144:147], v[16:19], a[80:83], v[144:147]\n"
    "v_accvgpr_read_b32 v48, a100\n"
    "v_accvgpr_read_b32 v49, a101\n"
    "v_mfma_f32_16x16x32_bf16 v[148:151], v[16:19], a[84:87], v[148:151]\n"
    "global_load_lds_dwordx4 v2, s[70:71] offset:2048\n"
    "v_accvgpr_read_b32 v50, a102\n"
    "v_accvgpr_read_b32 v51, a103\n"
    "v_mfma_f32_16x16x32_bf16 v[152:155], v[16:19], a[88:91], v[152:155]\n"
    "v_mul_f32 v48, v48, v13\n"
    "v_mul_f32 v49, v49, v13\n"
    "v_mfma_f32_16x16x32_bf16 v[156:159], v[16:19], a[92:95], v[156:159]\n"
    "s_waitcnt lgkmcnt(5)\n"
    "ds_read_b64_tr_b16 v[16:17], v56 offset:160\n"
    "ds_read_b64_tr_b16 v[18:19], v56 offset:3744\n"
    "v_mfma_f32_16x16x32_bf16 v[160:163], v[20:23], a[80:83], v[160:163]\n"
    "v_mul_f32 v50, v50, v13\n"
    "v_mul_f32 v51, v51, v13\n"
    "v_fma_f32 v68, v68, v9, v48\n"
    "v_mfma_f32_16x16x32_bf16 v[164:167], v[20:23], a[84:87], v[164:167]\n"
    "global_load_lds_dwordx4 v3, s[70:71] offset:3072\n"
    "v_fma_f32 v69, v69, v9, v49\n"
    "v_mfma_f32_16x16x32_bf16 v[168:171], v[20:23], a[88:91], v[168:171]\n"
    "v_fma_f32 v70, v70, v9, v50\n"
    "v_fma_f32 v71, v71, v9, v51\n"
    "v_mfma_f32_16x16x32_bf16 v[172:175], v[20:23], a[92:95], v[172:175]\n"
    "s_waitcnt lgkmcnt(5)\n"
    "ds_read_b64_tr_b16 v[20:21], v56 offset:192\n"
    "ds_read_b64_tr_b16 v[22:23], v56 offset:3776\n"
    "v_mfma_f32_16x16x32_bf16 v[176:179], v[24:27], a[80:83], v[176:179]\n"
    "s_add_u32 m0, s75, 0x22000\n"
    "v_accvgpr_read_b32 v52, a116\n"
    "v_accvgpr_read_b32 v53, a117\n"
    "v_accvgpr_read_b32 v54, a118\n"
    "v_mfma_f32_16x16x32_bf16 v[180:183], v[24:27], a[84:87], v[180:183]\n"
    "global_load_lds_dwordx4 v60, s[70:71]\n"
    "v_accvgpr_read_b32 v55, a119\n"
    "v_mfma_f32_16x16x32_bf16 v[184:187], v[24:27], a[88:91], v[184:187]\n"
    "v_mul_f32 v52, v52, v13\n"
    "v_mul_f32 v53, v53, v13\n"
    "v_mfma_f32_16x16x32_bf16 v[188:191], v[24:27], a[92:95], v[188:191]\n"
    "s_waitcnt lgkmcnt(4)\n"
    "s_waitcnt vmcnt(14)\n"
    "ds_read_b64_tr_b16 v[24:25], v57\n"
    "ds_read_b64_tr_b16 v[26:27], v57 offset:3584\n"
    "v_mul_f32 v54, v54, v13\n"
    "v_mfma_f32_16x16x32_bf16 v[192:195], v[28:31], a[80:83], v[192:195]\n"
    "v_mul_f32 v55, v55, v13\n"
    "v_fma_f32 v84, v84, v9, v52\n"
    "v_mfma_f32_16x16x32_bf16 v[196:199], v[28:31], a[84:87], v[196:199]\n"
    "global_load_lds_dwordx4 v61, s[70:71] offset:1024\n"
    "v_fma_f32 v85, v85, v9, v53\n"
    "v_mfma_f32_16x16x32_bf16 v[200:203], v[28:31], a[88:91], v[200:203]\n"
    "v_fma_f32 v86, v86, v9, v54\n"
    "v_fma_f32 v87, v87, v9, v55\n"
    "v_mfma_f32_16x16x32_bf16 v[204:207], v[28:31], a[92:95], v[204:207]\n"
    "s_waitcnt lgkmcnt(4)\n"
    "ds_read_b64_tr_b16 v[28:29], v57 offset:32\n"
    "ds_read_b64_tr_b16 v[30:31], v57 offset:3616\n"
    "v_cvt_pk_bf16_f32 v68, v68, v69\n"
    "v_mfma_f32_16x16x32_bf16 v[208:211], v[16:19], a[80:83], v[208:211]\n"
    "v_cvt_pk_bf16_f32 v69, v70, v71\n"
    "v_cvt_pk_bf16_f32 v70, v84, v85\n"
    "v_mfma_f32_16x16x32_bf16 v[212:215], v[16:19], a[84:87], v[212:215]\n"
    "global_load_lds_dwordx4 v62, s[70:71] offset:2048\n"
    "v_cvt_pk_bf16_f32 v71, v86, v87\n"
    "v_mfma_f32_16x16x32_bf16 v[216:219], v[16:19], a[88:91], v[216:219]\n"
    "s_add_u32 s70, s70, 0x1c000\n"
    "s_addc_u32 s71, s71, 0\n"
    "ds_write_b128 v245, v[68:71] offset:1024\n"
    "v_mfma_f32_16x16x32_bf16 v[220:223], v[16:19], a[92:95], v[220:223]\n"
    "s_waitcnt lgkmcnt(5)\n"
    "ds_read_b64_tr_b16 v[16:17], v57 offset:64\n"
    "ds_read_b64_tr_b16 v[18:19], v57 offset:3648\n"
    "v_mfma_f32_16x16x32_bf16 v[224:227], v[20:23], a[80:83], v[224:227]\n"
    "s_add_u32 m0, s75, 0x1a000\n"
    "v_accvgpr_read_b32 v48, a104\n"
    "v_accvgpr_read_b32 v49, a105\n"
    "v_mfma_f32_16x16x32_bf16 v[228:231], v[20:23], a[84:87], v[228:231]\n"
    "global_load_lds_dwordx4 v0, s[70:71]\n"
    "v_accvgpr_read_b32 v50, a106\n"
    "v_mfma_f32_16x16x32_bf16 v[232:235], v[20:23], a[88:91], v[232:235]\n"
    "v_accvgpr_read_b32 v51, a107\n"
    "v_mul_f32 v48, v48, v14\n"
    "v_mul_f32 v49, v49, v14\n"
    "v_mfma_f32_16x16x32_bf16 v[236:239], v[20:23], a[92:95], v[236:239]\n"
    "s_waitcnt lgkmcnt(5)\n"
    "ds_read_b64_tr_b16 v[20:21], v57 offset:96\n"
    "ds_read_b64_tr_b16 v[22:23], v57 offset:3680\n"
    "v_mfma_f32_16x16x32_bf16 v[128:131], v[24:27], a[192:195], v[128:131]\n"
    "v_mul_f32 v50, v50, v14\n"
    "v_mul_f32 v51, v51, v14\n"
    "v_mfma_f32_16x16x32_bf16 v[132:135], v[24:27], a[196:199], v[132:135]\n"
    "global_load_lds_dwordx4 v1, s[70:71] offset:1024\n"
    "v_fma_f32 v72, v72, v10, v48\n"
    "v_fma_f32 v73, v73, v10, v49\n"
    "v_mfma_f32_16x16x32_bf16 v[136:139], v[24:27], a[200:203], v[136:139]\n"
    "v_fma_f32 v74, v74, v10, v50\n"
    "v_fma_f32 v75, v75, v10, v51\n"
    "v_mfma_f32_16x16x32_bf16 v[140:143], v[24:27], a[204:207], v[140:143]\n"
    "s_waitcnt lgkmcnt(5)\n"
    "ds_read_b64_tr_b16 v[24:25], v57 offset:128\n"
    "ds_read_b64_tr_b16 v[26:27], v57 offset:3712\n"
    "v_mfma_f32_16x16x32_bf16 v[144:147], v[28:31], a[192:195], v[144:147]\n"
    "v_accvgpr_read_b32 v52, a120\n"
    "v_accvgpr_read_b32 v53, a121\n"
    "v_mfma_f32_16x16x32_bf16 v[148:151], v[28:31], a[196:199], v[148:151]\n"
    "global_load_lds_dwordx4 v2, s[70:71] offset:2048\n"
    "v_accvgpr_read_b32 v54, a122\n"
    "v_accvgpr_read_b32 v55, a123\n"
    "v_mfma_f32_16x16x32_bf16 v[152:155], v[28:31], a[200:203], v[152:155]\n"
    "v_mul_f32 v52, v52, v14\n"
    "v_mul_f32 v53, v53, v14\n"
    "v_mfma_f32_16x16x32_bf16 v[156:159], v[28:31], a[204:207], v[156:159]\n"
    "s_waitcnt lgkmcnt(4)\n"
    "ds_read_b64_tr_b16 v[28:29], v57 offset:160\n"
    "ds_read_b64_tr_b16 v[30:31], v57 offset:3744\n"
    "v_mfma_f32_16x16x32_bf16 v[160:163], v[16:19], a[192:195], v[160:163]\n"
    "v_mul_f32 v54, v54, v14\n"
    "v_mul_f32 v55, v55, v14\n"
    "v_fma_f32 v88, v88, v10, v52\n"
    "v_mfma_f32_16x16x32_bf16 v[164:167], v[16:19], a[196:199], v[164:167]\n"
    "global_load_lds_dwordx4 v3, s[70:71] offset:3072\n"
    "v_fma_f32 v89, v89, v10, v53\n"
    "v_mfma_f32_16x16x32_bf16 v[168:171], v[16:19], a[200:203], v[168:171]\n"
    "v_fma_f32 v90, v90, v10, v54\n"
    "v_fma_f32 v91, v91, v10, v55\n"
    "v_mfma_f32_16x16x32_bf16 v[172:175], v[16:19], a[204:207], v[172:175]\n"
    "s_waitcnt lgkmcnt(4)\n"
    "ds_read_b64_tr_b16 v[16:17], v57 offset:192\n"
    "ds_read_b64_tr_b16 v[18:19], v57 offset:3776\n"
    "v_mfma_f32_16x16x32_bf16 v[176:179], v[20:23], a[192:195], v[176:179]\n"
    "s_add_u32 m0, s75, 0x1b000\n"
    "v_cvt_pk_bf16_f32 v72, v72, v73\n"
    "v_cvt_pk_bf16_f32 v73, v74, v75\n"
    "v_cvt_pk_bf16_f32 v74, v88, v89\n"
    "v_mfma_f32_16x16x32_bf16 v[180:183], v[20:23], a[196:199], v[180:183]\n"
    "global_load_lds_dwordx4 v60, s[70:71]\n"
    "v_cvt_pk_bf16_f32 v75, v90, v91\n"
    "v_mfma_f32_16x16x32_bf16 v[184:187], v[20:23], a[200:203], v[184:187]\n"
    "ds_write_b128 v245, v[72:75] offset:2048\n"
    "v_mfma_f32_16x16x32_bf16 v[188:191], v[20:23], a[204:207], v[188:191]\n"
    "s_waitcnt lgkmcnt(5)\n"
    "s_waitcnt vmcnt(12)\n"
    "ds_read_b64_tr_b16 v[20:21], v58\n"
    "ds_read_b64_tr_b16 v[22:23], v58 offset:3584\n"
    "v_accvgpr_read_b32 v48, a108\n"
    "v_mfma_f32_16x16x32_bf16 v[192:195], v[24:27], a[192:195], v[192:195]\n"
    "v_accvgpr_read_b32 v49, a109\n"
    "v_accvgpr_read_b32 v50, a110\n"
    "v_mfma_f32_16x16x32_bf16 v[196:199], v[24:27], a[196:199], v[196:199]\n"
    "global_load_lds_dwordx4 v61, s[70:71] offset:1024\n"
    "v_accvgpr_read_b32 v51, a111\n"
    "v_mfma_f32_16x16x32_bf16 v[200:203], v[24:27], a[200:203], v[200:203]\n"
    "v_mul_f32 v48, v48, v15\n"
    "v_mul_f32 v49, v49, v15\n"
    "v_mfma_f32_16x16x32_bf16 v[204:207], v[24:27], a[204:207], v[204:207]\n"
    "s_waitcnt lgkmcnt(5)\n"
    "ds_read_b64_tr_b16 v[24:25], v58 offset:32\n"
    "ds_read_b64_tr_b16 v[26:27], v58 offset:3616\n"
    "v_mul_f32 v50, v50, v15\n"
    "v_mfma_f32_16x16x32_bf16 v[208:211], v[28:31], a[192:195], v[208:211]\n"
    "v_mul_f32 v51, v51, v15\n"
    "v_fma_f32 v76, v76, v11, v48\n"
    "v_mfma_f32_16x16x32_bf16 v[212:215], v[28:31], a[196:199], v[212:215]\n"
    "global_load_lds_dwordx4 v62, s[70:71] offset:2048\n"
    "v_fma_f32 v77, v77, v11, v49\n"
    "v_mfma_f32_16x16x32_bf16 v[216:219], v[28:31], a[200:203], v[216:219]\n"
    "s_add_u32 s70, s70, 0x1c000\n"
    "s_addc_u32 s71, s71, 0\n"
    "v_fma_f32 v78, v78, v11, v50\n"
    "v_fma_f32 v79, v79, v11, v51\n"
    "v_mfma_f32_16x16x32_bf16 v[220:223], v[28:31], a[204:207], v[220:223]\n"
    "s_waitcnt lgkmcnt(5)\n"
    "ds_read_b64_tr_b16 v[28:29], v58 offset:64\n"
    "ds_read_b64_tr_b16 v[30:31], v58 offset:3648\n"
    "v_mfma_f32_16x16x32_bf16 v[224:227], v[16:19], a[192:195], v[224:227]\n"
    "s_add_u32 m0, s75, 0x13000\n"
    "v_accvgpr_read_b32 v52, a124\n"
    "v_accvgpr_read_b32 v53, a125\n"
    "v_mfma_f32_16x16x32_bf16 v[228:231], v[16:19], a[196:199], v[228:231]\n"
    "global_load_lds_dwordx4 v0, s[70:71]\n"
    "v_accvgpr_read_b32 v54, a126\n"
    "v_mfma_f32_16x16x32_bf16 v[232:235], v[16:19], a[200:203], v[232:235]\n"
    "v_accvgpr_read_b32 v55, a127\n"
    "v_mul_f32 v52, v52, v15\n"
    "v_mul_f32 v53, v53, v15\n"
    "v_mfma_f32_16x16x32_bf16 v[236:239], v[16:19], a[204:207], v[236:239]\n"
    "s_waitcnt lgkmcnt(4)\n"
    "ds_read_b64_tr_b16 v[16:17], v58 offset:96\n"
    "ds_read_b64_tr_b16 v[18:19], v58 offset:3680\n"
    "v_mfma_f32_16x16x32_bf16 v[128:131], v[20:23], a[208:211], v[128:131]\n"
    "v_mul_f32 v54, v54, v15\n"
    "v_mul_f32 v55, v55, v15\n"
    "v_mfma_f32_16x16x32_bf16 v[132:135], v[20:23], a[212:215], v[132:135]\n"
    "global_load_lds_dwordx4 v1, s[70:71] offset:1024\n"
    "v_fma_f32 v92, v92, v11, v52\n"
    "v_fma_f32 v93, v93, v11, v53\n"
    "v_mfma_f32_16x16x32_bf16 v[136:139], v[20:23], a[216:219], v[136:139]\n"
    "v_fma_f32 v94, v94, v11, v54\n"
    "v_fma_f32 v95, v95, v11, v55\n"
    "v_mfma_f32_16x16x32_bf16 v[140:143], v[20:23], a[220:223], v[140:143]\n"
    "s_waitcnt lgkmcnt(4)\n"
    "ds_read_b64_tr_b16 v[20:21], v58 offset:128\n"
    "ds_read_b64_tr_b16 v[22:23], v58 offset:3712\n"
    "v_mfma_f32_16x16x32_bf16 v[144:147], v[24:27], a[208:211], v[144:147]\n"
    "v_cvt_pk_bf16_f32 v76, v76, v77\n"
    "v_cvt_pk_bf16_f32 v77, v78, v79\n"
    "v_mfma_f32_16x16x32_bf16 v[148:151], v[24:27], a[212:215], v[148:151]\n"
    "global_load_lds_dwordx4 v2, s[70:71] offset:2048\n"
    "v_cvt_pk_bf16_f32 v78, v92, v93\n"
    "v_cvt_pk_bf16_f32 v79, v94, v95\n"
    "v_mfma_f32_16x16x32_bf16 v[152:155], v[24:27], a[216:219], v[152:155]\n"
    "ds_write_b128 v245, v[76:79] offset:3072\n"
    "v_mfma_f32_16x16x32_bf16 v[156:159], v[24:27], a[220:223], v[156:159]\n"
    "s_waitcnt lgkmcnt(5)\n"
    "ds_read_b64_tr_b16 v[24:25], v58 offset:160\n"
    "ds_read_b64_tr_b16 v[26:27], v58 offset:3744\n"
    "v_mfma_f32_16x16x32_bf16 v[160:163], v[28:31], a[208:211], v[160:163]\n"
    "v_mfma_f32_16x16x32_bf16 v[164:167], v[28:31], a[212:215], v[164:167]\n"
    "global_load_lds_dwordx4 v3, s[70:71] offset:3072\n"
    "v_mfma_f32_16x16x32_bf16 v[168:171], v[28:31], a[216:219], v[168:171]\n"
    "v_mfma_f32_16x16x32_bf16 v[172:175], v[28:31], a[220:223], v[172:175]\n"
    "s_waitcnt lgkmcnt(5)\n"
    "ds_read_b64_tr_b16 v[28:29], v58 offset:192\n"
    "ds_read_b64_tr_b16 v[30:31], v58 offset:3776\n"
    "s_waitcnt lgkmcnt(0)\n"
    "s_barrier\n"
    "ds_read_b128 a[96:99], v246\n"
    "ds_read_b128 a[100:103], v246 offset:1024\n"
    "ds_read_b128 a[104:107], v246 offset:2048\n"
    "ds_read_b128 a[108:111], v246 offset:3072\n"
    "ds_read_b128 a[112:115], v246 offset:4096\n"
    "ds_read_b128 a[116:119], v246 offset:5120\n"
    "ds_read_b128 a[120:123], v246 offset:6144\n"
    "ds_read_b128 a[124:127], v246 offset:7168\n"
    "ds_read_b128 a[224:227], v246 offset:8192\n"
    "ds_read_b128 a[228:231], v246 offset:9216\n"
    "ds_read_b128 a[232:235], v246 offset:10240\n"
    "ds_read_b128 a[236:239], v246 offset:11264\n"
    "ds_read_b128 a[240:243], v246 offset:12288\n"
    "ds_read_b128 a[244:247], v246 offset:13312\n"
    "ds_read_b128 a[248:251], v246 offset:14336\n"
    "ds_read_b128 a[252:255], v246 offset:15360\n"
    "v_mfma_f32_16x16x32_bf16 v[176:179], v[16:19], a[208:211], v[176:179]\n"
    "s_add_u32 m0, s75, 0x14000\n"
    "v_mfma_f32_16x16x32_bf16 v[180:183], v[16:19], a[212:215], v[180:183]\n"
    "global_load_lds_dwordx4 v60, s[70:71]\n"
    "v_mfma_f32_16x16x32_bf16 v[184:187], v[16:19], a[216:219], v[184:187]\n"
    "v_mfma_f32_16x16x32_bf16 v[188:191], v[16:19], a[220:223], v[188:191]\n"
    "s_waitcnt vmcnt(12)\n"
    "ds_read_b64_tr_b16 v[16:17], v4\n"
    "ds_read_b64_tr_b16 v[18:19], v4 offset:3584\n"
    "v_mfma_f32_16x16x32_bf16 v[192:195], v[20:23], a[208:211], v[192:195]\n"
    "v_mfma_f32_16x16x32_bf16 v[196:199], v[20:23], a[212:215], v[196:199]\n"
    "global_load_lds_dwordx4 v61, s[70:71] offset:1024\n"
    "v_mfma_f32_16x16x32_bf16 v[200:203], v[20:23], a[216:219], v[200:203]\n"
    "v_mfma_f32_16x16x32_bf16 v[204:207], v[20:23], a[220:223], v[204:207]\n"
    "ds_read_b64_tr_b16 v[20:21], v4 offset:32\n"
    "ds_read_b64_tr_b16 v[22:23], v4 offset:3616\n"
    "v_mfma_f32_16x16x32_bf16 v[208:211], v[24:27], a[208:211], v[208:211]\n"
    "v_mfma_f32_16x16x32_bf16 v[212:215], v[24:27], a[212:215], v[212:215]\n"
    "global_load_lds_dwordx4 v62, s[70:71] offset:2048\n"
    "v_mfma_f32_16x16x32_bf16 v[216:219], v[24:27], a[216:219], v[216:219]\n"
    "s_add_u32 s70, s70, 0x1c000\n"
    "s_addc_u32 s71, s71, 0\n"
    "v_mfma_f32_16x16x32_bf16 v[220:223], v[24:27], a[220:223], v[220:223]\n"
    "ds_read_b64_tr_b16 v[24:25], v4 offset:64\n"
    "ds_read_b64_tr_b16 v[26:27], v4 offset:3648\n"
    "v_mfma_f32_16x16x32_bf16 v[224:227], v[28:31], a[208:211], v[224:227]\n"
    "s_add_u32 m0, s75, 0xc000\n"
    "v_mfma_f32_16x16x32_bf16 v[228:231], v[28:31], a[212:215], v[228:231]\n"
    "global_load_lds_dwordx4 v0, s[70:71]\n"
    "v_mfma_f32_16x16x32_bf16 v[232:235], v[28:31], a[216:219], v[232:235]\n"
    "v_mfma_f32_16x16x32_bf16 v[236:239], v[28:31], a[220:223], v[236:239]\n"
    "s_waitcnt lgkmcnt(4)\n"
    "ds_read_b64_tr_b16 v[28:29], v4 offset:96\n"
    "ds_read_b64_tr_b16 v[30:31], v4 offset:3680\n"
    "global_load_lds_dwordx4 v1, s[70:71] offset:1024\n"
    "v_mfma_f32_16x16x32_bf16 v[128:131], v[16:19], a[96:99], v[128:131]\n"
    "v_mfma_f32_16x16x32_bf16 v[132:135], v[16:19], a[100:103], v[132:135]\n"
    "global_load_lds_dwordx4 v2, s[70:71] offset:2048\n"
    "v_mfma_f32_16x16x32_bf16 v[136:139], v[16:19], a[104:107], v[136:139]\n"
    "v_mfma_f32_16x16x32_bf16 v[140:143], v[16:19], a[108:111], v[140:143]\n"
    "s_waitcnt lgkmcnt(4)\n"
    "ds_read_b64_tr_b16 v[16:17], v4 offset:128\n"
    "ds_read_b64_tr_b16 v[18:19], v4 offset:3712\n"
    "global_load_lds_dwordx4 v3, s[70:71] offset:3072\n"
    "v_mfma_f32_16x16x32_bf16 v[144:147], v[20:23], a[96:99], v[144:147]\n"
    "s_add_u32 m0, s75, 0xd000\n"
    "v_mfma_f32_16x16x32_bf16 v[148:151], v[20:23], a[100:103], v[148:151]\n"
    "global_load_lds_dwordx4 v60, s[70:71]\n"
    "v_mfma_f32_16x16x32_bf16 v[152:155], v[20:23], a[104:107], v[152:155]\n"
    "v_mfma_f32_16x16x32_bf16 v[156:159], v[20:23], a[108:111], v[156:159]\n"
    "s_waitcnt lgkmcnt(4)\n"
    "ds_read_b64_tr_b16 v[20:21], v4 offset:160\n"
    "ds_read_b64_tr_b16 v[22:23], v4 offset:3744\n"
    "global_load_lds_dwordx4 v61, s[70:71] offset:1024\n"
    "v_mfma_f32_16x16x32_bf16 v[160:163], v[24:27], a[96:99], v[160:163]\n"
    "v_mfma_f32_16x16x32_bf16 v[164:167], v[24:27], a[100:103], v[164:167]\n"
    "global_load_lds_dwordx4 v62, s[70:71] offset:2048\n"
    "v_mfma_f32_16x16x32_bf16 v[168:171], v[24:27], a[104:107], v[168:171]\n"
    "v_mfma_f32_16x16x32_bf16 v[172:175], v[24:27], a[108:111], v[172:175]\n"
    "s_waitcnt lgkmcnt(4)\n"
    "ds_read_b64_tr_b16 v[24:25], v4 offset:192\n"
    "ds_read_b64_tr_b16 v[26:27], v4 offset:3776\n"
    "v_mfma_f32_16x16x32_bf16 v[176:179], v[28:31], a[96:99], v[176:179]\n"
    "v_mfma_f32_16x16x32_bf16 v[180:183], v[28:31], a[100:103], v[180:183]\n"
    "v_mfma_f32_16x16x32_bf16 v[184:187], v[28:31], a[104:107], v[184:187]\n"
    "v_mfma_f32_16x16x32_bf16 v[188:191], v[28:31], a[108:111], v[188:191]\n"
    "s_waitcnt lgkmcnt(4)\n"
    "s_waitcnt vmcnt(14)\n"
    "ds_read_b64_tr_b16 v[28:29], v56\n"
    "ds_read_b64_tr_b16 v[30:31], v56 offset:3584\n"
    "v_mfma_f32_16x16x32_bf16 v[192:195], v[16:19], a[96:99], v[192:195]\n"
    "v_mfma_f32_16x16x32_bf16 v[196:199], v[16:19], a[100:103], v[196:199]\n"
    "v_mfma_f32_16x16x32_bf16 v[200:203], v[16:19], a[104:107], v[200:203]\n"
    "v_mfma_f32_16x16x32_bf16 v[204:207], v[16:19], a[108:111], v[204:207]\n"
    "s_waitcnt lgkmcnt(4)\n"
    "ds_read_b64_tr_b16 v[16:17], v56 offset:32\n"
    "ds_read_b64_tr_b16 v[18:19], v56 offset:3616\n"
    "v_mfma_f32_16x16x32_bf16 v[208:211], v[20:23], a[96:99], v[208:211]\n"
    "v_mfma_f32_16x16x32_bf16 v[212:215], v[20:23], a[100:103], v[212:215]\n"
    "v_mfma_f32_16x16x32_bf16 v[216:219], v[20:23], a[104:107], v[216:219]\n"
    "v_mfma_f32_16x16x32_bf16 v[220:223], v[20:23], a[108:111], v[220:223]\n"
    "s_waitcnt lgkmcnt(4)\n"
    "ds_read_b64_tr_b16 v[20:21], v56 offset:64\n"
    "ds_read_b64_tr_b16 v[22:23], v56 offset:3648\n"
    "v_mfma_f32_16x16x32_bf16 v[224:227], v[24:27], a[96:99], v[224:227]\n"
    "v_mfma_f32_16x16x32_bf16 v[228:231], v[24:27], a[100:103], v[228:231]\n"
    "v_mfma_f32_16x16x32_bf16 v[232:235], v[24:27], a[104:107], v[232:235]\n"
    "v_mfma_f32_16x16x32_bf16 v[236:239], v[24:27], a[108:111], v[236:239]\n"
    "s_waitcnt lgkmcnt(4)\n"
    "ds_read_b64_tr_b16 v[24:25], v56 offset:96\n"
    "ds_read_b64_tr_b16 v[26:27], v56 offset:3680\n"
    "v_mfma_f32_16x16x32_bf16 v[128:131], v[28:31], a[112:115], v[128:131]\n"
    "v_mfma_f32_16x16x32_bf16 v[132:135], v[28:31], a[116:119], v[132:135]\n"
    "v_mfma_f32_16x16x32_bf16 v[136:139], v[28:31], a[120:123], v[136:139]\n"
    "s_and_b32 s42, s13, 1\n"
    "s_lshr_b32 s43, s13, 1\n"
    "s_cmp_eq_u32 s43, s11\n"
    "s_cselect_b32 s43, s42, 2\n"
    "v_mfma_f32_16x16x32_bf16 v[140:143], v[28:31], a[124:127], v[140:143]\n"
    "s_waitcnt lgkmcnt(4)\n"
    "ds_read_b64_tr_b16 v[28:29], v56 offset:128\n"
    "ds_read_b64_tr_b16 v[30:31], v56 offset:3712\n"
    "v_mfma_f32_16x16x32_bf16 v[144:147], v[16:19], a[112:115], v[144:147]\n"
    "v_mfma_f32_16x16x32_bf16 v[148:151], v[16:19], a[116:119], v[148:151]\n"
    "v_mfma_f32_16x16x32_bf16 v[152:155], v[16:19], a[120:123], v[152:155]\n"
    "v_mfma_f32_16x16x32_bf16 v[156:159], v[16:19], a[124:127], v[156:159]\n"
    "s_waitcnt lgkmcnt(4)\n"
    "ds_read_b64_tr_b16 v[16:17], v56 offset:160\n"
    "ds_read_b64_tr_b16 v[18:19], v56 offset:3744\n"
    "v_mfma_f32_16x16x32_bf16 v[160:163], v[20:23], a[112:115], v[160:163]\n"
    "v_mfma_f32_16x16x32_bf16 v[164:167], v[20:23], a[116:119], v[164:167]\n"
    "v_mfma_f32_16x16x32_bf16 v[168:171], v[20:23], a[120:123], v[168:171]\n"
    "v_mfma_f32_16x16x32_bf16 v[172:175], v[20:23], a[124:127], v[172:175]\n"
    "s_waitcnt lgkmcnt(4)\n"
    "ds_read_b64_tr_b16 v[20:21], v56 offset:192\n"
    "ds_read_b64_tr_b16 v[22:23], v56 offset:3776\n"
    "v_mfma_f32_16x16x32_bf16 v[176:179], v[24:27], a[112:115], v[176:179]\n"
    "v_mfma_f32_16x16x32_bf16 v[180:183], v[24:27], a[116:119], v[180:183]\n"
    "v_mfma_f32_16x16x32_bf16 v[184:187], v[24:27], a[120:123], v[184:187]\n"
    "v_mfma_f32_16x16x32_bf16 v[188:191], v[24:27], a[124:127], v[188:191]\n"
    "s_waitcnt lgkmcnt(4)\n"
    "s_waitcnt vmcnt(7)\n"
    "ds_read_b64_tr_b16 v[24:25], v57\n"
    "ds_read_b64_tr_b16 v[26:27], v57 offset:3584\n"
    "v_mfma_f32_16x16x32_bf16 v[192:195], v[28:31], a[112:115], v[192:195]\n"
    "v_mfma_f32_16x16x32_bf16 v[196:199], v[28:31], a[116:119], v[196:199]\n"
    "v_mfma_f32_16x16x32_bf16 v[200:203], v[28:31], a[120:123], v[200:203]\n"
    "v_mfma_f32_16x16x32_bf16 v[204:207], v[28:31], a[124:127], v[204:207]\n"
    "s_waitcnt lgkmcnt(4)\n"
    "s_waitcnt vmcnt(0)\n"
    "ds_read_b64_tr_b16 v[28:29], v58\n"
    "ds_read_b64_tr_b16 v[30:31], v58 offset:3584\n"
    "v_mfma_f32_16x16x32_bf16 v[208:211], v[16:19], a[112:115], v[208:211]\n"
    "v_mfma_f32_16x16x32_bf16 v[212:215], v[16:19], a[116:119], v[212:215]\n"
    "v_mfma_f32_16x16x32_bf16 v[216:219], v[16:19], a[120:123], v[216:219]\n"
    "v_mfma_f32_16x16x32_bf16 v[220:223], v[16:19], a[124:127], v[220:223]\n"
    "s_waitcnt lgkmcnt(4)\n"
    "ds_read_b64_tr_b16 v[16:17], v57 offset:32\n"
    "ds_read_b64_tr_b16 v[18:19], v57 offset:3616\n"
    "v_mfma_f32_16x16x32_bf16 v[224:227], v[20:23], a[112:115], v[224:227]\n"
    "v_mfma_f32_16x16x32_bf16 v[228:231], v[20:23], a[116:119], v[228:231]\n"
    "v_mfma_f32_16x16x32_bf16 v[232:235], v[20:23], a[120:123], v[232:235]\n"
    "v_mfma_f32_16x16x32_bf16 v[236:239], v[20:23], a[124:127], v[236:239]\n"
    "s_waitcnt lgkmcnt(4)\n"
    "ds_read_b64_tr_b16 v[20:21], v58 offset:32\n"
    "ds_read_b64_tr_b16 v[22:23], v58 offset:3616\n"
    "v_mfma_f32_16x16x32_bf16 v[128:131], v[24:27], a[224:227], v[128:131]\n"
    "v_mfma_f32_16x16x32_bf16 v[132:135], v[24:27], a[228:231], v[132:135]\n"
    "v_mfma_f32_16x16x32_bf16 v[136:139], v[24:27], a[232:235], v[136:139]\n"
    "v_mfma_f32_16x16x32_bf16 v[140:143], v[24:27], a[236:239], v[140:143]\n"
    "s_waitcnt lgkmcnt(4)\n"
    "ds_read_b64_tr_b16 v[24:25], v57 offset:64\n"
    "ds_read_b64_tr_b16 v[26:27], v57 offset:3648\n"
    "v_mfma_f32_16x16x32_bf16 v[128:131], v[28:31], a[240:243], v[128:131]\n"
    "v_mfma_f32_16x16x32_bf16 v[132:135], v[28:31], a[244:247], v[132:135]\n"
    "v_mfma_f32_16x16x32_bf16 v[136:139], v[28:31], a[248:251], v[136:139]\n"
    "v_mfma_f32_16x16x32_bf16 v[140:143], v[28:31], a[252:255], v[140:143]\n"
    "s_waitcnt lgkmcnt(4)\n"
    "ds_read_b64_tr_b16 v[28:29], v58 offset:64\n"
    "ds_read_b64_tr_b16 v[30:31], v58 offset:3648\n"
    "v_mfma_f32_16x16x32_bf16 v[144:147], v[16:19], a[224:227], v[144:147]\n"
    "v_mfma_f32_16x16x32_bf16 v[148:151], v[16:19], a[228:231], v[148:151]\n"
    "v_cvt_pk_bf16_f32 v128, v128, v129\n"
    "v_cvt_pk_bf16_f32 v129, v130, v131\n"
    "v_cvt_pk_bf16_f32 v130, v132, v133\n"
    "v_cvt_pk_bf16_f32 v131, v134, v135\n"
    "s_cmp_eq_u32 s43, 0\ns_cbranch_scc1 L_own0\nglobal_store_dwordx4 v247, v[128:131], s[72:73] sc1\nL_ownr0:\n"
    "v_mfma_f32_16x16x32_bf16 v[152:155], v[16:19], a[232:235], v[152:155]\n"
    "v_cvt_pk_bf16_f32 v136, v136, v137\n"
    "v_cvt_pk_bf16_f32 v137, v138, v139\n"
    "v_cvt_pk_bf16_f32 v138, v140, v141\n"
    "v_cvt_pk_bf16_f32 v139, v142, v143\n"
    "s_cmp_eq_u32 s43, 0\ns_cbranch_scc1 L_own1\nglobal_store_dwordx4 v247, v[136:139], s[72:73] offset:1024 sc1\nL_ownr1:\n"
    "v_mfma_f32_16x16x32_bf16 v[156:159], v[16:19], a[236:239], v[156:159]\n"
    "s_waitcnt lgkmcnt(4)\n"
    "ds_read_b64_tr_b16 v[16:17], v57 offset:96\n"
    "ds_read_b64_tr_b16 v[18:19], v57 offset:3680\n"
    "v_mfma_f32_16x16x32_bf16 v[144:147], v[20:23], a[240:243], v[144:147]\n"
    "v_mfma_f32_16x16x32_bf16 v[148:151], v[20:23], a[244:247], v[148:151]\n"
    "v_mfma_f32_16x16x32_bf16 v[152:155], v[20:23], a[248:251], v[152:155]\n"
    "v_mfma_f32_16x16x32_bf16 v[156:159], v[20:23], a[252:255], v[156:159]\n"
    "s_waitcnt lgkmcnt(4)\n"
    "ds_read_b64_tr_b16 v[20:21], v58 offset:96\n"
    "ds_read_b64_tr_b16 v[22:23], v58 offset:3680\n"
    "v_mfma_f32_16x16x32_bf16 v[160:163], v[24:27], a[224:227], v[160:163]\n"
    "v_mfma_f32_16x16x32_bf16 v[164:167], v[24:27], a[228:231], v[164:167]\n"
    "v_cvt_pk_bf16_f32 v144, v144, v145\n"
    "v_cvt_pk_bf16_f32 v145, v146, v147\n"
    "v_cvt_pk_bf16_f32 v146, v148, v149\n"
    "v_cvt_pk_bf16_f32 v147, v150, v151\n"
    "s_cmp_eq_u32 s43, 0\ns_cbranch_scc1 L_own2\nglobal_store_dwordx4 v247, v[144:147], s[72:73] offset:2048 sc1\nL_ownr2:\n"
    "v_mfma_f32_16x16x32_bf16 v[168:171], v[24:27], a[232:235], v[168:171]\n"
    "v_cvt_pk_bf16_f32 v152, v152, v153\n"
    "v_cvt_pk_bf16_f32 v153, v154, v155\n"
    "v_cvt_pk_bf16_f32 v154, v156, v157\n"
    "v_cvt_pk_bf16_f32 v155, v158, v159\n"
    "s_cmp_eq_u32 s43, 0\ns_cbranch_scc1 L_own3\nglobal_store_dwordx4 v247, v[152:155], s[72:73] offset:3072 sc1\nL_ownr3:\n"
    "v_mfma_f32_16x16x32_bf16 v[172:175], v[24:27], a[236:239], v[172:175]\n"
    "s_waitcnt lgkmcnt(4)\n"
    "ds_read_b64_tr_b16 v[24:25], v57 offset:128\n"
    "ds_read_b64_tr_b16 v[26:27], v57 offset:3712\n"
    "s_add_u32 s72, s72, 0x1000\n"
    "s_addc_u32 s73, s73, 0\n"
    "v_mfma_f32_16x16x32_bf16 v[160:163], v[28:31], a[240:243], v[160:163]\n"
    "v_mfma_f32_16x16x32_bf16 v[164:167], v[28:31], a[244:247], v[164:167]\n"
    "v_mfma_f32_16x16x32_bf16 v[168:171], v[28:31], a[248:251], v[168:171]\n"
    "v_mfma_f32_16x16x32_bf16 v[172:175], v[28:31], a[252:255], v[172:175]\n"
    "s_waitcnt lgkmcnt(4)\n"
    "ds_read_b64_tr_b16 v[28:29], v58 offset:128\n"
    "ds_read_b64_tr_b16 v[30:31], v58 offset:3712\n"
    "v_mfma_f32_16x16x32_bf16 v[176:179], v[16:19], a[224:227], v[176:179]\n"
    "v_mfma_f32_16x16x32_bf16 v[180:183], v[16:19], a[228:231], v[180:183]\n"
    "v_cvt_pk_bf16_f32 v160, v160, v161\n"
    "v_cvt_pk_bf16_f32 v161, v162, v163\n"
    "v_cvt_pk_bf16_f32 v162, v164, v165\n"
    "v_cvt_pk_bf16_f32 v163, v166, v167\n"
    "s_cmp_eq_u32 s43, 0\ns_cbranch_scc1 L_own4\nglobal_store_dwordx4 v247, v[160:163], s[72:73] sc1\nL_ownr4:\n"
    "v_mfma_f32_16x16x32_bf16 v[184:187], v[16:19], a[232:235], v[184:187]\n"
    "v_cvt_pk_bf16_f32 v168, v168, v169\n"
    "v_cvt_pk_bf16_f32 v169, v170, v171\n"
    "v_cvt_pk_bf16_f32 v170, v172, v173\n"
    "v_cvt_pk_bf16_f32 v171, v174, v175\n"
    "s_cmp_eq_u32 s43, 0\ns_cbranch_scc1 L_own5\nglobal_store_dwordx4 v247, v[168:171], s[72:73] offset:1024 sc1\nL_ownr5:\n"
    "v_mfma_f32_16x16x32_bf16 v[188:191], v[16:19], a[236:239], v[188:191]\n"
    "s_waitcnt lgkmcnt(4)\n"
    "ds_read_b64_tr_b16 v[16:17], v57 offset:160\n"
    "ds_read_b64_tr_b16 v[18:19], v57 offset:3744\n"
    "v_mfma_f32_16x16x32_bf16 v[176:179], v[20:23], a[240:243], v[176:179]\n"
    "v_mfma_f32_16x16x32_bf16 v[180:183], v[20:23], a[244:247], v[180:183]\n"
    "v_mfma_f32_16x16x32_bf16 v[184:187], v[20:23], a[248:251], v[184:187]\n"
    "v_mfma_f32_16x16x32_bf16 v[188:191], v[20:23], a[252:255], v[188:191]\n"
    "s_waitcnt lgkmcnt(4)\n"
    "ds_read_b64_tr_b16 v[20:21], v58 offset:160\n"
    "ds_read_b64_tr_b16 v[22:23], v58 offset:3744\n"
    "v_mfma_f32_16x16x32_bf16 v[192:195], v[24:27], a[224:227], v[192:195]\n"
    "v_mfma_f32_16x16x32_bf16 v[196:199], v[24:27], a[228:231], v[196:199]\n"
    "v_cvt_pk_bf16_f32 v176, v176, v177\n"
    "v_cvt_pk_bf16_f32 v177, v178, v179\n"
    "v_cvt_pk_bf16_f32 v178, v180, v181\n"
    "v_cvt_pk_bf16_f32 v179, v182, v183\n"
    "s_cmp_eq_u32 s43, 0\ns_cbranch_scc1 L_own6\nglobal_store_dwordx4 v247, v[176:179], s[72:73] offset:2048 sc1\nL_ownr6:\n"
    "v_mfma_f32_16x16x32_bf16 v[200:203], v[24:27], a[232:235], v[200:203]\n"
    "v_cvt_pk_bf16_f32 v184, v184, v185\n"
    "v_cvt_pk_bf16_f32 v185, v186, v187\n"
    "v_cvt_pk_bf16_f32 v186, v188, v189\n"
    "v_cvt_pk_bf16_f32 v187, v190, v191\n"
    "s_cmp_eq_u32 s43, 1\ns_cbranch_scc1 L_own7\nglobal_store_dwordx4 v247, v[184:187], s[72:73] offset:3072 sc1\nL_ownr7:\n"
    "v_mfma_f32_16x16x32_bf16 v[204:207], v[24:27], a[236:239], v[204:207]\n"
    "s_waitcnt lgkmcnt(4)\n"
    "ds_read_b64_tr_b16 v[24:25], v57 offset:192\n"
    "ds_read_b64_tr_b16 v[26:27], v57 offset:3776\n"
    "s_add_u32 s72, s72, 0x1000\n"
    "s_addc_u32 s73, s73, 0\n"
    "v_mfma_f32_16x16x32_bf16 v[192:195], v[28:31], a[240:243], v[192:195]\n"
    "v_mfma_f32_16x16x32_bf16 v[196:199], v[28:31], a[244:247], v[196:199]\n"
    "v_mfma_f32_16x16x32_bf16 v[200:203], v[28:31], a[248:251], v[200:203]\n"
    "v_mfma_f32_16x16x32_bf16 v[204:207], v[28:31], a[252:255], v[204:207]\n"
    "s_waitcnt lgkmcnt(4)\n"
    "ds_read_b64_tr_b16 v[28:29], v58 offset:192\n"
    "ds_read_b64_tr_b16 v[30:31], v58 offset:3776\n"
    "v_mfma_f32_16x16x32_bf16 v[208:211], v[16:19], a[224:227], v[208:211]\n"
    "v_mfma_f32_16x16x32_bf16 v[212:215], v[16:19], a[228:231], v[212:215]\n"
    "v_cvt_pk_bf16_f32 v192, v192, v193\n"
    "v_cvt_pk_bf16_f32 v193, v194, v195\n"
    "v_cvt_pk_bf16_f32 v194, v196, v197\n"
    "v_cvt_pk_bf16_f32 v195, v198, v199\n"
    "s_cmp_eq_u32 s43, 1\ns_cbranch_scc1 L_own8\nglobal_store_dwordx4 v247, v[192:195], s[72:73] sc1\nL_ownr8:\n"
    "v_mfma_f32_16x16x32_bf16 v[216:219], v[16:19], a[232:235], v[216:219]\n"
    "v_cvt_pk_bf16_f32 v200, v200, v201\n"
    "v_cvt_pk_bf16_f32 v201, v202, v203\n"
    "v_cvt_pk_bf16_f32 v202, v204, v205\n"
    "v_cvt_pk_bf16_f32 v203, v206, v207\n"
    "s_cmp_eq_u32 s43, 1\ns_cbranch_scc1 L_own9\nglobal_store_dwordx4 v247, v[200:203], s[72:73] offset:1024 sc1\nL_ownr9:\n"
    "v_mfma_f32_16x16x32_bf16 v[220:223], v[16:19], a[236:239], v[220:223]\n"
    "s_waitcnt lgkmcnt(4)\n"
    "v_mfma_f32_16x16x32_bf16 v[208:211], v[20:23], a[240:243], v[208:211]\n"
    "v_mfma_f32_16x16x32_bf16 v[212:215], v[20:23], a[244:247], v[212:215]\n"
    "v_mfma_f32_16x16x32_bf16 v[216:219], v[20:23], a[248:251], v[216:219]\n"
    "v_mfma_f32_16x16x32_bf16 v[220:223], v[20:23], a[252:255], v[220:223]\n"
    "s_waitcnt lgkmcnt(2)\n"
    "v_mfma_f32_16x16x32_bf16 v[224:227], v[24:27], a[224:227], v[224:227]\n"
    "v_mfma_f32_16x16x32_bf16 v[228:231], v[24:27], a[228:231], v[228:231]\n"
    "s_nop 1\n"
    "v_cvt_pk_bf16_f32 v208, v208, v209\n"
    "v_cvt_pk_bf16_f32 v209, v210, v211\n"
    "v_cvt_pk_bf16_f32 v210, v212, v213\n"
    "v_cvt_pk_bf16_f32 v211, v214, v215\n"
    "s_cmp_eq_u32 s43, 1\ns_cbranch_scc1 L_own10\nglobal_store_dwordx4 v247, v[208:211], s[72:73] offset:2048 sc1\nL_ownr10:\n"
    "v_mfma_f32_16x16x32_bf16 v[232:235], v[24:27], a[232:235], v[232:235]\n"
    "v_cvt_pk_bf16_f32 v216, v216, v217\n"
    "v_cvt_pk_bf16_f32 v217, v218, v219\n"
    "v_cvt_pk_bf16_f32 v218, v220, v221\n"
    "v_cvt_pk_bf16_f32 v219, v222, v223\n"
    "s_cmp_eq_u32 s43, 1\ns_cbranch_scc1 L_own11\nglobal_store_dwordx4 v247, v[216:219], s[72:73] offset:3072 sc1\nL_ownr11:\n"
    "v_mfma_f32_16x16x32_bf16 v[236:239], v[24:27], a[236:239], v[236:239]\n"
    "s_waitcnt lgkmcnt(0)\n"
    "s_add_u32 s72, s72, 0x1000\n"
    "s_addc_u32 s73, s73, 0\n"
    "v_mfma_f32_16x16x32_bf16 v[224:227], v[28:31], a[240:243], v[224:227]\n"
    "v_mfma_f32_16x16x32_bf16 v[228:231], v[28:31], a[244:247], v[228:231]\n"
    "v_mfma_f32_16x16x32_bf16 v[232:235], v[28:31], a[248:251], v[232:235]\n"
    "v_mfma_f32_16x16x32_bf16 v[236:239], v[28:31], a[252:255], v[236:239]\n"
    "s_nop 4\n"
    "v_cvt_pk_bf16_f32 v224, v224, v225\n"
    "v_cvt_pk_bf16_f32 v225, v226, v227\n"
    "v_cvt_pk_bf16_f32 v226, v228, v229\n"
    "v_cvt_pk_bf16_f32 v227, v230, v231\n"
    "s_cmp_eq_u32 s43, 1\ns_cbranch_scc1 L_own12\nglobal_store_dwordx4 v247, v[224:227], s[72:73] sc1\nL_ownr12:\n"
    "v_cvt_pk_bf16_f32 v232, v232, v233\n"
    "v_cvt_pk_bf16_f32 v233, v234, v235\n"
    "v_cvt_pk_bf16_f32 v234, v236, v237\n"
    "v_cvt_pk_bf16_f32 v235, v238, v239\n"
    "s_cmp_eq_u32 s43, 1\ns_cbranch_scc1 L_own13\nglobal_store_dwordx4 v247, v[232:235], s[72:73] offset:1024 sc1\nL_ownr13:\n"
    "v_lshrrev_b32 v48, 4, v240\n"
    "v_bfe_u32 v49, v240, 2, 2\n"
    "v_cmp_eq_u32 vcc, v48, v49\n"
    "v_and_b32 v48, 1, v240\n"
    "v_lshlrev_b32 v48, 4, v48\n"
    "v_lshlrev_b32_e64 v50, v48, s76\n"
    "v_cndmask_b32 v50, 0, v50\n"
    "v_and_b32 v48, 2, v240\n"
    "v_cmp_eq_u32 vcc, 0, v48\n"
    "s_nop 1\n"
    "v_cndmask_b32 v114, 0, v50\n"
    "v_sub_u32 v115, v50, v114\n"
    "v_mov_b32 v112, 0\n"
    "v_mov_b32 v113, 0\n"
    "v_mov_b32 v116, 0\n"
    "v_mov_b32 v117, 0\n"
    "s_waitcnt lgkmcnt(0)\n"
    "s_barrier\n"
    "ds_read_b128 v[128:131], v45\n"
    "ds_read_b128 v[132:135], v46\n"
    "s_xor_b32 s45, 1, s13\n"
    "s_mul_i32 s45, s45, 0x1c0000\n"
    "v_add_u32 v80, s45, v45\n"
    "v_add_u32 v81, s45, v46\n"
    "s_xor_b32 s46, 2, s13\n"
    "s_mul_i32 s46, s46, 0x1c0000\n"
    "v_add_u32 v82, s46, v45\n"
    "v_add_u32 v83, s46, v46\n"
    "s_xor_b32 s44, 3, s13\n"
    "s_mul_i32 s44, s44, 0x1c0000\n"
    "v_add_u32 v84, s44, v45\n"
    "v_add_u32 v85, s44, v46\n"
    "s_xor_b32 s45, 4, s13\n"
    "s_mul_i32 s45, s45, 0x1c0000\n"
    "v_add_u32 v86, s45, v45\n"
    "v_add_u32 v87, s45, v46\n"
    "s_xor_b32 s46, 5, s13\n"
    "s_mul_i32 s46, s46, 0x1c0000\n"
    "v_add_u32 v88, s46, v45\n"
    "v_add_u32 v89, s46, v46\n"
    "s_xor_b32 s44, 6, s13\n"
    "s_mul_i32 s44, s44, 0x1c0000\n"
    "v_add_u32 v90, s44, v45\n"
    "v_add_u32 v91, s44, v46\n"
    "s_xor_b32 s45, 7, s13\n"
    "s_mul_i32 s45, s45, 0x1c0000\n"
    "v_add_u32 v92, s45, v45\n"
    "v_add_u32 v93, s45, v46\n"
    "s_mov_b64 s[30:31], s[34:35]\n"
    "s_cmp_lt_u32 s13, 4\n"
    "s_cbranch_scc1 L_oe1\n"
    "global_load_dwordx4 v[160:163], v86, s[30:31] sc1\n"
    "global_load_dwordx4 v[164:167], v87, s[30:31] sc1\n"
    "global_load_dwordx4 v[168:171], v88, s[30:31] sc1\n"
    "global_load_dwordx4 v[172:175], v89, s[30:31] sc1\n"
    "global_load_dwordx4 v[176:179], v90, s[30:31] sc1\n"
    "global_load_dwordx4 v[180:183], v91, s[30:31] sc1\n"
    "global_load_dwordx4 v[184:187], v92, s[30:31] sc1\n"
    "global_load_dwordx4 v[188:191], v93, s[30:31] sc1\n"
    "L_oe1:\n"
    "s_or_b32 s46, s10, s11\n"
    "s_cmp_lg_u32 s46, 0\n"
    "s_cbranch_scc1 L_ep_pd\n"
    "s_add_u32 s28, s68, 50335744\n"
    "s_addc_u32 s29, s69, 0\n"
    "v_lshrrev_b32 v76, 1, v240\n"
    "v_lshlrev_b32 v76, 8, v76\n"
    "v_and_b32 v77, 1, v240\n"
    "v_lshl_add_u32 v76, v77, 5, v76\n"
    "global_load_dwordx4 v[68:71], v76, s[28:29] sc1\n"
    "global_load_dwordx4 v[72:75], v76, s[28:29] offset:16 sc1\n"
    "L_ep_pd:\n"
    "v_mfma_f32_16x16x32_bf16 v[96:99], v[114:117], a[0:3], 0\n"
    "v_mfma_f32_16x16x32_bf16 v[100:103], v[112:115], a[0:3], 0\n"
    "v_mfma_f32_16x16x32_bf16 v[104:107], v[114:117], a[4:7], 0\n"
    "v_mfma_f32_16x16x32_bf16 v[108:111], v[112:115], a[4:7], 0\n"
    "v_mfma_f32_16x16x32_bf16 v[96:99], v[114:117], a[8:11], v[96:99]\n"
    "v_mfma_f32_16x16x32_bf16 v[100:103], v[112:115], a[8:11], v[100:103]\n"
    "v_mfma_f32_16x16x32_bf16 v[104:107], v[114:117], a[12:15], v[104:107]\n"
    "v_mfma_f32_16x16x32_bf16 v[108:111], v[112:115], a[12:15], v[108:111]\n"
    "v_mfma_f32_16x16x32_bf16 v[96:99], v[114:117], a[16:19], v[96:99]\n"
    "v_mfma_f32_16x16x32_bf16 v[100:103], v[112:115], a[16:19], v[100:103]\n"
    "v_mfma_f32_16x16x32_bf16 v[104:107], v[114:117], a[20:23], v[104:107]\n"
    "v_mfma_f32_16x16x32_bf16 v[108:111], v[112:115], a[20:23], v[108:111]\n"
    "v_mfma_f32_16x16x32_bf16 v[96:99], v[114:117], a[24:27], v[96:99]\n"
    "v_mfma_f32_16x16x32_bf16 v[100:103], v[112:115], a[24:27], v[100:103]\n"
    "v_mfma_f32_16x16x32_bf16 v[104:107], v[114:117], a[28:31], v[104:107]\n"
    "v_mfma_f32_16x16x32_bf16 v[108:111], v[112:115], a[28:31], v[108:111]\n"
    "v_mfma_f32_16x16x32_bf16 v[96:99], v[114:117], a[128:131], v[96:99]\n"
    "v_mfma_f32_16x16x32_bf16 v[100:103], v[112:115], a[128:131], v[100:103]\n"
    "v_mfma_f32_16x16x32_bf16 v[104:107], v[114:117], a[132:135], v[104:107]\n"
    "v_mfma_f32_16x16x32_bf16 v[108:111], v[112:115], a[132:135], v[108:111]\n"
    "v_mfma_f32_16x16x32_bf16 v[96:99], v[114:117], a[136:139], v[96:99]\n"
    "v_mfma_f32_16x16x32_bf16 v[100:103], v[112:115], a[136:139], v[100:103]\n"
    "v_mfma_f32_16x16x32_bf16 v[104:107], v[114:117], a[140:143], v[104:107]\n"
    "v_mfma_f32_16x16x32_bf16 v[108:111], v[112:115], a[140:143], v[108:111]\n"
    "v_mfma_f32_16x16x32_bf16 v[96:99], v[114:117], a[144:147], v[96:99]\n"
    "v_mfma_f32_16x16x32_bf16 v[100:103], v[112:115], a[144:147], v[100:103]\n"
    "v_mfma_f32_16x16x32_bf16 v[104:107], v[114:117], a[148:151], v[104:107]\n"
    "v_mfma_f32_16x16x32_bf16 v[108:111], v[112:115], a[148:151], v[108:111]\n"
    "v_mfma_f32_16x16x32_bf16 v[96:99], v[114:117], a[152:155], v[96:99]\n"
    "v_mfma_f32_16x16x32_bf16 v[100:103], v[112:115], a[152:155], v[100:103]\n"
    "v_mfma_f32_16x16x32_bf16 v[104:107], v[114:117], a[156:159], v[104:107]\n"
    "v_mfma_f32_16x16x32_bf16 v[108:111], v[112:115], a[156:159], v[108:111]\n"
    "v_lshlrev_b32 v56, 16, v40\n"
    "v_and_b32 v57, 0xffff0000, v40\n"
    "v_lshlrev_b32 v58, 16, v41\n"
    "v_and_b32 v63, 0xffff0000, v41\n"
    "s_nop 0\n"
    "v_add_f32 v96, v96, v56\n"
    "v_add_f32 v97, v97, v57\n"
    "v_add_f32 v98, v98, v58\n"
    "v_add_f32 v99, v99, v63\n"
    "v_add_f32 v100, v100, v56\n"
    "v_add_f32 v101, v101, v57\n"
    "v_add_f32 v102, v102, v58\n"
    "v_add_f32 v103, v103, v63\n"
    "v_mul_f32 v48, 0xbfb8aa3b, v96\n"
    "v_mul_f32 v49, 0xbfb8aa3b, v97\n"
    "v_mul_f32 v50, 0xbfb8aa3b, v98\n"
    "v_mul_f32 v51, 0xbfb8aa3b, v99\n"
    "v_mul_f32 v52, 0xbfb8aa3b, v100\n"
    "v_mul_f32 v53, 0xbfb8aa3b, v101\n"
    "v_mul_f32 v54, 0xbfb8aa3b, v102\n"
    "v_mul_f32 v55, 0xbfb8aa3b, v103\n"
    "v_exp_f32 v48, v48\n"
    "v_exp_f32 v49, v49\n"
    "v_exp_f32 v50, v50\n"
    "v_exp_f32 v51, v51\n"
    "v_exp_f32 v52, v52\n"
    "v_exp_f32 v53, v53\n"
    "v_exp_f32 v54, v54\n"
    "v_exp_f32 v55, v55\n"
    "v_add_f32 v48, 1.0, v48\n"
    "v_add_f32 v49, 1.0, v49\n"
    "v_add_f32 v50, 1.0, v50\n"
    "v_add_f32 v51, 1.0, v51\n"
    "v_add_f32 v52, 1.0, v52\n"
    "v_add_f32 v53, 1.0, v53\n"
    "v_add_f32 v54, 1.0, v54\n"
    "v_add_f32 v55, 1.0, v55\n"
    "v_rcp_f32 v48, v48\n"
    "v_rcp_f32 v49, v49\n"
    "v_rcp_f32 v50, v50\n"
    "v_rcp_f32 v51, v51\n"
    "v_rcp_f32 v52, v52\n"
    "v_rcp_f32 v53, v53\n"
    "v_rcp_f32 v54, v54\n"
    "v_rcp_f32 v55, v55\n"
    "v_mul_f32 v96, v96, v48\n"
    "v_mul_f32 v97, v97, v49\n"
    "v_mul_f32 v98, v98, v50\n"
    "v_mul_f32 v99, v99, v51\n"
    "v_mul_f32 v100, v100, v52\n"
    "v_mul_f32 v101, v101, v53\n"
    "v_mul_f32 v102, v102, v54\n"
    "v_mul_f32 v103, v103, v55\n"
    "v_lshlrev_b32 v56, 16, v42\n"
    "v_and_b32 v57, 0xffff0000, v42\n"
    "v_lshlrev_b32 v58, 16, v43\n"
    "v_and_b32 v63, 0xffff0000, v43\n"
    "v_add_f32 v104, v104, v56\n"
    "v_add_f32 v105, v105, v57\n"
    "v_add_f32 v106, v106, v58\n"
    "v_add_f32 v107, v107, v63\n"
    "v_add_f32 v108, v108, v56\n"
    "v_add_f32 v109, v109, v57\n"
    "v_add_f32 v110, v110, v58\n"
    "v_add_f32 v111, v111, v63\n"
    "v_mul_f32 v48, 0xbfb8aa3b, v104\n"
    "v_mul_f32 v49, 0xbfb8aa3b, v105\n"
    "v_mul_f32 v50, 0xbfb8aa3b, v106\n"
    "v_mul_f32 v51, 0xbfb8aa3b, v107\n"
    "v_mul_f32 v52, 0xbfb8aa3b, v108\n"
    "v_mul_f32 v53, 0xbfb8aa3b, v109\n"
    "v_mul_f32 v54, 0xbfb8aa3b, v110\n"
    "v_mul_f32 v55, 0xbfb8aa3b, v111\n"
    "v_exp_f32 v48, v48\n"
    "v_exp_f32 v49, v49\n"
    "v_exp_f32 v50, v50\n"
    "v_exp_f32 v51, v51\n"
    "v_exp_f32 v52, v52\n"
    "v_exp_f32 v53, v53\n"
    "v_exp_f32 v54, v54\n"
    "v_exp_f32 v55, v55\n"
    "v_add_f32 v48, 1.0, v48\n"
    "v_add_f32 v49, 1.0, v49\n"
    "v_add_f32 v50, 1.0, v50\n"
    "v_add_f32 v51, 1.0, v51\n"
    "v_add_f32 v52, 1.0, v52\n"
    "v_add_f32 v53, 1.0, v53\n"
    "v_add_f32 v54, 1.0, v54\n"
    "v_add_f32 v55, 1.0, v55\n"
    "v_rcp_f32 v48, v48\n"
    "v_rcp_f32 v49, v49\n"
    "v_rcp_f32 v50, v50\n"
    "v_rcp_f32 v51, v51\n"
    "v_rcp_f32 v52, v52\n"
    "v_rcp_f32 v53, v53\n"
    "v_rcp_f32 v54, v54\n"
    "v_rcp_f32 v55, v55\n"
    "v_mul_f32 v104, v104, v48\n"
    "v_mul_f32 v105, v105, v49\n"
    "v_mul_f32 v106, v106, v50\n"
    "v_mul_f32 v107, v107, v51\n"
    "v_mul_f32 v108, v108, v52\n"
    "v_mul_f32 v109, v109, v53\n"
    "v_mul_f32 v110, v110, v54\n"
    "v_mul_f32 v111, v111, v55\n"
    "s_mov_b64 s[30:31], s[34:35]\n"
    "s_waitcnt vmcnt(0) lgkmcnt(0)\n"
    "global_load_dwordx4 v[136:139], v80, s[30:31] sc1\n"
    "global_load_dwordx4 v[140:143], v81, s[30:31] sc1\n"
    "global_load_dwordx4 v[144:147], v82, s[30:31] sc1\n"
    "global_load_dwordx4 v[148:151], v83, s[30:31] sc1\n"
    "global_load_dwordx4 v[152:155], v84, s[30:31] sc1\n"
    "global_load_dwordx4 v[156:159], v85, s[30:31] sc1\n"
    "s_cmp_ge_u32 s13, 4\n"
    "s_cbranch_scc1 L_oe2\n"
    "global_load_dwordx4 v[160:163], v86, s[30:31] sc1\n"
    "global_load_dwordx4 v[164:167], v87, s[30:31] sc1\n"
    "global_load_dwordx4 v[168:171], v88, s[30:31] sc1\n"
    "global_load_dwordx4 v[172:175], v89, s[30:31] sc1\n"
    "global_load_dwordx4 v[176:179], v90, s[30:31] sc1\n"
    "global_load_dwordx4 v[180:183], v91, s[30:31] sc1\n"
    "global_load_dwordx4 v[184:187], v92, s[30:31] sc1\n"
    "global_load_dwordx4 v[188:191], v93, s[30:31] sc1\n"
    "L_oe2:\n"
    "v_mfma_f32_16x16x32_bf16 v[16:19], v[114:117], v[128:131], 0\n"
    "v_mfma_f32_16x16x32_bf16 v[20:23], v[112:115], v[128:131], 0\n"
    "v_mfma_f32_16x16x32_bf16 v[24:27], v[114:117], v[132:135], 0\n"
    "v_mfma_f32_16x16x32_bf16 v[28:31], v[112:115], v[132:135], 0\n"
    "s_waitcnt vmcnt(5)\n"
    "v_mfma_f32_16x16x32_bf16 v[16:19], v[114:117], v[136:139], v[16:19]\n"
    "v_mfma_f32_16x16x32_bf16 v[20:23], v[112:115], v[136:139], v[20:23]\n"
    "s_waitcnt vmcnt(4)\n"
    "v_mfma_f32_16x16x32_bf16 v[24:27], v[114:117], v[140:143], v[24:27]\n"
    "v_mfma_f32_16x16x32_bf16 v[28:31], v[112:115], v[140:143], v[28:31]\n"
    "s_waitcnt vmcnt(3)\n"
    "v_mfma_f32_16x16x32_bf16 v[16:19], v[114:117], v[144:147], v[16:19]\n"
    "v_mfma_f32_16x16x32_bf16 v[20:23], v[112:115], v[144:147], v[20:23]\n"
    "s_waitcnt vmcnt(2)\n"
    "v_mfma_f32_16x16x32_bf16 v[24:27], v[114:117], v[148:151], v[24:27]\n"
    "v_mfma_f32_16x16x32_bf16 v[28:31], v[112:115], v[148:151], v[28:31]\n"
    "s_waitcnt vmcnt(1)\n"
    "v_mfma_f32_16x16x32_bf16 v[16:19], v[114:117], v[152:155], v[16:19]\n"
    "v_mfma_f32_16x16x32_bf16 v[20:23], v[112:115], v[152:155], v[20:23]\n"
    "s_waitcnt vmcnt(0)\n"
    "v_mfma_f32_16x16x32_bf16 v[24:27], v[114:117], v[156:159], v[24:27]\n"
    "v_mfma_f32_16x16x32_bf16 v[28:31], v[112:115], v[156:159], v[28:31]\n"
    "s_waitcnt vmcnt(0)\n"
    "v_mfma_f32_16x16x32_bf16 v[16:19], v[114:117], v[160:163], v[16:19]\n"
    "v_mfma_f32_16x16x32_bf16 v[20:23], v[112:115], v[160:163], v[20:23]\n"
    "v_mfma_f32_16x16x32_bf16 v[24:27], v[114:117], v[164:167], v[24:27]\n"
    "v_mfma_f32_16x16x32_bf16 v[28:31], v[112:115], v[164:167], v[28:31]\n"
    "v_mfma_f32_16x16x32_bf16 v[16:19], v[114:117], v[168:171], v[16:19]\n"
    "v_mfma_f32_16x16x32_bf16 v[20:23], v[112:115], v[168:171], v[20:23]\n"
    "v_mfma_f32_16x16x32_bf16 v[24:27], v[114:117], v[172:175], v[24:27]\n"
    "v_mfma_f32_16x16x32_bf16 v[28:31], v[112:115], v[172:175], v[28:31]\n"
    "v_mfma_f32_16x16x32_bf16 v[16:19], v[114:117], v[176:179], v[16:19]\n"
    "v_mfma_f32_16x16x32_bf16 v[20:23], v[112:115], v[176:179], v[20:23]\n"
    "v_mfma_f32_16x16x32_bf16 v[24:27], v[114:117], v[180:183], v[24:27]\n"
    "v_mfma_f32_16x16x32_bf16 v[28:31], v[112:115], v[180:183], v[28:31]\n"
    "v_mfma_f32_16x16x32_bf16 v[16:19], v[114:117], v[184:187], v[16:19]\n"
    "v_mfma_f32_16x16x32_bf16 v[20:23], v[112:115], v[184:187], v[20:23]\n"
    "v_mfma_f32_16x16x32_bf16 v[24:27], v[114:117], v[188:191], v[24:27]\n"
    "v_mfma_f32_16x16x32_bf16 v[28:31], v[112:115], v[188:191], v[28:31]\n"
    "s_nop 6\n"
    "v_cmp_u_f32 vcc, v16, v24\n"
    "s_cbranch_vccnz L_sw_slow\n"
    "L_sw_ok:\n"
    "v_lshlrev_b32 v48, 16, v32\n"
    "v_and_b32 v49, 0xffff0000, v32\n"
    "v_lshlrev_b32 v50, 16, v33\n"
    "v_and_b32 v51, 0xffff0000, v33\n"
    "v_fma_f32 v48, v16, v96, v48\n"
    "v_fma_f32 v49, v17, v97, v49\n"
    "v_fma_f32 v50, v18, v98, v50\n"
    "v_fma_f32 v51, v19, v99, v51\n"
    "v_cvt_pk_bf16_f32 v32, v48, v49\n"
    "v_cvt_pk_bf16_f32 v33, v50, v51\n"
    "global_store_dwordx2 v47, v[32:33], s[66:67]\n"
    "v_lshlrev_b32 v52, 16, v34\n"
    "v_and_b32 v53, 0xffff0000, v34\n"
    "v_lshlrev_b32 v54, 16, v35\n"
    "v_and_b32 v55, 0xffff0000, v35\n"
    "v_fma_f32 v52, v20, v100, v52\n"
    "v_fma_f32 v53, v21, v101, v53\n"
    "v_fma_f32 v54, v22, v102, v54\n"
    "v_fma_f32 v55, v23, v103, v55\n"
    "v_cvt_pk_bf16_f32 v34, v52, v53\n"
    "v_cvt_pk_bf16_f32 v35, v54, v55\n"
    "v_add_u32 v47, 0x3800, v47\n"
    "global_store_dwordx2 v47, v[34:35], s[66:67]\n"
    "v_lshlrev_b32 v48, 16, v36\n"
    "v_and_b32 v49, 0xffff0000, v36\n"
    "v_lshlrev_b32 v50, 16, v37\n"
    "v_and_b32 v51, 0xffff0000, v37\n"
    "v_fma_f32 v48, v24, v104, v48\n"
    "v_fma_f32 v49, v25, v105, v49\n"
    "v_fma_f32 v50, v26, v106, v50\n"
    "v_fma_f32 v51, v27, v107, v51\n"
    "v_cvt_pk_bf16_f32 v36, v48, v49\n"
    "v_cvt_pk_bf16_f32 v37, v50, v51\n"
    "global_store_dwordx2 v44, v[36:37], s[66:67]\n"
    "v_lshlrev_b32 v52, 16, v38\n"
    "v_and_b32 v53, 0xffff0000, v38\n"
    "v_lshlrev_b32 v54, 16, v39\n"
    "v_and_b32 v55, 0xffff0000, v39\n"
    "v_fma_f32 v52, v28, v108, v52\n"
    "v_fma_f32 v53, v29, v109, v53\n"
    "v_fma_f32 v54, v30, v110, v54\n"
    "v_fma_f32 v55, v31, v111, v55\n"
    "v_cvt_pk_bf16_f32 v38, v52, v53\n"
    "v_cvt_pk_bf16_f32 v39, v54, v55\n"
    "v_add_u32 v44, 0x3800, v44\n"
    "global_store_dwordx2 v44, v[38:39], s[66:67]\n"
    "s_or_b32 s46, s10, s11\n"
    "s_cmp_lg_u32 s46, 0\n"
    "s_cbranch_scc1 L_ep_done\n"
    "s_add_u32 s28, s68, 50335744\n"
    "s_addc_u32 s29, s69, 0\n"
    "s_add_u32 s30, s68, 50331648\n"
    "s_addc_u32 s31, s69, 0\n"
    "v_lshrrev_b32 v64, 1, v240\n"
    "v_lshlrev_b32 v64, 8, v64\n"
    "v_and_b32 v65, 1, v240\n"
    "v_lshl_add_u32 v64, v65, 5, v64\n"
    "s_mov_b32 s42, 200000\n"
    "s_branch L_ep_cmp\n"
    "L_ep_lp:\n"
    "global_load_dwordx4 v[68:71], v64, s[28:29] sc1\n"
    "global_load_dwordx4 v[72:75], v64, s[28:29] offset:16 sc1\n"
    "s_waitcnt vmcnt(0)\n"
    "L_ep_cmp:\n"
    "v_cmp_eq_u32 vcc, s100, v68\n"
    "v_cmp_eq_u32_e64 s[44:45], s101, v69\n"
    "s_and_b64 vcc, vcc, s[44:45]\n"
    "s_mov_b64 s[46:47], vcc\n"
    "v_cmp_eq_u32 vcc, s100, v70\n"
    "v_cmp_eq_u32_e64 s[44:45], s101, v71\n"
    "s_and_b64 vcc, vcc, s[44:45]\n"
    "s_and_b64 s[46:47], s[46:47], vcc\n"
    "v_cmp_eq_u32 vcc, s100, v72\n"
    "v_cmp_eq_u32_e64 s[44:45], s101, v73\n"
    "s_and_b64 vcc, vcc, s[44:45]\n"
    "s_and_b64 s[46:47], s[46:47], vcc\n"
    "v_cmp_eq_u32 vcc, s100, v74\n"
    "v_cmp_eq_u32_e64 s[44:45], s101, v75\n"
    "s_and_b64 vcc, vcc, s[44:45]\n"
    "s_and_b64 s[46:47], s[46:47], vcc\n"
    "s_cmp_eq_u64 s[46:47], -1\n"
    "s_cbranch_scc1 L_ep_ok\n"
    "s_sub_u32 s42, s42, 1\n"
    "s_cmp_lg_u32 s42, 0\n"
    "s_cbranch_scc1 L_ep_lp\n"
    "L_ep_ok:\n"
    "v_mov_b32 v68, s100\n"
    "v_mov_b32 v64, 0\n"
    "s_mov_b64 exec, 1\n"
    "s_nop 3\n"
    "global_store_dword v64, v68, s[30:31] sc1\n"
    "s_nop 1\n"
    "s_mov_b64 exec, -1\n"
    "L_ep_done:\n"
    "s_waitcnt vmcnt(0)\n"
    "s_branch L_end\n"
    "L_mp_slow:\n"
    "s_mov_b32 s42, 200000\n"
    "L_mp_lp:\n"
    "global_load_dwordx2 v[252:253], v7, s[28:29] sc1\n"
    "s_waitcnt vmcnt(0)\n"
    "v_cmp_eq_u32 vcc, s100, v252\n"
    "v_cmp_eq_u32_e64 s[44:45], s101, v253\n"
    "s_and_b64 vcc, vcc, s[44:45]\n"
    "s_cmp_eq_u64 vcc, -1\n"
    "s_cbranch_scc1 L_mp_ok\n"
    "s_sub_u32 s42, s42, 1\n"
    "s_cmp_lg_u32 s42, 0\n"
    "s_cbranch_scc1 L_mp_lp\n"
    "s_branch L_mp_ok\n"
    "L_sw_slow:\n"
    "s_mov_b32 s42, 20000\n"
    "s_mov_b32 s44, 0xff\n"
    "L_sw_lp:\n"
    "s_mov_b32 s45, 0\n"
    "s_bitcmp1_b32 s44, 1\n"
    "s_cbranch_scc0 L_sw_s1\n"
    "v_max3_u32 v64, v136, v137, v138\n"
    "v_max3_u32 v65, v139, v140, v141\n"
    "v_max3_u32 v64, v64, v142, v143\n"
    "v_max_u32 v64, v64, v65\n"
    "v_cmp_eq_u32 vcc, -1, v64\n"
    "s_add_u32 s34, s30, 0x1c0000\n"
    "s_addc_u32 s35, s31, 0\n"
    "s_nop 2\n"
    "s_cbranch_vccz L_sw_s1\n"
    "global_load_dwordx4 v[136:139], v80, s[30:31] sc1\n"
    "global_load_dwordx4 v[140:143], v81, s[30:31] sc1\n"
    "s_bitset1_b32 s45, 1\n"
    "L_sw_s1:\n"
    "s_bitcmp1_b32 s44, 2\n"
    "s_cbranch_scc0 L_sw_s2\n"
    "v_max3_u32 v64, v144, v145, v146\n"
    "v_max3_u32 v65, v147, v148, v149\n"
    "v_max3_u32 v64, v64, v150, v151\n"
    "v_max_u32 v64, v64, v65\n"
    "v_cmp_eq_u32 vcc, -1, v64\n"
    "s_add_u32 s34, s30, 0x380000\n"
    "s_addc_u32 s35, s31, 0\n"
    "s_nop 2\n"
    "s_cbranch_vccz L_sw_s2\n"
    "global_load_dwordx4 v[144:147], v82, s[30:31] sc1\n"
    "global_load_dwordx4 v[148:151], v83, s[30:31] sc1\n"
    "s_bitset1_b32 s45, 2\n"
    "L_sw_s2:\n"
    "s_bitcmp1_b32 s44, 3\n"
    "s_cbranch_scc0 L_sw_s3\n"
    "v_max3_u32 v64, v152, v153, v154\n"
    "v_max3_u32 v65, v155, v156, v157\n"
    "v_max3_u32 v64, v64, v158, v159\n"
    "v_max_u32 v64, v64, v65\n"
    "v_cmp_eq_u32 vcc, -1, v64\n"
    "s_add_u32 s34, s30, 0x540000\n"
    "s_addc_u32 s35, s31, 0\n"
    "s_nop 2\n"
    "s_cbranch_vccz L_sw_s3\n"
    "global_load_dwordx4 v[152:155], v84, s[30:31] sc1\n"
    "global_load_dwordx4 v[156:159], v85, s[30:31] sc1\n"
    "s_bitset1_b32 s45, 3\n"
    "L_sw_s3:\n"
    "s_bitcmp1_b32 s44, 4\n"
    "s_cbranch_scc0 L_sw_s4\n"
    "v_max3_u32 v64, v160, v161, v162\n"
    "v_max3_u32 v65, v163, v164, v165\n"
    "v_max3_u32 v64, v64, v166, v167\n"
    "v_max_u32 v64, v64, v65\n"
    "v_cmp_eq_u32 vcc, -1, v64\n"
    "s_add_u32 s34, s30, 0x700000\n"
    "s_addc_u32 s35, s31, 0\n"
    "s_nop 2\n"
    "s_cbranch_vccz L_sw_s4\n"
    "global_load_dwordx4 v[160:163], v86, s[30:31] sc1\n"
    "global_load_dwordx4 v[164:167], v87, s[30:31] sc1\n"
    "s_bitset1_b32 s45, 4\n"
    "L_sw_s4:\n"
    "s_bitcmp1_b32 s44, 5\n"
    "s_cbranch_scc0 L_sw_s5\n"
    "v_max3_u32 v64, v168, v169, v170\n"
    "v_max3_u32 v65, v171, v172, v173\n"
    "v_max3_u32 v64, v64, v174, v175\n"
    "v_max_u32 v64, v64, v65\n"
    "v_cmp_eq_u32 vcc, -1, v64\n"
    "s_add_u32 s34, s30, 0x8c0000\n"
    "s_addc_u32 s35, s31, 0\n"
    "s_nop 2\n"
    "s_cbranch_vccz L_sw_s5\n"
    "global_load_dwordx4 v[168:171], v88, s[30:31] sc1\n"
    "global_load_dwordx4 v[172:175], v89, s[30:31] sc1\n"
    "s_bitset1_b32 s45, 5\n"
    "L_sw_s5:\n"
    "s_bitcmp1_b32 s44, 6\n"
    "s_cbranch_scc0 L_sw_s6\n"
    "v_max3_u32 v64, v176, v177, v178\n"
    "v_max3_u32 v65, v179, v180, v181\n"
    "v_max3_u32 v64, v64, v182, v183\n"
    "v_max_u32 v64, v64, v65\n"
    "v_cmp_eq_u32 vcc, -1, v64\n"
    "s_add_u32 s34, s30, 0xa80000\n"
    "s_addc_u32 s35, s31, 0\n"
    "s_nop 2\n"
    "s_cbranch_vccz L_sw_s6\n"
    "global_load_dwordx4 v[176:179], v90, s[30:31] sc1\n"
    "global_load_dwordx4 v[180:183], v91, s[30:31] sc1\n"
    "s_bitset1_b32 s45, 6\n"
    "L_sw_s6:\n"
    "s_bitcmp1_b32 s44, 7\n"
    "s_cbranch_scc0 L_sw_s7\n"
    "v_max3_u32 v64, v184, v185, v186\n"
    "v_max3_u32 v65, v187, v188, v189\n"
    "v_max3_u32 v64, v64, v190, v191\n"
    "v_max_u32 v64, v64, v65\n"
    "v_cmp_eq_u32 vcc, -1, v64\n"
    "s_add_u32 s34, s30, 0xc40000\n"
    "s_addc_u32 s35, s31, 0\n"
    "s_nop 2\n"
    "s_cbranch_vccz L_sw_s7\n"
    "global_load_dwordx4 v[184:187], v92, s[30:31] sc1\n"
    "global_load_dwordx4 v[188:191], v93, s[30:31] sc1\n"
    "s_bitset1_b32 s45, 7\n"
    "L_sw_s7:\n"
    "s_cmp_eq_u32 s45, 0\n"
    "s_cbranch_scc1 L_sw_fin\n"
    "s_mov_b32 s44, s45\n"
    "s_sub_u32 s42, s42, 1\n"
    "s_cmp_lg_u32 s42, 0\n"
    "s_waitcnt vmcnt(0)\n"
    "s_cbranch_scc1 L_sw_lp\n"
    "L_sw_fin:\n"
    "v_mfma_f32_16x16x32_bf16 v[16:19], v[114:117], v[128:131], 0\n"
    "v_mfma_f32_16x16x32_bf16 v[20:23], v[112:115], v[128:131], 0\n"
    "v_mfma_f32_16x16x32_bf16 v[24:27], v[114:117], v[132:135], 0\n"
    "v_mfma_f32_16x16x32_bf16 v[28:31], v[112:115], v[132:135], 0\n"
    "v_mfma_f32_16x16x32_bf16 v[16:19], v[114:117], v[136:139], v[16:19]\n"
    "v_mfma_f32_16x16x32_bf16 v[20:23], v[112:115], v[136:139], v[20:23]\n"
    "v_mfma_f32_16x16x32_bf16 v[24:27], v[114:117], v[140:143], v[24:27]\n"
    "v_mfma_f32_16x16x32_bf16 v[28:31], v[112:115], v[140:143], v[28:31]\n"
    "v_mfma_f32_16x16x32_bf16 v[16:19], v[114:117], v[144:147], v[16:19]\n"
    "v_mfma_f32_16x16x32_bf16 v[20:23], v[112:115], v[144:147], v[20:23]\n"
    "v_mfma_f32_16x16x32_bf16 v[24:27], v[114:117], v[148:151], v[24:27]\n"
    "v_mfma_f32_16x16x32_bf16 v[28:31], v[112:115], v[148:151], v[28:31]\n"
    "v_mfma_f32_16x16x32_bf16 v[16:19], v[114:117], v[152:155], v[16:19]\n"
    "v_mfma_f32_16x16x32_bf16 v[20:23], v[112:115], v[152:155], v[20:23]\n"
    "v_mfma_f32_16x16x32_bf16 v[24:27], v[114:117], v[156:159], v[24:27]\n"
    "v_mfma_f32_16x16x32_bf16 v[28:31], v[112:115], v[156:159], v[28:31]\n"
    "v_mfma_f32_16x16x32_bf16 v[16:19], v[114:117], v[160:163], v[16:19]\n"
    "v_mfma_f32_16x16x32_bf16 v[20:23], v[112:115], v[160:163], v[20:23]\n"
    "v_mfma_f32_16x16x32_bf16 v[24:27], v[114:117], v[164:167], v[24:27]\n"
    "v_mfma_f32_16x16x32_bf16 v[28:31], v[112:115], v[164:167], v[28:31]\n"
    "v_mfma_f32_16x16x32_bf16 v[16:19], v[114:117], v[168:171], v[16:19]\n"
    "v_mfma_f32_16x16x32_bf16 v[20:23], v[112:115], v[168:171], v[20:23]\n"
    "v_mfma_f32_16x16x32_bf16 v[24:27], v[114:117], v[172:175], v[24:27]\n"
    "v_mfma_f32_16x16x32_bf16 v[28:31], v[112:115], v[172:175], v[28:31]\n"
    "v_mfma_f32_16x16x32_bf16 v[16:19], v[114:117], v[176:179], v[16:19]\n"
    "v_mfma_f32_16x16x32_bf16 v[20:23], v[112:115], v[176:179], v[20:23]\n"
    "v_mfma_f32_16x16x32_bf16 v[24:27], v[114:117], v[180:183], v[24:27]\n"
    "v_mfma_f32_16x16x32_bf16 v[28:31], v[112:115], v[180:183], v[28:31]\n"
    "v_mfma_f32_16x16x32_bf16 v[16:19], v[114:117], v[184:187], v[16:19]\n"
    "v_mfma_f32_16x16x32_bf16 v[20:23], v[112:115], v[184:187], v[20:23]\n"
    "v_mfma_f32_16x16x32_bf16 v[24:27], v[114:117], v[188:191], v[24:27]\n"
    "v_mfma_f32_16x16x32_bf16 v[28:31], v[112:115], v[188:191], v[28:31]\n"
    "s_nop 7\n"
    "s_nop 7\n"
    "s_nop 1\n"
    "s_branch L_sw_ok\n"
    "L_own0:\n"
    "ds_write_b128 v247, v[128:131] offset:0\n"
    "s_branch L_ownr0\n"
    "L_own1:\n"
    "ds_write_b128 v247, v[136:139] offset:1024\n"
    "s_branch L_ownr1\n"
    "L_own2:\n"
    "ds_write_b128 v247, v[144:147] offset:2048\n"
    "s_branch L_ownr2\n"
    "L_own3:\n"
    "ds_write_b128 v247, v[152:155] offset:3072\n"
    "s_branch L_ownr3\n"
    "L_own4:\n"
    "ds_write_b128 v247, v[160:163] offset:4096\n"
    "s_branch L_ownr4\n"
    "L_own5:\n"
    "ds_write_b128 v247, v[168:171] offset:5120\n"
    "s_branch L_ownr5\n"
    "L_own6:\n"
    "ds_write_b128 v247, v[176:179] offset:6144\n"
    "s_branch L_ownr6\n"
    "L_own7:\n"
    "ds_write_b128 v247, v[184:187] offset:0\n"
    "s_branch L_ownr7\n"
    "L_own8:\n"
    "ds_write_b128 v247, v[192:195] offset:1024\n"
    "s_branch L_ownr8\n"
    "L_own9:\n"
    "ds_write_b128 v247, v[200:203] offset:2048\n"
    "s_branch L_ownr9\n"
    "L_own10:\n"
    "ds_write_b128 v247, v[208:211] offset:3072\n"
    "s_branch L_ownr10\n"
    "L_own11:\n"
    "ds_write_b128 v247, v[216:219] offset:4096\n"
    "s_branch L_ownr11\n"
    "L_own12:\n"
    "ds_write_b128 v247, v[224:227] offset:5120\n"
    "s_branch L_ownr12\n"
    "L_own13:\n"
    "ds_write_b128 v247, v[232:235] offset:6144\n"
    "s_branch L_ownr13\n"
    "L_mpub:\n"
    "s_lshl_b32 s44, s12, 8\n"
    "s_lshl_b32 s45, s13, 3\n"
    "s_add_u32 s44, s44, s45\n"
    "s_add_u32 s44, s44, 0x3001000\n"
    "s_add_u32 s44, s68, s44\n"
    "s_addc_u32 s45, s69, 0\n"
    "v_mov_b32 v48, s100\n"
    "v_mov_b32 v49, s101\n"
    "v_mov_b32 v50, 0\n"
    "s_mov_b64 exec, 1\n"
    "s_nop 3\n"
    "global_store_dwordx2 v50, v[48:49], s[44:45] sc1\n"
    "s_nop 1\n"
    "s_mov_b64 exec, -1\n"
    "s_branch L_mpub_ret\n"
    "L_rareA_p0:\n"
    "v_max3_f32 v48, v64, v65, v66\n"
    "v_max3_f32 v48, v48, v67, v68\n"
    "v_max3_f32 v48, v48, v69, v70\n"
    "v_max_f32 v48, v48, v71\n"
    "v_max3_f32 v49, v72, v73, v74\n"
    "v_max3_f32 v49, v49, v75, v76\n"
    "v_max3_f32 v49, v49, v77, v78\n"
    "v_max_f32 v49, v49, v79\n"
    "v_max3_f32 v50, v80, v81, v82\n"
    "v_max3_f32 v50, v50, v83, v84\n"
    "v_max3_f32 v50, v50, v85, v86\n"
    "v_max_f32 v50, v50, v87\n"
    "v_max3_f32 v51, v88, v89, v90\n"
    "v_max3_f32 v51, v51, v91, v92\n"
    "v_max3_f32 v51, v51, v93, v94\n"
    "v_max_f32 v51, v51, v95\n"
    "v_mov_b32 v52, v48\n"
    "v_mov_b32 v53, v49\n"
    "v_mov_b32 v54, v50\n"
    "v_mov_b32 v55, v51\n"
    "v_permlane16_swap_b32 v48, v52\n"
    "v_permlane16_swap_b32 v49, v53\n"
    "v_permlane16_swap_b32 v50, v54\n"
    "v_permlane16_swap_b32 v51, v55\n"
    "v_max_f32 v48, v48, v52\n"
    "v_max_f32 v49, v49, v53\n"
    "v_max_f32 v50, v50, v54\n"
    "v_max_f32 v51, v51, v55\n"
    "v_mov_b32 v52, v48\n"
    "v_mov_b32 v53, v49\n"
    "v_mov_b32 v54, v50\n"
    "v_mov_b32 v55, v51\n"
    "v_permlane32_swap_b32 v48, v52\n"
    "v_permlane32_swap_b32 v49, v53\n"
    "v_permlane32_swap_b32 v50, v54\n"
    "v_permlane32_swap_b32 v51, v55\n"
    "v_max_f32 v48, v48, v52\n"
    "v_max_f32 v49, v49, v53\n"
    "v_max_f32 v50, v50, v54\n"
    "v_max_f32 v51, v51, v55\n"
    "v_sub_f32 v64, v64, v48\n"
    "v_sub_f32 v65, v65, v48\n"
    "v_sub_f32 v66, v66, v48\n"
    "v_sub_f32 v67, v67, v48\n"
    "v_sub_f32 v68, v68, v48\n"
    "v_sub_f32 v69, v69, v48\n"
    "v_sub_f32 v70, v70, v48\n"
    "v_sub_f32 v71, v71, v48\n"
    "v_sub_f32 v32, v32, v48\n"
    "v_sub_f32 v33, v33, v48\n"
    "v_sub_f32 v34, v34, v48\n"
    "v_sub_f32 v35, v35, v48\n"
    "v_sub_f32 v72, v72, v49\n"
    "v_sub_f32 v73, v73, v49\n"
    "v_sub_f32 v74, v74, v49\n"
    "v_sub_f32 v75, v75, v49\n"
    "v_sub_f32 v76, v76, v49\n"
    "v_sub_f32 v77, v77, v49\n"
    "v_sub_f32 v78, v78, v49\n"
    "v_sub_f32 v79, v79, v49\n"
    "v_sub_f32 v36, v36, v49\n"
    "v_sub_f32 v37, v37, v49\n"
    "v_sub_f32 v38, v38, v49\n"
    "v_sub_f32 v39, v39, v49\n"
    "v_sub_f32 v80, v80, v50\n"
    "v_sub_f32 v81, v81, v50\n"
    "v_sub_f32 v82, v82, v50\n"
    "v_sub_f32 v83, v83, v50\n"
    "v_sub_f32 v84, v84, v50\n"
    "v_sub_f32 v85, v85, v50\n"
    "v_sub_f32 v86, v86, v50\n"
    "v_sub_f32 v87, v87, v50\n"
    "v_sub_f32 v40, v40, v50\n"
    "v_sub_f32 v41, v41, v50\n"
    "v_sub_f32 v42, v42, v50\n"
    "v_sub_f32 v43, v43, v50\n"
    "v_sub_f32 v88, v88, v51\n"
    "v_sub_f32 v89, v89, v51\n"
    "v_sub_f32 v90, v90, v51\n"
    "v_sub_f32 v91, v91, v51\n"
    "v_sub_f32 v92, v92, v51\n"
    "v_sub_f32 v93, v93, v51\n"
    "v_sub_f32 v94, v94, v51\n"
    "v_sub_f32 v95, v95, v51\n"
    "v_sub_f32 v44, v44, v51\n"
    "v_sub_f32 v45, v45, v51\n"
    "v_sub_f32 v46, v46, v51\n"
    "v_sub_f32 v47, v47, v51\n"
    "v_mov_b32 v48, -1\n"
    "v_mov_b32 v49, -1\n"
    "v_mov_b32 v50, -1\n"
    "v_mov_b32 v51, -1\n"
    "s_branch L_rareA_ret_p0\n"
    "L_rareA_p1:\n"
    "v_max3_f32 v48, v96, v97, v98\n"
    "v_max3_f32 v48, v48, v99, v100\n"
    "v_max3_f32 v48, v48, v101, v102\n"
    "v_max_f32 v48, v48, v103\n"
    "v_max3_f32 v49, v104, v105, v106\n"
    "v_max3_f32 v49, v49, v107, v108\n"
    "v_max3_f32 v49, v49, v109, v110\n"
    "v_max_f32 v49, v49, v111\n"
    "v_max3_f32 v50, v112, v113, v114\n"
    "v_max3_f32 v50, v50, v115, v116\n"
    "v_max3_f32 v50, v50, v117, v118\n"
    "v_max_f32 v50, v50, v119\n"
    "v_max3_f32 v51, v120, v121, v122\n"
    "v_max3_f32 v51, v51, v123, v124\n"
    "v_max3_f32 v51, v51, v125, v126\n"
    "v_max_f32 v51, v51, v127\n"
    "v_mov_b32 v52, v48\n"
    "v_mov_b32 v53, v49\n"
    "v_mov_b32 v54, v50\n"
    "s_waitcnt vmcnt(18)\n"
    "v_mov_b32 v55, v51\n"
    "v_permlane16_swap_b32 v48, v52\n"
    "v_permlane16_swap_b32 v49, v53\n"
    "v_permlane16_swap_b32 v50, v54\n"
    "v_permlane16_swap_b32 v51, v55\n"
    "v_max_f32 v48, v48, v52\n"
    "v_max_f32 v49, v49, v53\n"
    "v_max_f32 v50, v50, v54\n"
    "v_max_f32 v51, v51, v55\n"
    "v_mov_b32 v52, v48\n"
    "v_mov_b32 v53, v49\n"
    "v_mov_b32 v54, v50\n"
    "v_mov_b32 v55, v51\n"
    "v_permlane32_swap_b32 v48, v52\n"
    "v_permlane32_swap_b32 v49, v53\n"
    "v_permlane32_swap_b32 v50, v54\n"
    "v_permlane32_swap_b32 v51, v55\n"
    "v_max_f32 v48, v48, v52\n"
    "v_max_f32 v49, v49, v53\n"
    "v_max_f32 v50, v50, v54\n"
    "v_max_f32 v51, v51, v55\n"
    "v_max_f32 v48, 0, v48\n"
    "v_max_f32 v49, 0, v49\n"
    "v_max_f32 v50, 0, v50\n"
    "v_max_f32 v51, 0, v51\n"
    "v_sub_f32 v96, v96, v48\n"
    "v_sub_f32 v97, v97, v48\n"
    "v_sub_f32 v98, v98, v48\n"
    "v_sub_f32 v99, v99, v48\n"
    "v_sub_f32 v100, v100, v48\n"
    "v_sub_f32 v101, v101, v48\n"
    "v_sub_f32 v102, v102, v48\n"
    "v_sub_f32 v103, v103, v48\n"
    "v_sub_f32 v32, v32, v48\n"
    "v_sub_f32 v33, v33, v48\n"
    "v_sub_f32 v34, v34, v48\n"
    "v_sub_f32 v35, v35, v48\n"
    "v_sub_f32 v104, v104, v49\n"
    "v_sub_f32 v105, v105, v49\n"
    "v_sub_f32 v106, v106, v49\n"
    "v_sub_f32 v107, v107, v49\n"
    "v_sub_f32 v108, v108, v49\n"
    "v_sub_f32 v109, v109, v49\n"
    "v_sub_f32 v110, v110, v49\n"
    "v_sub_f32 v111, v111, v49\n"
    "v_sub_f32 v36, v36, v49\n"
    "v_sub_f32 v37, v37, v49\n"
    "v_sub_f32 v38, v38, v49\n"
    "v_sub_f32 v39, v39, v49\n"
    "v_sub_f32 v112, v112, v50\n"
    "v_sub_f32 v113, v113, v50\n"
    "v_sub_f32 v114, v114, v50\n"
    "v_sub_f32 v115, v115, v50\n"
    "v_sub_f32 v116, v116, v50\n"
    "v_sub_f32 v117, v117, v50\n"
    "v_sub_f32 v118, v118, v50\n"
    "v_sub_f32 v119, v119, v50\n"
    "v_sub_f32 v40, v40, v50\n"
    "v_sub_f32 v41, v41, v50\n"
    "v_sub_f32 v42, v42, v50\n"
    "v_sub_f32 v43, v43, v50\n"
    "v_sub_f32 v120, v120, v51\n"
    "v_sub_f32 v121, v121, v51\n"
    "v_sub_f32 v122, v122, v51\n"
    "v_sub_f32 v123, v123, v51\n"
    "v_sub_f32 v124, v124, v51\n"
    "v_sub_f32 v125, v125, v51\n"
    "v_sub_f32 v126, v126, v51\n"
    "v_sub_f32 v127, v127, v51\n"
    "v_sub_f32 v44, v44, v51\n"
    "v_sub_f32 v45, v45, v51\n"
    "v_sub_f32 v46, v46, v51\n"
    "v_sub_f32 v47, v47, v51\n"
    "v_exp_f32 v12, -v48\n"
    "v_exp_f32 v13, -v49\n"
    "v_exp_f32 v14, -v50\n"
    "v_exp_f32 v15, -v51\n"
    "v_mul_f32 v8, v8, v12\n"
    "v_mul_f32 v9, v9, v13\n"
    "v_mul_f32 v10, v10, v14\n"
    "v_mul_f32 v11, v11, v15\n"
    "s_mov_b32 s37, 1\n"
    "v_mov_b32 v48, -1\n"
    "v_mov_b32 v49, -1\n"
    "v_mov_b32 v50, -1\n"
    "v_mov_b32 v51, -1\n"
    "s_branch L_rareA_ret_p1\n"
    "L_rareA_p2:\n"
    "v_max3_f32 v48, v64, v65, v66\n"
    "v_max3_f32 v48, v48, v67, v68\n"
    "v_max3_f32 v48, v48, v69, v70\n"
    "v_max_f32 v48, v48, v71\n"
    "v_max3_f32 v49, v72, v73, v74\n"
    "v_max3_f32 v49, v49, v75, v76\n"
    "v_max3_f32 v49, v49, v77, v78\n"
    "v_max_f32 v49, v49, v79\n"
    "v_max3_f32 v50, v80, v81, v82\n"
    "v_max3_f32 v50, v50, v83, v84\n"
    "v_max3_f32 v50, v50, v85, v86\n"
    "v_max_f32 v50, v50, v87\n"
    "v_max3_f32 v51, v88, v89, v90\n"
    "v_max3_f32 v51, v51, v91, v92\n"
    "v_max3_f32 v51, v51, v93, v94\n"
    "v_max_f32 v51, v51, v95\n"
    "v_mov_b32 v52, v48\n"
    "v_mov_b32 v53, v49\n"
    "v_mov_b32 v54, v50\n"
    "s_waitcnt vmcnt(0)\n"
    "v_mov_b32 v55, v51\n"
    "v_permlane16_swap_b32 v48, v52\n"
    "v_permlane16_swap_b32 v49, v53\n"
    "v_permlane16_swap_b32 v50, v54\n"
    "v_permlane16_swap_b32 v51, v55\n"
    "v_max_f32 v48, v48, v52\n"
    "v_max_f32 v49, v49, v53\n"
    "v_max_f32 v50, v50, v54\n"
    "v_max_f32 v51, v51, v55\n"
    "v_mov_b32 v52, v48\n"
    "v_mov_b32 v53, v49\n"
    "v_mov_b32 v54, v50\n"
    "v_mov_b32 v55, v51\n"
    "v_permlane32_swap_b32 v48, v52\n"
    "v_permlane32_swap_b32 v49, v53\n"
    "v_permlane32_swap_b32 v50, v54\n"
    "v_permlane32_swap_b32 v51, v55\n"
    "v_max_f32 v48, v48, v52\n"
    "v_max_f32 v49, v49, v53\n"
    "v_max_f32 v50, v50, v54\n"
    "v_max_f32 v51, v51, v55\n"
    "v_max_f32 v48, 0, v48\n"
    "v_max_f32 v49, 0, v49\n"
    "v_max_f32 v50, 0, v50\n"
    "v_max_f32 v51, 0, v51\n"
    "v_sub_f32 v64, v64, v48\n"
    "v_sub_f32 v65, v65, v48\n"
    "v_sub_f32 v66, v66, v48\n"
    "v_sub_f32 v67, v67, v48\n"
    "v_sub_f32 v68, v68, v48\n"
    "v_sub_f32 v69, v69, v48\n"
    "v_sub_f32 v70, v70, v48\n"
    "v_sub_f32 v71, v71, v48\n"
    "v_sub_f32 v32, v32, v48\n"
    "v_sub_f32 v33, v33, v48\n"
    "v_sub_f32 v34, v34, v48\n"
    "v_sub_f32 v35, v35, v48\n"
    "v_sub_f32 v72, v72, v49\n"
    "v_sub_f32 v73, v73, v49\n"
    "v_sub_f32 v74, v74, v49\n"
    "v_sub_f32 v75, v75, v49\n"
    "v_sub_f32 v76, v76, v49\n"
    "v_sub_f32 v77, v77, v49\n"
    "v_sub_f32 v78, v78, v49\n"
    "v_sub_f32 v79, v79, v49\n"
    "v_sub_f32 v36, v36, v49\n"
    "v_sub_f32 v37, v37, v49\n"
    "v_sub_f32 v38, v38, v49\n"
    "v_sub_f32 v39, v39, v49\n"
    "v_sub_f32 v80, v80, v50\n"
    "v_sub_f32 v81, v81, v50\n"
    "v_sub_f32 v82, v82, v50\n"
    "v_sub_f32 v83, v83, v50\n"
    "v_sub_f32 v84, v84, v50\n"
    "v_sub_f32 v85, v85, v50\n"
    "v_sub_f32 v86, v86, v50\n"
    "v_sub_f32 v87, v87, v50\n"
    "v_sub_f32 v40, v40, v50\n"
    "v_sub_f32 v41, v41, v50\n"
    "v_sub_f32 v42, v42, v50\n"
    "v_sub_f32 v43, v43, v50\n"
    "v_sub_f32 v88, v88, v51\n"
    "v_sub_f32 v89, v89, v51\n"
    "v_sub_f32 v90, v90, v51\n"
    "v_sub_f32 v91, v91, v51\n"
    "v_sub_f32 v92, v92, v51\n"
    "v_sub_f32 v93, v93, v51\n"
    "v_sub_f32 v94, v94, v51\n"
    "v_sub_f32 v95, v95, v51\n"
    "v_sub_f32 v44, v44, v51\n"
    "v_sub_f32 v45, v45, v51\n"
    "v_sub_f32 v46, v46, v51\n"
    "v_sub_f32 v47, v47, v51\n"
    "v_exp_f32 v12, -v48\n"
    "v_exp_f32 v13, -v49\n"
    "v_exp_f32 v14, -v50\n"
    "v_exp_f32 v15, -v51\n"
    "v_mul_f32 v8, v8, v12\n"
    "v_mul_f32 v9, v9, v13\n"
    "v_mul_f32 v10, v10, v14\n"
    "v_mul_f32 v11, v11, v15\n"
    "s_mov_b32 s37, 1\n"
    "v_mov_b32 v48, -1\n"
    "v_mov_b32 v49, -1\n"
    "v_mov_b32 v50, -1\n"
    "v_mov_b32 v51, -1\n"
    "s_branch L_rareA_ret_p2\n"
    "L_rareA_p31:\n"
    "v_max3_f32 v48, v96, v97, v98\n"
    "v_max3_f32 v48, v48, v99, v100\n"
    "v_max3_f32 v48, v48, v101, v102\n"
    "v_max_f32 v48, v48, v103\n"
    "v_max3_f32 v49, v104, v105, v106\n"
    "v_max3_f32 v49, v49, v107, v108\n"
    "v_max3_f32 v49, v49, v109, v110\n"
    "v_max_f32 v49, v49, v111\n"
    "v_max3_f32 v50, v112, v113, v114\n"
    "v_max3_f32 v50, v50, v115, v116\n"
    "v_max3_f32 v50, v50, v117, v118\n"
    "v_max_f32 v50, v50, v119\n"
    "v_max3_f32 v51, v120, v121, v122\n"
    "v_max3_f32 v51, v51, v123, v124\n"
    "v_max3_f32 v51, v51, v125, v126\n"
    "v_max_f32 v51, v51, v127\n"
    "v_mov_b32 v52, v48\n"
    "v_mov_b32 v53, v49\n"
    "v_mov_b32 v54, v50\n"
    "s_waitcnt vmcnt(17)\n"
    "v_mov_b32 v55, v51\n"
    "v_permlane16_swap_b32 v48, v52\n"
    "v_permlane16_swap_b32 v49, v53\n"
    "v_permlane16_swap_b32 v50, v54\n"
    "v_permlane16_swap_b32 v51, v55\n"
    "v_max_f32 v48, v48, v52\n"
    "v_max_f32 v49, v49, v53\n"
    "v_max_f32 v50, v50, v54\n"
    "v_max_f32 v51, v51, v55\n"
    "v_mov_b32 v52, v48\n"
    "v_mov_b32 v53, v49\n"
    "v_mov_b32 v54, v50\n"
    "v_mov_b32 v55, v51\n"
    "v_permlane32_swap_b32 v48, v52\n"
    "v_permlane32_swap_b32 v49, v53\n"
    "v_permlane32_swap_b32 v50, v54\n"
    "v_permlane32_swap_b32 v51, v55\n"
    "v_max_f32 v48, v48, v52\n"
    "v_max_f32 v49, v49, v53\n"
    "v_max_f32 v50, v50, v54\n"
    "v_max_f32 v51, v51, v55\n"
    "v_max_f32 v48, 0, v48\n"
    "v_max_f32 v49, 0, v49\n"
    "v_max_f32 v50, 0, v50\n"
    "v_max_f32 v51, 0, v51\n"
    "v_sub_f32 v96, v96, v48\n"
    "v_sub_f32 v97, v97, v48\n"
    "v_sub_f32 v98, v98, v48\n"
    "v_sub_f32 v99, v99, v48\n"
    "v_sub_f32 v100, v100, v48\n"
    "v_sub_f32 v101, v101, v48\n"
    "v_sub_f32 v102, v102, v48\n"
    "v_sub_f32 v103, v103, v48\n"
    "v_sub_f32 v32, v32, v48\n"
    "v_sub_f32 v33, v33, v48\n"
    "v_sub_f32 v34, v34, v48\n"
    "v_sub_f32 v35, v35, v48\n"
    "v_sub_f32 v104, v104, v49\n"
    "v_sub_f32 v105, v105, v49\n"
    "v_sub_f32 v106, v106, v49\n"
    "v_sub_f32 v107, v107, v49\n"
    "v_sub_f32 v108, v108, v49\n"
    "v_sub_f32 v109, v109, v49\n"
    "v_sub_f32 v110, v110, v49\n"
    "v_sub_f32 v111, v111, v49\n"
    "v_sub_f32 v36, v36, v49\n"
    "v_sub_f32 v37, v37, v49\n"
    "v_sub_f32 v38, v38, v49\n"
    "v_sub_f32 v39, v39, v49\n"
    "v_sub_f32 v112, v112, v50\n"
    "v_sub_f32 v113, v113, v50\n"
    "v_sub_f32 v114, v114, v50\n"
    "v_sub_f32 v115, v115, v50\n"
    "v_sub_f32 v116, v116, v50\n"
    "v_sub_f32 v117, v117, v50\n"
    "v_sub_f32 v118, v118, v50\n"
    "v_sub_f32 v119, v119, v50\n"
    "v_sub_f32 v40, v40, v50\n"
    "v_sub_f32 v41, v41, v50\n"
    "v_sub_f32 v42, v42, v50\n"
    "v_sub_f32 v43, v43, v50\n"
    "v_sub_f32 v120, v120, v51\n"
    "v_sub_f32 v121, v121, v51\n"
    "v_sub_f32 v122, v122, v51\n"
    "v_sub_f32 v123, v123, v51\n"
    "v_sub_f32 v124, v124, v51\n"
    "v_sub_f32 v125, v125, v51\n"
    "v_sub_f32 v126, v126, v51\n"
    "v_sub_f32 v127, v127, v51\n"
    "v_sub_f32 v44, v44, v51\n"
    "v_sub_f32 v45, v45, v51\n"
    "v_sub_f32 v46, v46, v51\n"
    "v_sub_f32 v47, v47, v51\n"
    "v_exp_f32 v12, -v48\n"
    "v_exp_f32 v13, -v49\n"
    "v_exp_f32 v14, -v50\n"
    "v_exp_f32 v15, -v51\n"
    "v_mul_f32 v8, v8, v12\n"
    "v_mul_f32 v9, v9, v13\n"
    "v_mul_f32 v10, v10, v14\n"
    "v_mul_f32 v11, v11, v15\n"
    "s_mov_b32 s37, 1\n"
    "v_mov_b32 v48, -1\n"
    "v_mov_b32 v49, -1\n"
    "v_mov_b32 v50, -1\n"
    "v_mov_b32 v51, -1\n"
    "s_branch L_rareA_ret_p31\n"
    "L_rb_p1:\n"
    "s_swappc_b64 s[42:43], s[40:41]\n"
    "s_branch L_rb_ret_p1\n"
    "L_rb_p2:\n"
    "s_swappc_b64 s[42:43], s[40:41]\n"
    "s_branch L_rb_ret_p2\n"
    "L_rb_p31:\n"
    "s_swappc_b64 s[42:43], s[40:41]\n"
    "s_branch L_rb_ret_p31\n"
    "L_rareB:\n"
    "v_accvgpr_read_b32 v48, a0\n"
    "v_accvgpr_read_b32 v49, a1\n"
    "v_accvgpr_read_b32 v50, a2\n"
    "v_accvgpr_read_b32 v51, a3\n"
    "v_accvgpr_read_b32 v52, a4\n"
    "v_accvgpr_read_b32 v53, a5\n"
    "v_accvgpr_read_b32 v54, a6\n"
    "s_waitcnt vmcnt(3)\n"
    "v_accvgpr_read_b32 v55, a7\n"
    "v_mul_f32 v48, v48, v12\n"
    "v_mul_f32 v49, v49, v12\n"
    "v_mul_f32 v50, v50, v12\n"
    "v_mul_f32 v51, v51, v12\n"
    "v_mul_f32 v52, v52, v13\n"
    "v_mul_f32 v53, v53, v13\n"
    "v_mul_f32 v54, v54, v13\n"
    "v_mul_f32 v55, v55, v13\n"
    "v_accvgpr_write_b32 a0, v48\n"
    "v_accvgpr_write_b32 a1, v49\n"
    "v_accvgpr_write_b32 a2, v50\n"
    "v_accvgpr_write_b32 a3, v51\n"
    "v_accvgpr_write_b32 a4, v52\n"
    "v_accvgpr_write_b32 a5, v53\n"
    "v_accvgpr_write_b32 a6, v54\n"
    "v_accvgpr_write_b32 a7, v55\n"
    "v_accvgpr_read_b32 v48, a8\n"
    "v_accvgpr_read_b32 v49, a9\n"
    "v_accvgpr_read_b32 v50, a10\n"
    "v_accvgpr_read_b32 v51, a11\n"
    "v_accvgpr_read_b32 v52, a12\n"
    "v_accvgpr_read_b32 v53, a13\n"
    "v_accvgpr_read_b32 v54, a14\n"
    "v_accvgpr_read_b32 v55, a15\n"
    "v_mul_f32 v48, v48, v14\n"
    "v_mul_f32 v49, v49, v14\n"
    "v_mul_f32 v50, v50, v14\n"
    "v_mul_f32 v51, v51, v14\n"
    "v_mul_f32 v52, v52, v15\n"
    "v_mul_f32 v53, v53, v15\n"
    "v_mul_f32 v54, v54, v15\n"
    "v_mul_f32 v55, v55, v15\n"
    "v_accvgpr_write_b32 a8, v48\n"
    "v_accvgpr_write_b32 a9, v49\n"
    "v_accvgpr_write_b32 a10, v50\n"
    "v_accvgpr_write_b32 a11, v51\n"
    "v_accvgpr_write_b32 a12, v52\n"
    "v_accvgpr_write_b32 a13, v53\n"
    "v_accvgpr_write_b32 a14, v54\n"
    "v_accvgpr_write_b32 a15, v55\n"
    "v_accvgpr_read_b32 v48, a16\n"
    "v_accvgpr_read_b32 v49, a17\n"
    "v_accvgpr_read_b32 v50, a18\n"
    "v_accvgpr_read_b32 v51, a19\n"
    "v_accvgpr_read_b32 v52, a20\n"
    "v_accvgpr_read_b32 v53, a21\n"
    "v_accvgpr_read_b32 v54, a22\n"
    "v_accvgpr_read_b32 v55, a23\n"
    "v_mul_f32 v48, v48, v12\n"
    "v_mul_f32 v49, v49, v12\n"
    "v_mul_f32 v50, v50, v12\n"
    "v_mul_f32 v51, v51, v12\n"
    "v_mul_f32 v52, v52, v13\n"
    "v_mul_f32 v53, v53, v13\n"
    "v_mul_f32 v54, v54, v13\n"
    "v_mul_f32 v55, v55, v13\n"
    "v_accvgpr_write_b32 a16, v48\n"
    "v_accvgpr_write_b32 a17, v49\n"
    "v_accvgpr_write_b32 a18, v50\n"
    "v_accvgpr_write_b32 a19, v51\n"
    "v_accvgpr_write_b32 a20, v52\n"
    "v_accvgpr_write_b32 a21, v53\n"
    "v_accvgpr_write_b32 a22, v54\n"
    "v_accvgpr_write_b32 a23, v55\n"
    "v_accvgpr_read_b32 v48, a24\n"
    "v_accvgpr_read_b32 v49, a25\n"
    "v_accvgpr_read_b32 v50, a26\n"
    "v_accvgpr_read_b32 v51, a27\n"
    "v_accvgpr_read_b32 v52, a28\n"
    "v_accvgpr_read_b32 v53, a29\n"
    "v_accvgpr_read_b32 v54, a30\n"
    "v_accvgpr_read_b32 v55, a31\n"
    "v_mul_f32 v48, v48, v14\n"
    "v_mul_f32 v49, v49, v14\n"
    "v_mul_f32 v50, v50, v14\n"
    "v_mul_f32 v51, v51, v14\n"
    "v_mul_f32 v52, v52, v15\n"
    "v_mul_f32 v53, v53, v15\n"
    "v_mul_f32 v54, v54, v15\n"
    "v_mul_f32 v55, v55, v15\n"
    "v_accvgpr_write_b32 a24, v48\n"
    "v_accvgpr_write_b32 a25, v49\n"
    "v_accvgpr_write_b32 a26, v50\n"
    "v_accvgpr_write_b32 a27, v51\n"
    "v_accvgpr_write_b32 a28, v52\n"
    "v_accvgpr_write_b32 a29, v53\n"
    "v_accvgpr_write_b32 a30, v54\n"
    "v_accvgpr_write_b32 a31, v55\n"
    "v_accvgpr_read_b32 v48, a32\n"
    "v_accvgpr_read_b32 v49, a33\n"
    "v_accvgpr_read_b32 v50, a34\n"
    "v_accvgpr_read_b32 v51, a35\n"
    "v_accvgpr_read_b32 v52, a36\n"
    "v_accvgpr_read_b32 v53, a37\n"
    "v_accvgpr_read_b32 v54, a38\n"
    "v_accvgpr_read_b32 v55, a39\n"
    "v_mul_f32 v48, v48, v12\n"
    "v_mul_f32 v49, v49, v12\n"
    "v_mul_f32 v50, v50, v12\n"
    "v_mul_f32 v51, v51, v12\n"
    "v_mul_f32 v52, v52, v13\n"
    "v_mul_f32 v53, v53, v13\n"
    "v_mul_f32 v54, v54, v13\n"
    "v_mul_f32 v55, v55, v13\n"
    "v_accvgpr_write_b32 a32, v48\n"
    "v_accvgpr_write_b32 a33, v49\n"
    "v_accvgpr_write_b32 a34, v50\n"
    "v_accvgpr_write_b32 a35, v51\n"
    "v_accvgpr_write_b32 a36, v52\n"
    "v_accvgpr_write_b32 a37, v53\n"
    "v_accvgpr_write_b32 a38, v54\n"
    "v_accvgpr_write_b32 a39, v55\n"
    "v_accvgpr_read_b32 v48, a40\n"
    "v_accvgpr_read_b32 v49, a41\n"
    "v_accvgpr_read_b32 v50, a42\n"
    "v_accvgpr_read_b32 v51, a43\n"
    "v_accvgpr_read_b32 v52, a44\n"
    "v_accvgpr_read_b32 v53, a45\n"
    "v_accvgpr_read_b32 v54, a46\n"
    "v_accvgpr_read_b32 v55, a47\n"
    "v_mul_f32 v48, v48, v14\n"
    "v_mul_f32 v49, v49, v14\n"
    "v_mul_f32 v50, v50, v14\n"
    "v_mul_f32 v51, v51, v14\n"
    "v_mul_f32 v52, v52, v15\n"
    "v_mul_f32 v53, v53, v15\n"
    "v_mul_f32 v54, v54, v15\n"
    "v_mul_f32 v55, v55, v15\n"
    "v_accvgpr_write_b32 a40, v48\n"
    "v_accvgpr_write_b32 a41, v49\n"
    "v_accvgpr_write_b32 a42, v50\n"
    "v_accvgpr_write_b32 a43, v51\n"
    "v_accvgpr_write_b32 a44, v52\n"
    "v_accvgpr_write_b32 a45, v53\n"
    "v_accvgpr_write_b32 a46, v54\n"
    "v_accvgpr_write_b32 a47, v55\n"
    "v_accvgpr_read_b32 v48, a48\n"
    "v_accvgpr_read_b32 v49, a49\n"
    "v_accvgpr_read_b32 v50, a50\n"
    "v_accvgpr_read_b32 v51, a51\n"
    "v_accvgpr_read_b32 v52, a52\n"
    "v_accvgpr_read_b32 v53, a53\n"
    "v_accvgpr_read_b32 v54, a54\n"
    "v_accvgpr_read_b32 v55, a55\n"
    "v_mul_f32 v48, v48, v12\n"
    "v_mul_f32 v49, v49, v12\n"
    "v_mul_f32 v50, v50, v12\n"
    "v_mul_f32 v51, v51, v12\n"
    "v_mul_f32 v52, v52, v13\n"
    "v_mul_f32 v53, v53, v13\n"
    "v_mul_f32 v54, v54, v13\n"
    "v_mul_f32 v55, v55, v13\n"
    "v_accvgpr_write_b32 a48, v48\n"
    "v_accvgpr_write_b32 a49, v49\n"
    "v_accvgpr_write_b32 a50, v50\n"
    "v_accvgpr_write_b32 a51, v51\n"
    "v_accvgpr_write_b32 a52, v52\n"
    "v_accvgpr_write_b32 a53, v53\n"
    "v_accvgpr_write_b32 a54, v54\n"
    "v_accvgpr_write_b32 a55, v55\n"
    "v_accvgpr_read_b32 v48, a56\n"
    "v_accvgpr_read_b32 v49, a57\n"
    "v_accvgpr_read_b32 v50, a58\n"
    "v_accvgpr_read_b32 v51, a59\n"
    "v_accvgpr_read_b32 v52, a60\n"
    "v_accvgpr_read_b32 v53, a61\n"
    "v_accvgpr_read_b32 v54, a62\n"
    "v_accvgpr_read_b32 v55, a63\n"
    "v_mul_f32 v48, v48, v14\n"
    "v_mul_f32 v49, v49, v14\n"
    "v_mul_f32 v50, v50, v14\n"
    "v_mul_f32 v51, v51, v14\n"
    "v_mul_f32 v52, v52, v15\n"
    "v_mul_f32 v53, v53, v15\n"
    "v_mul_f32 v54, v54, v15\n"
    "v_mul_f32 v55, v55, v15\n"
    "v_accvgpr_write_b32 a56, v48\n"
    "v_accvgpr_write_b32 a57, v49\n"
    "v_accvgpr_write_b32 a58, v50\n"
    "v_accvgpr_write_b32 a59, v51\n"
    "v_accvgpr_write_b32 a60, v52\n"
    "v_accvgpr_write_b32 a61, v53\n"
    "v_accvgpr_write_b32 a62, v54\n"
    "v_accvgpr_write_b32 a63, v55\n"
    "v_accvgpr_read_b32 v48, a64\n"
    "v_accvgpr_read_b32 v49, a65\n"
    "v_accvgpr_read_b32 v50, a66\n"
    "v_accvgpr_read_b32 v51, a67\n"
    "v_accvgpr_read_b32 v52, a68\n"
    "v_accvgpr_read_b32 v53, a69\n"
    "v_accvgpr_read_b32 v54, a70\n"
    "v_accvgpr_read_b32 v55, a71\n"
    "v_mul_f32 v48, v48, v12\n"
    "v_mul_f32 v49, v49, v12\n"
    "v_mul_f32 v50, v50, v12\n"
    "v_mul_f32 v51, v51, v12\n"
    "v_mul_f32 v52, v52, v13\n"
    "v_mul_f32 v53, v53, v13\n"
    "v_mul_f32 v54, v54, v13\n"
    "v_mul_f32 v55, v55, v13\n"
    "v_accvgpr_write_b32 a64, v48\n"
    "v_accvgpr_write_b32 a65, v49\n"
    "v_accvgpr_write_b32 a66, v50\n"
    "v_accvgpr_write_b32 a67, v51\n"
    "v_accvgpr_write_b32 a68, v52\n"
    "v_accvgpr_write_b32 a69, v53\n"
    "v_accvgpr_write_b32 a70, v54\n"
    "v_accvgpr_write_b32 a71, v55\n"
    "v_accvgpr_read_b32 v48, a72\n"
    "v_accvgpr_read_b32 v49, a73\n"
    "v_accvgpr_read_b32 v50, a74\n"
    "v_accvgpr_read_b32 v51, a75\n"
    "v_accvgpr_read_b32 v52, a76\n"
    "v_accvgpr_read_b32 v53, a77\n"
    "v_accvgpr_read_b32 v54, a78\n"
    "v_accvgpr_read_b32 v55, a79\n"
    "v_mul_f32 v48, v48, v14\n"
    "v_mul_f32 v49, v49, v14\n"
    "v_mul_f32 v50, v50, v14\n"
    "v_mul_f32 v51, v51, v14\n"
    "v_mul_f32 v52, v52, v15\n"
    "v_mul_f32 v53, v53, v15\n"
    "v_mul_f32 v54, v54, v15\n"
    "v_mul_f32 v55, v55, v15\n"
    "v_accvgpr_write_b32 a72, v48\n"
    "v_accvgpr_write_b32 a73, v49\n"
    "v_accvgpr_write_b32 a74, v50\n"
    "v_accvgpr_write_b32 a75, v51\n"
    "v_accvgpr_write_b32 a76, v52\n"
    "v_accvgpr_write_b32 a77, v53\n"
    "v_accvgpr_write_b32 a78, v54\n"
    "v_accvgpr_write_b32 a79, v55\n"
    "v_accvgpr_read_b32 v48, a80\n"
    "v_accvgpr_read_b32 v49, a81\n"
    "v_accvgpr_read_b32 v50, a82\n"
    "v_accvgpr_read_b32 v51, a83\n"
    "v_accvgpr_read_b32 v52, a84\n"
    "v_accvgpr_read_b32 v53, a85\n"
    "v_accvgpr_read_b32 v54, a86\n"
    "v_accvgpr_read_b32 v55, a87\n"
    "v_mul_f32 v48, v48, v12\n"
    "v_mul_f32 v49, v49, v12\n"
    "v_mul_f32 v50, v50, v12\n"
    "v_mul_f32 v51, v51, v12\n"
    "v_mul_f32 v52, v52, v13\n"
    "v_mul_f32 v53, v53, v13\n"
    "v_mul_f32 v54, v54, v13\n"
    "v_mul_f32 v55, v55, v13\n"
    "v_accvgpr_write_b32 a80, v48\n"
    "v_accvgpr_write_b32 a81, v49\n"
    "v_accvgpr_write_b32 a82, v50\n"
    "v_accvgpr_write_b32 a83, v51\n"
    "v_accvgpr_write_b32 a84, v52\n"
    "v_accvgpr_write_b32 a85, v53\n"
    "v_accvgpr_write_b32 a86, v54\n"
    "v_accvgpr_write_b32 a87, v55\n"
    "v_accvgpr_read_b32 v48, a88\n"
    "v_accvgpr_read_b32 v49, a89\n"
    "v_accvgpr_read_b32 v50, a90\n"
    "v_accvgpr_read_b32 v51, a91\n"
    "v_accvgpr_read_b32 v52, a92\n"
    "v_accvgpr_read_b32 v53, a93\n"
    "v_accvgpr_read_b32 v54, a94\n"
    "v_accvgpr_read_b32 v55, a95\n"
    "v_mul_f32 v48, v48, v14\n"
    "v_mul_f32 v49, v49, v14\n"
    "v_mul_f32 v50, v50, v14\n"
    "v_mul_f32 v51, v51, v14\n"
    "v_mul_f32 v52, v52, v15\n"
    "v_mul_f32 v53, v53, v15\n"
    "v_mul_f32 v54, v54, v15\n"
    "v_mul_f32 v55, v55, v15\n"
    "v_accvgpr_write_b32 a88, v48\n"
    "v_accvgpr_write_b32 a89, v49\n"
    "v_accvgpr_write_b32 a90, v50\n"
    "v_accvgpr_write_b32 a91, v51\n"
    "v_accvgpr_write_b32 a92, v52\n"
    "v_accvgpr_write_b32 a93, v53\n"
    "v_accvgpr_write_b32 a94, v54\n"
    "v_accvgpr_write_b32 a95, v55\n"
    "v_accvgpr_read_b32 v48, a96\n"
    "v_accvgpr_read_b32 v49, a97\n"
    "v_accvgpr_read_b32 v50, a98\n"
    "v_accvgpr_read_b32 v51, a99\n"
    "v_accvgpr_read_b32 v52, a100\n"
    "v_accvgpr_read_b32 v53, a101\n"
    "v_accvgpr_read_b32 v54, a102\n"
    "v_accvgpr_read_b32 v55, a103\n"
    "v_mul_f32 v48, v48, v12\n"
    "v_mul_f32 v49, v49, v12\n"
    "v_mul_f32 v50, v50, v12\n"
    "v_mul_f32 v51, v51, v12\n"
    "v_mul_f32 v52, v52, v13\n"
    "v_mul_f32 v53, v53, v13\n"
    "v_mul_f32 v54, v54, v13\n"
    "v_mul_f32 v55, v55, v13\n"
    "v_accvgpr_write_b32 a96, v48\n"
    "v_accvgpr_write_b32 a97, v49\n"
    "v_accvgpr_write_b32 a98, v50\n"
    "v_accvgpr_write_b32 a99, v51\n"
    "v_accvgpr_write_b32 a100, v52\n"
    "v_accvgpr_write_b32 a101, v53\n"
    "v_accvgpr_write_b32 a102, v54\n"
    "v_accvgpr_write_b32 a103, v55\n"
    "v_accvgpr_read_b32 v48, a104\n"
    "v_accvgpr_read_b32 v49, a105\n"
    "v_accvgpr_read_b32 v50, a106\n"
    "v_accvgpr_read_b32 v51, a107\n"
    "v_accvgpr_read_b32 v52, a108\n"
    "v_accvgpr_read_b32 v53, a109\n"
    "v_accvgpr_read_b32 v54, a110\n"
    "v_accvgpr_read_b32 v55, a111\n"
    "v_mul_f32 v48, v48, v14\n"
    "v_mul_f32 v49, v49, v14\n"
    "v_mul_f32 v50, v50, v14\n"
    "v_mul_f32 v51, v51, v14\n"
    "v_mul_f32 v52, v52, v15\n"
    "v_mul_f32 v53, v53, v15\n"
    "v_mul_f32 v54, v54, v15\n"
    "v_mul_f32 v55, v55, v15\n"
    "v_accvgpr_write_b32 a104, v48\n"
    "v_accvgpr_write_b32 a105, v49\n"
    "v_accvgpr_write_b32 a106, v50\n"
    "v_accvgpr_write_b32 a107, v51\n"
    "v_accvgpr_write_b32 a108, v52\n"
    "v_accvgpr_write_b32 a109, v53\n"
    "v_accvgpr_write_b32 a110, v54\n"
    "v_accvgpr_write_b32 a111, v55\n"
    "v_accvgpr_read_b32 v48, a112\n"
    "v_accvgpr_read_b32 v49, a113\n"
    "v_accvgpr_read_b32 v50, a114\n"
    "v_accvgpr_read_b32 v51, a115\n"
    "v_accvgpr_read_b32 v52, a116\n"
    "v_accvgpr_read_b32 v53, a117\n"
    "v_accvgpr_read_b32 v54, a118\n"
    "v_accvgpr_read_b32 v55, a119\n"
    "v_mul_f32 v48, v48, v12\n"
    "v_mul_f32 v49, v49, v12\n"
    "v_mul_f32 v50, v50, v12\n"
    "v_mul_f32 v51, v51, v12\n"
    "v_mul_f32 v52, v52, v13\n"
    "v_mul_f32 v53, v53, v13\n"
    "v_mul_f32 v54, v54, v13\n"
    "v_mul_f32 v55, v55, v13\n"
    "v_accvgpr_write_b32 a112, v48\n"
    "v_accvgpr_write_b32 a113, v49\n"
    "v_accvgpr_write_b32 a114, v50\n"
    "v_accvgpr_write_b32 a115, v51\n"
    "v_accvgpr_write_b32 a116, v52\n"
    "v_accvgpr_write_b32 a117, v53\n"
    "v_accvgpr_write_b32 a118, v54\n"
    "v_accvgpr_write_b32 a119, v55\n"
    "v_accvgpr_read_b32 v48, a120\n"
    "v_accvgpr_read_b32 v49, a121\n"
    "v_accvgpr_read_b32 v50, a122\n"
    "v_accvgpr_read_b32 v51, a123\n"
    "v_accvgpr_read_b32 v52, a124\n"
    "v_accvgpr_read_b32 v53, a125\n"
    "v_accvgpr_read_b32 v54, a126\n"
    "v_accvgpr_read_b32 v55, a127\n"
    "v_mul_f32 v48, v48, v14\n"
    "v_mul_f32 v49, v49, v14\n"
    "v_mul_f32 v50, v50, v14\n"
    "v_mul_f32 v51, v51, v14\n"
    "v_mul_f32 v52, v52, v15\n"
    "v_mul_f32 v53, v53, v15\n"
    "v_mul_f32 v54, v54, v15\n"
    "v_mul_f32 v55, v55, v15\n"
    "v_accvgpr_write_b32 a120, v48\n"
    "v_accvgpr_write_b32 a121, v49\n"
    "v_accvgpr_write_b32 a122, v50\n"
    "v_accvgpr_write_b32 a123, v51\n"
    "v_accvgpr_write_b32 a124, v52\n"
    "v_accvgpr_write_b32 a125, v53\n"
    "v_accvgpr_write_b32 a126, v54\n"
    "v_accvgpr_write_b32 a127, v55\n"
    "v_accvgpr_read_b32 v48, a128\n"
    "v_accvgpr_read_b32 v49, a129\n"
    "v_accvgpr_read_b32 v50, a130\n"
    "v_accvgpr_read_b32 v51, a131\n"
    "v_accvgpr_read_b32 v52, a132\n"
    "v_accvgpr_read_b32 v53, a133\n"
    "v_accvgpr_read_b32 v54, a134\n"
    "v_accvgpr_read_b32 v55, a135\n"
    "v_mul_f32 v48, v48, v12\n"
    "v_mul_f32 v49, v49, v12\n"
    "v_mul_f32 v50, v50, v12\n"
    "v_mul_f32 v51, v51, v12\n"
    "v_mul_f32 v52, v52, v13\n"
    "v_mul_f32 v53, v53, v13\n"
    "v_mul_f32 v54, v54, v13\n"
    "v_mul_f32 v55, v55, v13\n"
    "v_accvgpr_write_b32 a128, v48\n"
    "v_accvgpr_write_b32 a129, v49\n"
    "v_accvgpr_write_b32 a130, v50\n"
    "v_accvgpr_write_b32 a131, v51\n"
    "v_accvgpr_write_b32 a132, v52\n"
    "v_accvgpr_write_b32 a133, v53\n"
    "v_accvgpr_write_b32 a134, v54\n"
    "v_accvgpr_write_b32 a135, v55\n"
    "v_accvgpr_read_b32 v48, a136\n"
    "v_accvgpr_read_b32 v49, a137\n"
    "v_accvgpr_read_b32 v50, a138\n"
    "v_accvgpr_read_b32 v51, a139\n"
    "v_accvgpr_read_b32 v52, a140\n"
    "v_accvgpr_read_b32 v53, a141\n"
    "v_accvgpr_read_b32 v54, a142\n"
    "v_accvgpr_read_b32 v55, a143\n"
    "v_mul_f32 v48, v48, v14\n"
    "v_mul_f32 v49, v49, v14\n"
    "v_mul_f32 v50, v50, v14\n"
    "v_mul_f32 v51, v51, v14\n"
    "v_mul_f32 v52, v52, v15\n"
    "v_mul_f32 v53, v53, v15\n"
    "v_mul_f32 v54, v54, v15\n"
    "v_mul_f32 v55, v55, v15\n"
    "v_accvgpr_write_b32 a136, v48\n"
    "v_accvgpr_write_b32 a137, v49\n"
    "v_accvgpr_write_b32 a138, v50\n"
    "v_accvgpr_write_b32 a139, v51\n"
    "v_accvgpr_write_b32 a140, v52\n"
    "v_accvgpr_write_b32 a141, v53\n"
    "v_accvgpr_write_b32 a142, v54\n"
    "v_accvgpr_write_b32 a143, v55\n"
    "v_accvgpr_read_b32 v48, a144\n"
    "v_accvgpr_read_b32 v49, a145\n"
    "v_accvgpr_read_b32 v50, a146\n"
    "v_accvgpr_read_b32 v51, a147\n"
    "v_accvgpr_read_b32 v52, a148\n"
    "v_accvgpr_read_b32 v53, a149\n"
    "v_accvgpr_read_b32 v54, a150\n"
    "v_accvgpr_read_b32 v55, a151\n"
    "v_mul_f32 v48, v48, v12\n"
    "v_mul_f32 v49, v49, v12\n"
    "v_mul_f32 v50, v50, v12\n"
    "v_mul_f32 v51, v51, v12\n"
    "v_mul_f32 v52, v52, v13\n"
    "v_mul_f32 v53, v53, v13\n"
    "v_mul_f32 v54, v54, v13\n"
    "v_mul_f32 v55, v55, v13\n"
    "v_accvgpr_write_b32 a144, v48\n"
    "v_accvgpr_write_b32 a145, v49\n"
    "v_accvgpr_write_b32 a146, v50\n"
    "v_accvgpr_write_b32 a147, v51\n"
    "v_accvgpr_write_b32 a148, v52\n"
    "v_accvgpr_write_b32 a149, v53\n"
    "v_accvgpr_write_b32 a150, v54\n"
    "v_accvgpr_write_b32 a151, v55\n"
    "v_accvgpr_read_b32 v48, a152\n"
    "v_accvgpr_read_b32 v49, a153\n"
    "v_accvgpr_read_b32 v50, a154\n"
    "v_accvgpr_read_b32 v51, a155\n"
    "v_accvgpr_read_b32 v52, a156\n"
    "v_accvgpr_read_b32 v53, a157\n"
    "v_accvgpr_read_b32 v54, a158\n"
    "v_accvgpr_read_b32 v55, a159\n"
    "v_mul_f32 v48, v48, v14\n"
    "v_mul_f32 v49, v49, v14\n"
    "v_mul_f32 v50, v50, v14\n"
    "v_mul_f32 v51, v51, v14\n"
    "v_mul_f32 v52, v52, v15\n"
    "v_mul_f32 v53, v53, v15\n"
    "v_mul_f32 v54, v54, v15\n"
    "v_mul_f32 v55, v55, v15\n"
    "v_accvgpr_write_b32 a152, v48\n"
    "v_accvgpr_write_b32 a153, v49\n"
    "v_accvgpr_write_b32 a154, v50\n"
    "v_accvgpr_write_b32 a155, v51\n"
    "v_accvgpr_write_b32 a156, v52\n"
    "v_accvgpr_write_b32 a157, v53\n"
    "v_accvgpr_write_b32 a158, v54\n"
    "v_accvgpr_write_b32 a159, v55\n"
    "v_accvgpr_read_b32 v48, a160\n"
    "v_accvgpr_read_b32 v49, a161\n"
    "v_accvgpr_read_b32 v50, a162\n"
    "v_accvgpr_read_b32 v51, a163\n"
    "v_accvgpr_read_b32 v52, a164\n"
    "v_accvgpr_read_b32 v53, a165\n"
    "v_accvgpr_read_b32 v54, a166\n"
    "v_accvgpr_read_b32 v55, a167\n"
    "v_mul_f32 v48, v48, v12\n"
    "v_mul_f32 v49, v49, v12\n"
    "v_mul_f32 v50, v50, v12\n"
    "v_mul_f32 v51, v51, v12\n"
    "v_mul_f32 v52, v52, v13\n"
    "v_mul_f32 v53, v53, v13\n"
    "v_mul_f32 v54, v54, v13\n"
    "v_mul_f32 v55, v55, v13\n"
    "v_accvgpr_write_b32 a160, v48\n"
    "v_accvgpr_write_b32 a161, v49\n"
    "v_accvgpr_write_b32 a162, v50\n"
    "v_accvgpr_write_b32 a163, v51\n"
    "v_accvgpr_write_b32 a164, v52\n"
    "v_accvgpr_write_b32 a165, v53\n"
    "v_accvgpr_write_b32 a166, v54\n"
    "v_accvgpr_write_b32 a167, v55\n"
    "v_accvgpr_read_b32 v48, a168\n"
    "v_accvgpr_read_b32 v49, a169\n"
    "v_accvgpr_read_b32 v50, a170\n"
    "v_accvgpr_read_b32 v51, a171\n"
    "v_accvgpr_read_b32 v52, a172\n"
    "v_accvgpr_read_b32 v53, a173\n"
    "v_accvgpr_read_b32 v54, a174\n"
    "v_accvgpr_read_b32 v55, a175\n"
    "v_mul_f32 v48, v48, v14\n"
    "v_mul_f32 v49, v49, v14\n"
    "v_mul_f32 v50, v50, v14\n"
    "v_mul_f32 v51, v51, v14\n"
    "v_mul_f32 v52, v52, v15\n"
    "v_mul_f32 v53, v53, v15\n"
    "v_mul_f32 v54, v54, v15\n"
    "v_mul_f32 v55, v55, v15\n"
    "v_accvgpr_write_b32 a168, v48\n"
    "v_accvgpr_write_b32 a169, v49\n"
    "v_accvgpr_write_b32 a170, v50\n"
    "v_accvgpr_write_b32 a171, v51\n"
    "v_accvgpr_write_b32 a172, v52\n"
    "v_accvgpr_write_b32 a173, v53\n"
    "v_accvgpr_write_b32 a174, v54\n"
    "v_accvgpr_write_b32 a175, v55\n"
    "v_accvgpr_read_b32 v48, a176\n"
    "v_accvgpr_read_b32 v49, a177\n"
    "v_accvgpr_read_b32 v50, a178\n"
    "v_accvgpr_read_b32 v51, a179\n"
    "v_accvgpr_read_b32 v52, a180\n"
    "v_accvgpr_read_b32 v53, a181\n"
    "v_accvgpr_read_b32 v54, a182\n"
    "v_accvgpr_read_b32 v55, a183\n"
    "v_mul_f32 v48, v48, v12\n"
    "v_mul_f32 v49, v49, v12\n"
    "v_mul_f32 v50, v50, v12\n"
    "v_mul_f32 v51, v51, v12\n"
    "v_mul_f32 v52, v52, v13\n"
    "v_mul_f32 v53, v53, v13\n"
    "v_mul_f32 v54, v54, v13\n"
    "v_mul_f32 v55, v55, v13\n"
    "v_accvgpr_write_b32 a176, v48\n"
    "v_accvgpr_write_b32 a177, v49\n"
    "v_accvgpr_write_b32 a178, v50\n"
    "v_accvgpr_write_b32 a179, v51\n"
    "v_accvgpr_write_b32 a180, v52\n"
    "v_accvgpr_write_b32 a181, v53\n"
    "v_accvgpr_write_b32 a182, v54\n"
    "v_accvgpr_write_b32 a183, v55\n"
    "v_accvgpr_read_b32 v48, a184\n"
    "v_accvgpr_read_b32 v49, a185\n"
    "v_accvgpr_read_b32 v50, a186\n"
    "v_accvgpr_read_b32 v51, a187\n"
    "v_accvgpr_read_b32 v52, a188\n"
    "v_accvgpr_read_b32 v53, a189\n"
    "v_accvgpr_read_b32 v54, a190\n"
    "v_accvgpr_read_b32 v55, a191\n"
    "v_mul_f32 v48, v48, v14\n"
    "v_mul_f32 v49, v49, v14\n"
    "v_mul_f32 v50, v50, v14\n"
    "v_mul_f32 v51, v51, v14\n"
    "v_mul_f32 v52, v52, v15\n"
    "v_mul_f32 v53, v53, v15\n"
    "v_mul_f32 v54, v54, v15\n"
    "v_mul_f32 v55, v55, v15\n"
    "v_accvgpr_write_b32 a184, v48\n"
    "v_accvgpr_write_b32 a185, v49\n"
    "v_accvgpr_write_b32 a186, v50\n"
    "v_accvgpr_write_b32 a187, v51\n"
    "v_accvgpr_write_b32 a188, v52\n"
    "v_accvgpr_write_b32 a189, v53\n"
    "v_accvgpr_write_b32 a190, v54\n"
    "v_accvgpr_write_b32 a191, v55\n"
    "v_accvgpr_read_b32 v48, a192\n"
    "v_accvgpr_read_b32 v49, a193\n"
    "v_accvgpr_read_b32 v50, a194\n"
    "v_accvgpr_read_b32 v51, a195\n"
    "v_accvgpr_read_b32 v52, a196\n"
    "v_accvgpr_read_b32 v53, a197\n"
    "v_accvgpr_read_b32 v54, a198\n"
    "v_accvgpr_read_b32 v55, a199\n"
    "v_mul_f32 v48, v48, v12\n"
    "v_mul_f32 v49, v49, v12\n"
    "v_mul_f32 v50, v50, v12\n"
    "v_mul_f32 v51, v51, v12\n"
    "v_mul_f32 v52, v52, v13\n"
    "v_mul_f32 v53, v53, v13\n"
    "v_mul_f32 v54, v54, v13\n"
    "v_mul_f32 v55, v55, v13\n"
    "v_accvgpr_write_b32 a192, v48\n"
    "v_accvgpr_write_b32 a193, v49\n"
    "v_accvgpr_write_b32 a194, v50\n"
    "v_accvgpr_write_b32 a195, v51\n"
    "v_accvgpr_write_b32 a196, v52\n"
    "v_accvgpr_write_b32 a197, v53\n"
    "v_accvgpr_write_b32 a198, v54\n"
    "v_accvgpr_write_b32 a199, v55\n"
    "v_accvgpr_read_b32 v48, a200\n"
    "v_accvgpr_read_b32 v49, a201\n"
    "v_accvgpr_read_b32 v50, a202\n"
    "v_accvgpr_read_b32 v51, a203\n"
    "v_accvgpr_read_b32 v52, a204\n"
    "v_accvgpr_read_b32 v53, a205\n"
    "v_accvgpr_read_b32 v54, a206\n"
    "v_accvgpr_read_b32 v55, a207\n"
    "v_mul_f32 v48, v48, v14\n"
    "v_mul_f32 v49, v49, v14\n"
    "v_mul_f32 v50, v50, v14\n"
    "v_mul_f32 v51, v51, v14\n"
    "v_mul_f32 v52, v52, v15\n"
    "v_mul_f32 v53, v53, v15\n"
    "v_mul_f32 v54, v54, v15\n"
    "v_mul_f32 v55, v55, v15\n"
    "v_accvgpr_write_b32 a200, v48\n"
    "v_accvgpr_write_b32 a201, v49\n"
    "v_accvgpr_write_b32 a202, v50\n"
    "v_accvgpr_write_b32 a203, v51\n"
    "v_accvgpr_write_b32 a204, v52\n"
    "v_accvgpr_write_b32 a205, v53\n"
    "v_accvgpr_write_b32 a206, v54\n"
    "v_accvgpr_write_b32 a207, v55\n"
    "v_accvgpr_read_b32 v48, a208\n"
    "v_accvgpr_read_b32 v49, a209\n"
    "v_accvgpr_read_b32 v50, a210\n"
    "v_accvgpr_read_b32 v51, a211\n"
    "v_accvgpr_read_b32 v52, a212\n"
    "v_accvgpr_read_b32 v53, a213\n"
    "v_accvgpr_read_b32 v54, a214\n"
    "v_accvgpr_read_b32 v55, a215\n"
    "v_mul_f32 v48, v48, v12\n"
    "v_mul_f32 v49, v49, v12\n"
    "v_mul_f32 v50, v50, v12\n"
    "v_mul_f32 v51, v51, v12\n"
    "v_mul_f32 v52, v52, v13\n"
    "v_mul_f32 v53, v53, v13\n"
    "v_mul_f32 v54, v54, v13\n"
    "v_mul_f32 v55, v55, v13\n"
    "v_accvgpr_write_b32 a208, v48\n"
    "v_accvgpr_write_b32 a209, v49\n"
    "v_accvgpr_write_b32 a210, v50\n"
    "v_accvgpr_write_b32 a211, v51\n"
    "v_accvgpr_write_b32 a212, v52\n"
    "v_accvgpr_write_b32 a213, v53\n"
    "v_accvgpr_write_b32 a214, v54\n"
    "v_accvgpr_write_b32 a215, v55\n"
    "v_accvgpr_read_b32 v48, a216\n"
    "v_accvgpr_read_b32 v49, a217\n"
    "v_accvgpr_read_b32 v50, a218\n"
    "v_accvgpr_read_b32 v51, a219\n"
    "v_accvgpr_read_b32 v52, a220\n"
    "v_accvgpr_read_b32 v53, a221\n"
    "v_accvgpr_read_b32 v54, a222\n"
    "v_accvgpr_read_b32 v55, a223\n"
    "v_mul_f32 v48, v48, v14\n"
    "v_mul_f32 v49, v49, v14\n"
    "v_mul_f32 v50, v50, v14\n"
    "v_mul_f32 v51, v51, v14\n"
    "v_mul_f32 v52, v52, v15\n"
    "v_mul_f32 v53, v53, v15\n"
    "v_mul_f32 v54, v54, v15\n"
    "v_mul_f32 v55, v55, v15\n"
    "v_accvgpr_write_b32 a216, v48\n"
    "v_accvgpr_write_b32 a217, v49\n"
    "v_accvgpr_write_b32 a218, v50\n"
    "v_accvgpr_write_b32 a219, v51\n"
    "v_accvgpr_write_b32 a220, v52\n"
    "v_accvgpr_write_b32 a221, v53\n"
    "v_accvgpr_write_b32 a222, v54\n"
    "v_accvgpr_write_b32 a223, v55\n"
    "v_accvgpr_read_b32 v48, a224\n"
    "v_accvgpr_read_b32 v49, a225\n"
    "v_accvgpr_read_b32 v50, a226\n"
    "v_accvgpr_read_b32 v51, a227\n"
    "v_accvgpr_read_b32 v52, a228\n"
    "v_accvgpr_read_b32 v53, a229\n"
    "v_accvgpr_read_b32 v54, a230\n"
    "v_accvgpr_read_b32 v55, a231\n"
    "v_mul_f32 v48, v48, v12\n"
    "v_mul_f32 v49, v49, v12\n"
    "v_mul_f32 v50, v50, v12\n"
    "v_mul_f32 v51, v51, v12\n"
    "v_mul_f32 v52, v52, v13\n"
    "v_mul_f32 v53, v53, v13\n"
    "v_mul_f32 v54, v54, v13\n"
    "v_mul_f32 v55, v55, v13\n"
    "v_accvgpr_write_b32 a224, v48\n"
    "v_accvgpr_write_b32 a225, v49\n"
    "v_accvgpr_write_b32 a226, v50\n"
    "v_accvgpr_write_b32 a227, v51\n"
    "v_accvgpr_write_b32 a228, v52\n"
    "v_accvgpr_write_b32 a229, v53\n"
    "v_accvgpr_write_b32 a230, v54\n"
    "v_accvgpr_write_b32 a231, v55\n"
    "v_accvgpr_read_b32 v48, a232\n"
    "v_accvgpr_read_b32 v49, a233\n"
    "v_accvgpr_read_b32 v50, a234\n"
    "v_accvgpr_read_b32 v51, a235\n"
    "v_accvgpr_read_b32 v52, a236\n"
    "v_accvgpr_read_b32 v53, a237\n"
    "v_accvgpr_read_b32 v54, a238\n"
    "v_accvgpr_read_b32 v55, a239\n"
    "v_mul_f32 v48, v48, v14\n"
    "v_mul_f32 v49, v49, v14\n"
    "v_mul_f32 v50, v50, v14\n"
    "v_mul_f32 v51, v51, v14\n"
    "v_mul_f32 v52, v52, v15\n"
    "v_mul_f32 v53, v53, v15\n"
    "v_mul_f32 v54, v54, v15\n"
    "v_mul_f32 v55, v55, v15\n"
    "v_accvgpr_write_b32 a232, v48\n"
    "v_accvgpr_write_b32 a233, v49\n"
    "v_accvgpr_write_b32 a234, v50\n"
    "v_accvgpr_write_b32 a235, v51\n"
    "v_accvgpr_write_b32 a236, v52\n"
    "v_accvgpr_write_b32 a237, v53\n"
    "v_accvgpr_write_b32 a238, v54\n"
    "v_accvgpr_write_b32 a239, v55\n"
    "v_accvgpr_read_b32 v48, a240\n"
    "v_accvgpr_read_b32 v49, a241\n"
    "v_accvgpr_read_b32 v50, a242\n"
    "v_accvgpr_read_b32 v51, a243\n"
    "v_accvgpr_read_b32 v52, a244\n"
    "v_accvgpr_read_b32 v53, a245\n"
    "v_accvgpr_read_b32 v54, a246\n"
    "v_accvgpr_read_b32 v55, a247\n"
    "v_mul_f32 v48, v48, v12\n"
    "v_mul_f32 v49, v49, v12\n"
    "v_mul_f32 v50, v50, v12\n"
    "v_mul_f32 v51, v51, v12\n"
    "v_mul_f32 v52, v52, v13\n"
    "v_mul_f32 v53, v53, v13\n"
    "v_mul_f32 v54, v54, v13\n"
    "v_mul_f32 v55, v55, v13\n"
    "v_accvgpr_write_b32 a240, v48\n"
    "v_accvgpr_write_b32 a241, v49\n"
    "v_accvgpr_write_b32 a242, v50\n"
    "v_accvgpr_write_b32 a243, v51\n"
    "v_accvgpr_write_b32 a244, v52\n"
    "v_accvgpr_write_b32 a245, v53\n"
    "v_accvgpr_write_b32 a246, v54\n"
    "v_accvgpr_write_b32 a247, v55\n"
    "v_accvgpr_read_b32 v48, a248\n"
    "v_accvgpr_read_b32 v49, a249\n"
    "v_accvgpr_read_b32 v50, a250\n"
    "v_accvgpr_read_b32 v51, a251\n"
    "v_accvgpr_read_b32 v52, a252\n"
    "v_accvgpr_read_b32 v53, a253\n"
    "v_accvgpr_read_b32 v54, a254\n"
    "v_accvgpr_read_b32 v55, a255\n"
    "v_mul_f32 v48, v48, v14\n"
    "v_mul_f32 v49, v49, v14\n"
    "v_mul_f32 v50, v50, v14\n"
    "v_mul_f32 v51, v51, v14\n"
    "v_mul_f32 v52, v52, v15\n"
    "v_mul_f32 v53, v53, v15\n"
    "v_mul_f32 v54, v54, v15\n"
    "v_mul_f32 v55, v55, v15\n"
    "v_accvgpr_write_b32 a248, v48\n"
    "v_accvgpr_write_b32 a249, v49\n"
    "v_accvgpr_write_b32 a250, v50\n"
    "v_accvgpr_write_b32 a251, v51\n"
    "v_accvgpr_write_b32 a252, v52\n"
    "v_accvgpr_write_b32 a253, v53\n"
    "v_accvgpr_write_b32 a254, v54\n"
    "v_accvgpr_write_b32 a255, v55\n"
    "v_mov_b32 v48, -1\n"
    "v_mov_b32 v49, -1\n"
    "v_mov_b32 v50, -1\n"
    "v_mov_b32 v51, -1\n"
    "s_mov_b32 s37, 0\n"
    "s_setpc_b64 s[42:43]\n"
    "L_end:\n"
    :: "s"(__builtin_amdgcn_kernarg_segment_ptr()), "s"(blockIdx.x), "s"(wave) : "v0","v1","v2","v3","v4","v5","v6","v7","v8","v9","v10","v11","v12","v13","v14","v15","v16","v17","v18","v19","v20","v21","v22","v23","v24","v25","v26","v27","v28","v29","v30","v31","v32","v33","v34","v35","v36","v37","v38","v39","v40","v41","v42","v43","v44","v45","v46","v47","v48","v49","v50","v51","v52","v53","v54","v55","v56","v57","v58","v59","v60","v61","v62","v63","v64","v65","v66","v67","v68","v69","v70","v71","v72","v73","v74","v75","v76","v77","v78","v79","v80","v81","v82","v83","v84","v85","v86","v87","v88","v89","v90","v91","v92","v93","v94","v95","v96","v97","v98","v99","v100","v101","v102","v103","v104","v105","v106","v107","v108","v109","v110","v111","v112","v113","v114","v115","v116","v117","v118","v119","v120","v121","v122","v123","v124","v125","v126","v127","v128","v129","v130","v131","v132","v133","v134","v135","v136","v137","v138","v139","v140","v141","v142","v143","v144","v145","v146","v147","v148","v149","v150","v151","v152","v153","v154","v155","v156","v157","v158","v159","v160","v161","v162","v163","v164","v165","v166","v167","v168","v169","v170","v171","v172","v173","v174","v175","v176","v177","v178","v179","v180","v181","v182","v183","v184","v185","v186","v187","v188","v189","v190","v191","v192","v193","v194","v195","v196","v197","v198","v199","v200","v201","v202","v203","v204","v205","v206","v207","v208","v209","v210","v211","v212","v213","v214","v215","v216","v217","v218","v219","v220","v221","v222","v223","v224","v225","v226","v227","v228","v229","v230","v231","v232","v233","v234","v235","v236","v237","v238","v239","v240","v241","v242","v243","v244","v245","v246","v247","v248","v249","v250","v251","v252","v253","v254","v255","a0","a1","a2","a3","a4","a5","a6","a7","a8","a9","a10","a11","a12","a13","a14","a15","a16","a17","a18","a19","a20","a21","a22","a23","a24","a25","a26","a27","a28","a29","a30","a31","a32","a33","a34","a35","a36","a37","a38","a39","a40","a41","a42","a43","a44","a45","a46","a47","a48","a49","a50","a51","a52","a53","a54","a55","a56","a57","a58","a59","a60","a61","a62","a63","a64","a65","a66","a67","a68","a69","a70","a71","a72","a73","a74","a75","a76","a77","a78","a79","a80","a81","a82","a83","a84","a85","a86","a87","a88","a89","a90","a91","a92","a93","a94","a95","a96","a97","a98","a99","a100","a101","a102","a103","a104","a105","a106","a107","a108","a109","a110","a111","a112","a113","a114","a115","a116","a117","a118","a119","a120","a121","a122","a123","a124","a125","a126","a127","a128","a129","a130","a131","a132","a133","a134","a135","a136","a137","a138","a139","a140","a141","a142","a143","a144","a145","a146","a147","a148","a149","a150","a151","a152","a153","a154","a155","a156","a157","a158","a159","a160","a161","a162","a163","a164","a165","a166","a167","a168","a169","a170","a171","a172","a173","a174","a175","a176","a177","a178","a179","a180","a181","a182","a183","a184","a185","a186","a187","a188","a189","a190","a191","a192","a193","a194","a195","a196","a197","a198","a199","a200","a201","a202","a203","a204","a205","a206","a207","a208","a209","a210","a211","a212","a213","a214","a215","a216","a217","a218","a219","a220","a221","a222","a223","a224","a225","a226","a227","a228","a229","a230","a231","a232","a233","a234","a235","a236","a237","a238","a239","a240","a241","a242","a243","a244","a245","a246","a247","a248","a249","a250","a251","a252","a253","a254","a255","s8","s9","s10","s11","s12","s13","s14","s15","s16","s17","s18","s19","s20","s21","s22","s23","s24","s25","s26","s27","s28","s29","s30","s31","s32","s33","s34","s35","s36","s37","s38","s39","s40","s41","s42","s43","s44","s45","s46","s47","s48","s49","s50","s51","s52","s53","s54","s55","s56","s57","s58","s59","s60","s61","s62","s63","s64","s65","s66","s67","s68","s69","s70","s71","s72","s73","s74","s75","s76","s77","s78","s79","s80","s81","s82","s83","s84","s85","s86","s87","s88","s89","s90","s91","s92","s93","s94","s95","s96","s97","s98","s99","s100","s101","vcc","m0","memory","scc");
}

extern "C" void kernel_launch(void* const* d_in, const int* in_sizes, int n_in, void* d_out, int out_size, void* d_ws, size_t ws_size, hipStream_t stream) {
  k_main<<<256,256,0,stream>>>(d_in[0],d_in[1],d_in[2],d_in[3],d_in[4],d_in[6],d_in[7],d_in[8],d_in[9],d_out,d_ws);

}
```
